```python
import torch

def _softmax(x, axis=-1):
    return torch.softmax(x, dim=axis)
def _gelu(x):
    return torch.nn.functional.gelu(x, approximate="tanh")
def _silu(x):
    return torch.nn.functional.silu(x)
def _sigmoid(x):
    return torch.sigmoid(x)
def _layernorm(x, axis=-1, eps=1e-5):
    m = x.mean(dim=axis, keepdim=True)
    v = x.var(dim=axis, keepdim=True, unbiased=False)
    return (x - m) / torch.sqrt(v + eps)
def _scatter_add(tgt, idx, src, axis=0):
    return tgt.clone().index_add_(axis, idx, src)
def _segment_sum(src, seg, n):
    out = torch.zeros((n,) + src.shape[1:], dtype=src.dtype)
    return out.index_add_(0, seg, src)
def _pad(x, pw):
    flat = tuple(v for pair in reversed(pw) for v in pair)
    return torch.nn.functional.pad(x, flat)

kernel_name = 'concat_heads__grouped_kv_mqa__ssm_conv_s4__prompt'

def setup_inputs():

    _w = (lambda a: (lambda t: t.to(torch.bfloat16) if t.is_floating_point() else t)(torch.as_tensor(a)))
    def _A_setup():
        B, S, D, H = 1, 2048, 576, 32
        Dh = D // H
        return {
            "x": _w((lambda *s: torch.randn(*s, dtype=torch.bfloat16) * 0.05)(B, S, D)),
            "wq": _w((lambda *s: torch.randn(*s, dtype=torch.bfloat16) * 0.8)(D, H * Dh)),
            "wk": _w((lambda *s: torch.randn(*s, dtype=torch.bfloat16) * 0.8)(D, Dh)),
            "wv": _w((lambda *s: torch.randn(*s, dtype=torch.bfloat16) * 0.05)(D, Dh)),
            "wo": _w((lambda *s: torch.randn(*s, dtype=torch.bfloat16) * 0.05)(H * Dh, D)),
        }
    _A_in = _A_setup()

    def _B_setup():
        B, L, D, N = 1, 2048, 576, 128
        A_re = -(lambda _x: torch.exp(torch.as_tensor(_x)))((lambda *s: torch.randn(*s, dtype=torch.bfloat16) * 6.4)(D, N))
        A_im = torch.pi * (lambda *s: torch.randn(*s, dtype=torch.bfloat16) * 0.05)(D, N)
        B_re = (lambda *s: torch.randn(*s, dtype=torch.bfloat16) * 0.05)(D, N)
        B_im = (lambda *s: torch.randn(*s, dtype=torch.bfloat16) * 0.05)(D, N)
        C_re = (lambda *s: torch.randn(*s, dtype=torch.bfloat16) * 0.05)(D, N)
        C_im = (lambda *s: torch.randn(*s, dtype=torch.bfloat16) * 0.05)(D, N)
        log_dt = (lambda *s: torch.randn(*s, dtype=torch.bfloat16) * 0.05)(D) * 0.5 - 4.0
        Dskip = (lambda *s: torch.randn(*s, dtype=torch.bfloat16) * 0.05)(D)
        return {
            "u": _w((lambda *s: torch.randn(*s, dtype=torch.bfloat16) * 0.05)(B, L, D)),
            "A_re": _w(A_re),
            "A_im": _w(A_im),
            "B_re": _w(B_re),
            "B_im": _w(B_im),
            "C_re": _w(C_re),
            "C_im": _w(C_im),
            "log_dt": _w(log_dt),
            "Dskip": _w(Dskip),
        }
    _B_in = _B_setup()

    return {
        "A_x": _A_in["x"],
        "A_wq": _A_in["wq"],
        "A_wk": _A_in["wk"],
        "A_wv": _A_in["wv"],
        "A_wo": _A_in["wo"],
        "B_u": _B_in["u"],
        "B_A_re": _B_in["A_re"],
        "B_A_im": _B_in["A_im"],
        "B_B_re": _B_in["B_re"],
        "B_B_im": _B_in["B_im"],
        "B_C_re": _B_in["C_re"],
        "B_C_im": _B_in["C_im"],
        "B_log_dt": _B_in["log_dt"],
        "B_Dskip": _B_in["Dskip"],
    }

def reference(A_x, A_wq, A_wk, A_wv, A_wo, B_u, B_A_re, B_A_im, B_B_re, B_B_im, B_C_re, B_C_im, B_log_dt, B_Dskip):
    def _A_ref(x, wq, wk, wv, wo):
        B, S, D = x.shape
        H = 32
        Dh = D // H
        q = (lambda x, *d: x.permute(*d))((x @ wq).reshape(B, S, H, Dh), 0, 2, 1, 3)
        k = x @ wk
        v = x @ wv
        scores = torch.einsum("bhqd,bkd->bhqk", q, k) * (Dh ** -0.5)
        p = _softmax(scores, axis=-1)
        o = torch.einsum("bhqk,bkd->bhqd", p, v)
        return (lambda x, *d: x.permute(*d))(o, 0, 2, 1, 3).reshape(B, S, H * Dh) @ wo

    def _B_ref(u, A_re, A_im, B_re, B_im, C_re, C_im, log_dt, Dskip):
        Bsz, L, D = u.shape
        dt = torch.exp(log_dt)[:, None]
        A = A_re + 1j * A_im
        Bc = B_re + 1j * B_im
        Cc = C_re + 1j * C_im
        logdA = dt * A
        dB = dt * Bc
        l = (lambda x, dt: x.to(dt))(torch.arange(L), A_re.dtype)
        P = torch.exp(logdA[:, :, None] * l[None, None, :])
        K = torch.einsum("dn,dnl,dn->dl", Cc, P, dB).real
        L2 = 2 * L
        Kp = _pad(K, ((0, 0), (0, L)))
        up = (lambda x, *d: x.permute(*d))(u, 0, 2, 1)
        up = _pad(up, ((0, 0), (0, 0), (0, L)))
        Kf = (lambda x, axes=(-2,-1): torch.fft.rfft2(x.float(), dim=axes))(Kp[:, None, :], axes=(-2, -1))
        Uf = (lambda x, axes=(-2,-1): torch.fft.rfft2(x.float(), dim=axes))(up[:, :, None, :], axes=(-2, -1))
        Yf = Uf * Kf[None, :, :, :]
        y = (lambda x, s, axes=(-2,-1): torch.fft.irfft2(x, s=s, dim=axes).to(torch.bfloat16))(Yf, (1, L2), axes=(-2, -1))[:, :, 0, :L]
        y = (lambda x, *d: x.permute(*d))(y, 0, 2, 1) + u * Dskip[None, None, :]
        return y

    a = _A_ref(A_x, A_wq, A_wk, A_wv, A_wo)
    b = _B_ref(B_u, B_A_re, B_A_im, B_B_re, B_B_im, B_C_re, B_C_im, B_log_dt, B_Dskip)
    return torch.concatenate([a, b], axis=-1)
```

```cpp
#include <hip/hip_runtime.h>
#include <hip/hip_bf16.h>
#include <stdint.h>

#define MK_SPLIT 0
#define MK_STAMPS 0

namespace {

typedef __attribute__((ext_vector_type(16))) float f32x16;
typedef __attribute__((ext_vector_type(4))) float f32x4;
typedef __attribute__((ext_vector_type(2))) float f32x2;
typedef __attribute__((ext_vector_type(8))) short bf16x8;
typedef __attribute__((ext_vector_type(4))) short bf16x4;
typedef __attribute__((ext_vector_type(2))) __bf16 bf16x2_t;
typedef __attribute__((ext_vector_type(4))) unsigned u32x4;
typedef __attribute__((ext_vector_type(2))) unsigned u32x2;
typedef short v4i16_t __attribute__((ext_vector_type(4)));
#define LAS __attribute__((address_space(3)))
#define GAS __attribute__((address_space(1)))
typedef LAS char* lds_p;
typedef const unsigned short* cbf;

constexpr int SEQ = 2048, DM = 576;
constexpr float QSCALE = 0.34005040627f;
constexpr float LOG2E = 1.4426950408889634f;
constexpr float INV2PI = 0.15915494309189535f;

constexpr int WS_EPOCH = 0;
constexpr int WS_FLAG1 = 4096;
constexpr int WS_FLAG2 = WS_FLAG1 + 16384;
constexpr int WS_KV = 32768;
constexpr int KVT = 2560;
constexpr int WS_O = WS_KV + 64 * KVT;
constexpr int WS_BYTES = WS_O + SEQ * DM * 2;
constexpr int WS_STAMP = 4 * 1024 * 1024;

constexpr int L_W = 0;
constexpr int L_X1 = 40960;
constexpr int L_S = 55296;
constexpr int L_Q = 147456;
constexpr int L_C = 159744;
constexpr int L_END = 160000;
constexpr int SCH = 21840;
constexpr int SCH_U = 16640;
enum { C_W = 0, C_P, C_Q, C_F1, C_RW0, C_RW1, C_RR0, C_RR1, C_O, C_BAD, C_F2, C_WO, C_S1, C_S2, C_S3, C_X0, C_X1, C_X2, C_X3, C_Y0, C_Y1, C_Y2, C_Y3, C_QR, C_NUM };

constexpr int RSRC_W3 = 0x00020000;
constexpr int AUX_SC1 = 16;
constexpr unsigned SPIN_LDS = 1u << 20, SPIN_GLB = 1u << 16;

#define SBAR() __builtin_amdgcn_sched_barrier(0)
#define PIN(x) asm volatile("" : "+v"(x))
#define CBAR() asm volatile("" ::: "memory")
#define DRAIN() asm volatile("s_waitcnt vmcnt(0)" ::: "memory")
#define MF32(a, b, c) __builtin_amdgcn_mfma_f32_32x32x16_bf16(a, b, c, 0, 0, 0)
#define MF32E(a, b, c) __builtin_amdgcn_mfma_f32_32x32x8bf16_1k(a, b, c, 0, 0, 0)
#define MF16(a, b, c) __builtin_amdgcn_mfma_f32_16x16x32_bf16(a, b, c, 0, 0, 0)
#define EXP2(x) __builtin_amdgcn_exp2f(x)
#define BC8(x) __builtin_bit_cast(bf16x8, x)
#define DI __device__ __forceinline__

DI unsigned cvtpk(float lo, float hi) { f32x2 v = {lo, hi}; return __builtin_bit_cast(unsigned, __builtin_convertvector(v, bf16x2_t)); }
DI float bflo(unsigned x) { return __builtin_bit_cast(float, x << 16); }
DI float bfhi(unsigned x) { return __builtin_bit_cast(float, x & 0xffff0000u); }
DI float bf1(unsigned short x) { return __builtin_bit_cast(float, (unsigned)x << 16); }
template <class T> DI T ldsr(lds_p p) { return *(const LAS T*)p; }
template <class T> DI void ldsw(lds_p p, T v) { *(LAS T*)p = v; }
DI bf16x4 ldtr(lds_p p) { return __builtin_bit_cast(bf16x4, __builtin_amdgcn_ds_read_tr16_b64_v4i16((LAS v4i16_t*)p)); }
DI bf16x8 cat8(bf16x4 a, bf16x4 b) { return __builtin_shufflevector(a, b, 0, 1, 2, 3, 4, 5, 6, 7); }
DI int swap23(int k) { return (k & ~12) | ((k & 4) << 1) | ((k & 8) >> 1); }
DI void dma16(__amdgpu_buffer_rsrc_t rs, int voff, unsigned lds_base) {
    unsigned keep;
    asm volatile("s_mov_b32 %0, m0\n\ts_mov_b32 m0, %3\n\ts_nop 0\n\tbuffer_load_dwordx4 %1, %2, 0 offen lds\n\ts_mov_b32 m0, %0"
                 : "=&s"(keep) : "v"(voff), "s"(rs), "s"(lds_base) : "memory");
}
DI void arrive(lds_p L, int c, int lane) {
    CBAR();
    if (lane == 0) (void)__hip_atomic_fetch_add((LAS unsigned*)(L + L_C) + c, 1u, __ATOMIC_RELAXED, __HIP_MEMORY_SCOPE_WORKGROUP);
    CBAR();
}
DI void waitc(lds_p L, int c, unsigned target) {
    CBAR();
    volatile LAS unsigned* p = (volatile LAS unsigned*)(L + L_C) + c;
    for (unsigned spins = 0; spins < SPIN_LDS; ++spins) {
        if ((unsigned)__builtin_amdgcn_readfirstlane(*p) >= target) break;
        __builtin_amdgcn_s_sleep(1);
    }
    CBAR();
}
DI unsigned gload_sc1(const unsigned* p) { return __hip_atomic_load((GAS unsigned*)p, __ATOMIC_RELAXED, __HIP_MEMORY_SCOPE_AGENT); }
DI void gstore_sc1(unsigned* p, unsigned v) { __hip_atomic_store((GAS unsigned*)p, v, __ATOMIC_RELAXED, __HIP_MEMORY_SCOPE_AGENT); }

struct cplx2 { f32x2 re, im; };
DI cplx2 cmul(const cplx2& a, const cplx2& b) { cplx2 r; r.re = a.re * b.re - a.im * b.im; r.im = a.re * b.im + a.im * b.re; return r; }
DI cplx2 cexp(f32x2 ar2, f32x2 airev, float e) {
    f32x2 m = ar2 * e, t = airev * e; cplx2 r;
#pragma unroll
    for (int i = 0; i < 2; ++i) {
        const float mag = EXP2(m[i]), fr = __builtin_amdgcn_fractf(t[i]);
        r.re[i] = mag * __builtin_amdgcn_cosf(fr); r.im[i] = mag * __builtin_amdgcn_sinf(fr);
    }
    return r;
}

struct Params {
    const unsigned short *x, *wq, *wk, *wv, *wo, *u, *are, *aim, *bre, *bim, *cre, *cim, *ldt, *dsk;
    unsigned short* out;
    char* ws;
};

#define STAMP(i) do {} while (0)

DI void ssm_wave(const Params& p, lds_p L, int s, int lane, int ch0) {
    const int d = ch0 + s;
    lds_p R = L + L_S + s * SCH;
    const int t0 = 512 * s + 8 * lane;
    u32x2 uu[8];
#pragma unroll
    for (int e = 0; e < 8; ++e) uu[e] = *(const u32x2*)(p.u + (size_t)(t0 + e) * DM + ch0);
    const int pb = d * 128 + lane;
    const f32x2 are = {bf1(p.are[pb]), bf1(p.are[pb + 64])}, aim = {bf1(p.aim[pb]), bf1(p.aim[pb + 64])};
    const f32x2 bre = {bf1(p.bre[pb]), bf1(p.bre[pb + 64])}, bim = {bf1(p.bim[pb]), bf1(p.bim[pb + 64])};
    const f32x2 cre = {bf1(p.cre[pb]), bf1(p.cre[pb + 64])}, cim = {bf1(p.cim[pb]), bf1(p.cim[pb + 64])};
    const float ldt = bf1(p.ldt[d]);
    const u32x2 dsk = *(const u32x2*)(p.dsk + ch0);
    const float dt = bflo(cvtpk(expf(ldt), 0.f));
    const f32x2 ar2 = (are * dt) * LOG2E, airev = (aim * dt) * INV2PI;
    cplx2 dB; dB.re = bre * dt; dB.im = bim * dt;
    cplx2 C; C.re = cre; C.im = cim;
    const cplx2 CB = cmul(C, dB);
    const cplx2 w1 = cexp(ar2, airev, 1.f), w8 = cexp(ar2, airev, 8.f), w128 = cexp(ar2, airev, 128.f);

    lds_p st0 = R + lane * 16, st1 = R + 4224 + lane * 16;
    const int kq = lane >> 4, q4 = (lane >> 2) & 3, p4 = lane & 3;
    lds_p trb = R + (p4 >> 1) * 4224 + (16 * (kq >> 1) + 4 * (kq & 1) + q4) * 16 + (p4 & 1) * 8;
#define PUT16(V, NEG) do { \
        u32x4 a0, a1, b0, b1, c0, c1, d0, d1; \
        _Pragma("unroll") for (int j = 0; j < 4; ++j) { \
            a0[j] = cvtpk(V[2 * j].re[0], V[2 * j + 1].re[0]); a1[j] = cvtpk(V[8 + 2 * j].re[0], V[9 + 2 * j].re[0]); \
            b0[j] = cvtpk(V[2 * j].re[1], V[2 * j + 1].re[1]); b1[j] = cvtpk(V[8 + 2 * j].re[1], V[9 + 2 * j].re[1]); \
            c0[j] = cvtpk(NEG V[2 * j].im[0], NEG V[2 * j + 1].im[0]); c1[j] = cvtpk(NEG V[8 + 2 * j].im[0], NEG V[9 + 2 * j].im[0]); \
            d0[j] = cvtpk(NEG V[2 * j].im[1], NEG V[2 * j + 1].im[1]); d1[j] = cvtpk(NEG V[8 + 2 * j].im[1], NEG V[9 + 2 * j].im[1]); } \
        ldsw(st0, a0); ldsw(st1, a1); ldsw(st0 + 1024, b0); ldsw(st1 + 1024, b1); \
        ldsw(st0 + 2048, c0); ldsw(st1 + 2048, c1); ldsw(st0 + 3072, d0); ldsw(st1 + 3072, d1); } while (0)
#define GETF(ks) cat8(ldtr(trb + (ks) * 512), ldtr(trb + (ks) * 512 + 128))
    bf16x8 yf[8];
    {
        cplx2 V[16];
        V[0].re = (f32x2){1.f, 1.f}; V[0].im = (f32x2){0.f, 0.f}; V[1] = w8;
        V[2] = cmul(w8, w8); V[3] = cmul(V[2], w8); V[4] = cmul(V[2], V[2]);
        V[5] = cmul(V[4], V[1]); V[6] = cmul(V[4], V[2]); V[7] = cmul(V[4], V[3]); V[8] = cmul(V[4], V[4]);
#pragma unroll
        for (int j = 9; j < 16; ++j) V[j] = cmul(V[8], V[j - 8]);
        PUT16(V, -);
#pragma unroll
        for (int ks = 0; ks < 8; ++ks) yf[ks] = GETF(ks);
    }
    const cplx2 w2 = cmul(w1, w1), w3 = cmul(w2, w1), w4 = cmul(w2, w2), w5 = cmul(w4, w1), w6 = cmul(w4, w2), w7 = cmul(w4, w3);
    f32x4 D0 = {}, D1 = {}, D2 = {};
    cplx2 G = cmul(CB, w128);
    {
        cplx2 V[16];
        V[0] = CB; V[1] = cmul(CB, w1); V[2] = cmul(CB, w2); V[3] = cmul(CB, w3); V[4] = cmul(CB, w4); V[5] = cmul(CB, w5); V[6] = cmul(CB, w6); V[7] = cmul(CB, w7);
        V[8] = G; V[9] = cmul(G, w1); V[10] = cmul(G, w2); V[11] = cmul(G, w3); V[12] = cmul(G, w4); V[13] = cmul(G, w5); V[14] = cmul(G, w6); V[15] = cmul(G, w7);
        PUT16(V, +);
#pragma unroll
        for (int ks = 0; ks < 8; ++ks) D0 = MF16(GETF(ks), yf[ks], D0);
    }
    {
        cplx2 V[16];
        G = cmul(G, w128); V[0] = G; V[1] = cmul(G, w2); V[2] = cmul(G, w4); V[3] = cmul(G, w6);
        G = cmul(G, w128); V[4] = G; V[5] = cmul(G, w2); V[6] = cmul(G, w4); V[7] = cmul(G, w6);
#pragma unroll
        for (int i = 0; i < 4; ++i) { G = cmul(G, w128); V[8 + 2 * i] = G; V[9 + 2 * i] = cmul(G, w4); }
        PUT16(V, +);
#pragma unroll
        for (int ks = 0; ks < 8; ++ks) D1 = MF16(GETF(ks), yf[ks], D1);
    }
    {
        cplx2 V[16];
#pragma unroll
        for (int i = 0; i < 9; ++i) { G = cmul(G, w128); V[i] = G; }
#pragma unroll
        for (int i = 9; i < 16; ++i) V[i] = G;
        PUT16(V, +);
#pragma unroll
        for (int ks = 0; ks < 8; ++ks) D2 = MF16(GETF(ks), yf[ks], D2);
    }
#undef PUT16
#undef GETF
    lds_p tab = R + 8448;
    {
        const int j = lane & 15, rq = lane >> 4;
#pragma unroll
        for (int i = 0; i < 4; ++i) {
            const int row = 4 * rq + i;
            const int i0 = (row < 8 ? 8 * j + row : 128 + 8 * j + row - 8);
            const int i1 = (row < 4 ? 256 + 4 * j + row : row < 8 ? 320 + 4 * j + row - 4 : 384 + 32 * ((row - 8) >> 1) + 2 * j + ((row - 8) & 1));
            const int i2 = (row < 8 ? 512 + 16 * row + j : 640);
            ldsw(tab + 2 * i0, (unsigned short)cvtpk(D0[i], 0.f));
            ldsw(tab + 2 * i1, (unsigned short)cvtpk(D1[i], 0.f));
            if (row < 8 || (row == 8 && j == 0)) ldsw(tab + 2 * i2, (unsigned short)cvtpk(D2[i], 0.f));
        }
    }
    {
        const int uo = SCH_U + 80 * ((t0 >> 5) + 1) + 2 * (t0 & 31);
#pragma unroll
        for (int c = 0; c < 4; ++c) {
            u32x4 v;
#pragma unroll
            for (int e = 0; e < 4; ++e) {
                const unsigned a = uu[2 * e][c >> 1], b = uu[2 * e + 1][c >> 1];
                v[e] = (c & 1) ? ((a >> 16) | (b & 0xffff0000u)) : ((a & 0xffffu) | (b << 16));
            }
            ldsw(L + L_S + c * SCH + uo, v);
        }
        if (lane < 5) ldsw(R + SCH_U + 16 * lane, (u32x4){0u, 0u, 0u, 0u});
    }
#pragma unroll
    for (int i = 0; i < 5; ++i) {
        const int g = lane + 64 * i;
        if (i < 4) {
            u32x4 v;
#pragma unroll
            for (int e = 0; e < 4; ++e) {
                const int l0 = 2047 - 8 * g - 2 * e, l1 = l0 - 1;
                const unsigned b = cvtpk((float)l0, (float)l1);
                int i0 = (int)(b & 0xffffu) - ((134 << 7) - 128), i1 = (int)(b >> 16) - ((134 << 7) - 128);
                if (i == 3) { i0 = l0 < 128 ? l0 : i0; i1 = l1 < 128 ? l1 : i1; }
                v[e] = (unsigned)ldsr<unsigned short>(tab + 2 * i0) | ((unsigned)ldsr<unsigned short>(tab + 2 * i1) << 16);
            }
            PIN(v);
            ldsw(R + 16 * g, v);
        } else if (lane < 4) ldsw(R + 16 * g, (u32x4){0u, 0u, 0u, 0u});
    }
#pragma unroll
    for (int i = 0; i < 5; ++i) {
        const int g = lane + 64 * i;
        if (i < 4 || lane < 4) {
            const u32x4 a = ldsr<u32x4>(R + 16 * g); const u32x2 b = ldsr<u32x2>(R + 16 * g + 16);
            const unsigned a0 = __builtin_amdgcn_alignbit(a[1], a[0], 16), a1 = __builtin_amdgcn_alignbit(a[2], a[1], 16), a2 = __builtin_amdgcn_alignbit(a[3], a[2], 16),
                           a3 = __builtin_amdgcn_alignbit(b[0], a[3], 16), a4 = __builtin_amdgcn_alignbit(b[1], b[0], 16);
            ldsw(R + 4160 + 16 * g, (u32x4){a0, a1, a2, a3});
            ldsw(R + 8320 + 16 * g, (u32x4){a[1], a[2], a[3], b[0]});
            ldsw(R + 12480 + 16 * g, (u32x4){a1, a2, a3, a4});
        }
    }
    arrive(L, C_S1, lane); waitc(L, C_S1, 4);
    f32x16 acc0 = {}, acc1 = {};
    {
        const int b = lane & 31, h = lane >> 5, c = (3 - b) & 3;
        int pa = c * 4160 + 2 * (2047 - b + 8 * h - c) - 2048;
        int pu = SCH_U + 80 * (b + 1) + 16 * h;
        const int pz = SCH_U + 16 * h;
#pragma unroll 2
        for (int dl = 0; dl < 32; ++dl) {
            lds_p A = R + pa; const int pu0 = pu > pz ? pu : pz; lds_p B0 = R + pu0, B1 = R + pu + 2560;
#pragma unroll
            for (int s2 = 0; s2 < 2; ++s2) {
                const bf16x4 a1l = *(const volatile LAS bf16x4*)(A + 2048 + 32 * s2), a1h = *(const volatile LAS bf16x4*)(A + 2048 + 32 * s2 + 8);
                const bf16x4 a2l = *(const volatile LAS bf16x4*)(A + 32 * s2), a2h = *(const volatile LAS bf16x4*)(A + 32 * s2 + 8);
                const bf16x8 b0 = ldsr<bf16x8>(B0 + 32 * s2), b1 = ldsr<bf16x8>(B1 + 32 * s2);
                const bf16x8 A1 = cat8(a1l, a1h), A2 = cat8(a2l, a2h);
                acc0 = MF32(A1, b0, acc0); acc1 = MF32(A1, b1, acc1); acc1 = MF32(A2, b0, acc1);
            }
            pa -= 64; pu -= 80;
        }
    }
    {
        const int a = lane & 31, h = lane >> 5;
#pragma unroll
        for (int g = 0; g < 4; ++g) {
            ldsw(R + 2 * (32 * a + 8 * g + 4 * h), (u32x2){cvtpk(acc0[4 * g], acc0[4 * g + 1]), cvtpk(acc0[4 * g + 2], acc0[4 * g + 3])});
            ldsw(R + 2048 + 2 * (32 * a + 8 * g + 4 * h), (u32x2){cvtpk(acc1[4 * g], acc1[4 * g + 1]), cvtpk(acc1[4 * g + 2], acc1[4 * g + 3])});
        }
    }
    arrive(L, C_S2, lane); waitc(L, C_S2, 4);
    {
        u32x4 yv[4];
#pragma unroll
        for (int c = 0; c < 4; ++c) yv[c] = ldsr<u32x4>(L + L_S + c * SCH + 2 * t0);
        const float dk[4] = {bflo(dsk[0]), bfhi(dsk[0]), bflo(dsk[1]), bfhi(dsk[1])};
#pragma unroll
        for (int e = 0; e < 8; ++e) {
            float r[4];
#pragma unroll
            for (int c = 0; c < 4; ++c) {
                const float yy = (e & 1) ? bfhi(yv[c][e >> 1]) : bflo(yv[c][e >> 1]);
                const float uv = (c & 1) ? bfhi(uu[e][c >> 1]) : bflo(uu[e][c >> 1]);
                r[c] = yy + bflo(cvtpk(uv * dk[c], 0.f));
            }
            *(u32x2*)(p.out + (size_t)(t0 + e) * (2 * DM) + DM + ch0) = (u32x2){cvtpk(r[0], r[1]), cvtpk(r[2], r[3])};
        }
    }
    arrive(L, C_S3, lane); waitc(L, C_S3, 4);
}

template <int MODE>
__global__ __launch_bounds__(768) void fused_kernel(Params p) {
    extern __shared__ __attribute__((aligned(16))) char smem[];
    lds_p L = (lds_p)smem;
    const unsigned lds0 = (unsigned)(uintptr_t)smem;
    const int tid = threadIdx.x, lane = tid & 63, w = __builtin_amdgcn_readfirstlane(tid >> 6);
    const int bid = blockIdx.x, xcd = bid & 7, idx = bid >> 3, tb = 2 * xcd + (idx & 1), pi = idx >> 1;
    if (tid < 64) ((LAS unsigned*)(L + L_C))[tid] = 0u;
    __syncthreads();
    STAMP(0);
    const __amdgpu_buffer_rsrc_t rs_ws = __builtin_amdgcn_make_buffer_rsrc((void*)p.ws, 0, WS_BYTES, RSRC_W3);

    if (w >= 8) {
        const int s = w - 8;
        if ((MODE == 0 || MODE == 1) && idx < 18) ssm_wave(p, L, s, lane, 72 * xcd + 4 * idx);
        STAMP(1);
        if (MODE == 0 || MODE == 3) {
            const __amdgpu_buffer_rsrc_t rs_wo = __builtin_amdgcn_make_buffer_rsrc((void*)p.wo, 0, DM * DM * 2, RSRC_W3);
            for (int inst = s; inst < 90; inst += 4) {
                const int sg = 64 * inst + lane, kp = sg / 10, g = sg - 10 * kp;
                if (g < 9) dma16(rs_wo, swap23(kp) * 1152 + 144 * (pi >> 1) + 16 * g, lds0 + L_S + 1024 * inst);
            }
            DRAIN();
            arrive(L, C_WO, lane);
        }
        STAMP(2);
        return;
    }

    unsigned tag = 0;
    if (MODE == 0) tag = (unsigned)__builtin_amdgcn_readfirstlane(gload_sc1((const unsigned*)(p.ws + WS_EPOCH))) + 1u;
    const int c16 = lane & 15, kq = lane >> 4, r32 = lane & 31, h = lane >> 5;
    const int q4 = (lane >> 2) & 3, p4 = lane & 3;
    const int kh = w >> 2, wq4 = w & 3;

    bf16x8 qmA, qmB; unsigned qeA, qeB;
    if (MODE != 3) {
        const int T0 = 128 * tb + 16 * w;
        const bool has_kv = (MODE != 2) && pi < 5;
        u32x2 kp0 = {}, vp0 = {}, kp1 = {}, vp1 = {};
        if (has_kv) {
            const __amdgpu_buffer_rsrc_t rs_wk = __builtin_amdgcn_make_buffer_rsrc((void*)p.wk, 0, DM * 18 * 2, RSRC_W3);
            const __amdgpu_buffer_rsrc_t rs_wv = __builtin_amdgcn_make_buffer_rsrc((void*)p.wv, 0, DM * 18 * 2, RSRC_W3);
            kp0 = __builtin_amdgcn_raw_buffer_load_b64(rs_wk, tid * 36 + 8 * pi, 0, 0);
            vp0 = __builtin_amdgcn_raw_buffer_load_b64(rs_wv, tid * 36 + 8 * pi, 0, 0);
            kp1 = __builtin_amdgcn_raw_buffer_load_b64(rs_wk, (512 + tid) * 36 + 8 * pi, 0, 0);
            vp1 = __builtin_amdgcn_raw_buffer_load_b64(rs_wv, (512 + tid) * 36 + 8 * pi, 0, 0);
        }
        SBAR();
        {
            const __amdgpu_buffer_rsrc_t rs_wq = __builtin_amdgcn_make_buffer_rsrc((void*)p.wq, 0, DM * DM * 2, RSRC_W3);
#pragma unroll
            for (int i = 0; i < 7; ++i) {
                const int inst = w + 8 * i;
                if (inst < 54) {
                    const int sg = 64 * inst + lane, kp = sg / 6, g = sg - 6 * kp;
                    if (g < 5) dma16(rs_wq, swap23(kp) * 1152 + 72 * pi + 16 * g, lds0 + L_W + 1024 * inst);
                }
            }
        }
        SBAR();
        bf16x8 xf[18];
        {
            const unsigned short* xp = p.x + (size_t)(T0 + c16) * DM + 8 * kq;
#pragma unroll
            for (int s = 0; s < 18; ++s) xf[s] = *(const bf16x8*)(xp + 32 * s);
        }
        SBAR();
        asm volatile("s_waitcnt vmcnt(18)" ::: "memory");
        SBAR();
        if (has_kv) {
            ldsw(L + L_W + swap23(tid) * 96 + 80, (u32x4){kp0[0], kp0[1], vp0[0], vp0[1]});
            if (tid < 64) ldsw(L + L_W + swap23(512 + tid) * 96 + 80, (u32x4){kp1[0], kp1[1], vp1[0], vp1[1]});
        }
        arrive(L, C_W, lane); waitc(L, C_W, 8);
        STAMP(1);
        f32x4 D0 = {}, D1 = {}, D2 = {}, D2t = {};
        {
            lds_p wb = L + L_W + (16 * (kq >> 1) + 4 * (kq & 1) + q4) * 96 + 8 * p4;
#pragma unroll
            for (int s = 0; s < 18; ++s) {
                const bf16x8 w0 = cat8(ldtr(wb + s * 3072), ldtr(wb + s * 3072 + 768));
                const bf16x8 w1 = cat8(ldtr(wb + s * 3072 + 32), ldtr(wb + s * 3072 + 768 + 32));
                const bf16x8 w2 = cat8(ldtr(wb + s * 3072 + 64), ldtr(wb + s * 3072 + 768 + 64));
                D0 = MF16(w0, xf[s], D0); D1 = MF16(w1, xf[s], D1); D2 = MF16(w2, xf[s], D2);
                D2t = MF16(xf[s], w2, D2t);
            }
        }
        STAMP(2);
        if (has_kv) {
            const int kt = 4 * tb + (w >> 1), rr = 16 * (w & 1) + c16, s16 = w & 1, tbase = WS_KV + kt * KVT;
            if (kq == 2) {
                u32x2 v = {cvtpk(D2[0], D2[1]), pi == 4 ? 0x3F803F80u : cvtpk(D2[2], D2[3])};
                const int off = pi == 4 ? 2304 + rr * 8 : ((pi >> 1) * 32 + rr) * 16 + (pi & 1) * 8;
                __builtin_amdgcn_raw_buffer_store_b64(v, rs_ws, tbase + off, 0, AUX_SC1);
            }
            if (c16 >= 12) {
                const int dv = 4 * pi + c16 - 12;
                u32x2 v = {cvtpk(D2t[0], D2t[1]), cvtpk(D2t[2], D2t[3])};
                if (dv == 18) v = (u32x2){0x3F803F80u, 0x3F803F80u};
                if (dv == 19) v = (u32x2){0u, 0u};
                __builtin_amdgcn_raw_buffer_store_b64(v, rs_ws, tbase + 1024 + ((s16 * 2 + (kq & 1)) * 20 + dv) * 16 + (kq >> 1) * 8, 0, AUX_SC1);
            }
        }
        if (MODE == 1) return;
        {
            lds_p qs = L + L_Q + w * 1536;
            ldsw(qs + c16 * 48 + 8 * kq, (u32x2){cvtpk(D0[0] * QSCALE, D0[1] * QSCALE), cvtpk(D0[2] * QSCALE, D0[3] * QSCALE)});
            const int a0 = kq == 0 ? c16 * 48 + 32 : (16 + c16) * 48 + 2 * (4 * kq - 2);
            const int a1 = (16 + c16) * 48 + 8 * kq;
            ldsw(qs + a0, cvtpk(D1[0] * QSCALE, D1[1] * QSCALE));
            ldsw(qs + a1, cvtpk(D1[2] * QSCALE, D1[3] * QSCALE));
            if (kq == 0) {
                ldsw(qs + (16 + c16) * 48 + 28, cvtpk(D2[0] * QSCALE, D2[1] * QSCALE));
                ldsw(qs + (16 + c16) * 48 + 32, cvtpk(D2[2] * QSCALE, D2[3] * QSCALE));
            }
        }
        arrive(L, C_Q, lane);
        if (MODE == 0) {
            if (has_kv) { DRAIN(); arrive(L, C_P, lane); }
            if (has_kv && w == 0) {
                waitc(L, C_P, 8);
                if (lane < 8) gstore_sc1((unsigned*)(p.ws + WS_FLAG1 + lane * 2048 + tb * 128 + pi * 4), tag);
            }
            if (w == 7) {
                const unsigned* f = (const unsigned*)(p.ws + WS_FLAG1 + xcd * 2048);
                const int i0 = lane, i1 = 64 + (lane & 15);
                const unsigned* f0 = f + (i0 / 5) * 32 + i0 % 5; const unsigned* f1 = f + (i1 / 5) * 32 + i1 % 5;
                for (unsigned spins = 0; spins < SPIN_GLB; ++spins) {
                    const bool ok = gload_sc1(f0) == tag && gload_sc1(f1) == tag;
                    if (__all(ok)) break;
                    __builtin_amdgcn_s_sleep(2);
                }
                __builtin_amdgcn_fence(__ATOMIC_ACQUIRE, "wavefront");
                arrive(L, C_F1, lane);
            }
        }
        waitc(L, C_Q, 8);
        {
            lds_p qa = L + L_Q + wq4 * 1536 + r32 * 48, qb = qa + 4 * 1536;
            qmA = ldsr<bf16x8>(qa + 16 * h); qmB = ldsr<bf16x8>(qb + 16 * h);
            qeA = ldsr<unsigned>(qa + 32); qeB = ldsr<unsigned>(qb + 32);
        }
        arrive(L, C_QR, lane);
        if (MODE == 0) waitc(L, C_F1, 1);
        STAMP(3);
    }

    if (MODE == 0 || MODE == 2) {
        lds_p ring = L + L_W + kh * (8 * KVT);
        const int gsrc = WS_KV + (32 * kh + wq4) * KVT;
        u32x4 st0, st1; u32x2 st2;
#define KV_LOAD(c) do { const int o_ = gsrc + (c) * (4 * KVT); \
        st0 = __builtin_amdgcn_raw_buffer_load_b128(rs_ws, o_ + lane * 16, 0, AUX_SC1); \
        st1 = __builtin_amdgcn_raw_buffer_load_b128(rs_ws, o_ + 1024 + lane * 16, 0, AUX_SC1); \
        st2 = __builtin_amdgcn_raw_buffer_load_b64(rs_ws, o_ + 2048 + lane * 8, 0, AUX_SC1); } while (0)
#define KV_PUT(c) do { lds_p d_ = ring + (((c) & 1) * 4 + wq4) * KVT; \
        ldsw(d_ + lane * 16, st0); ldsw(d_ + 1024 + lane * 16, st1); ldsw(d_ + 2048 + lane * 8, st2); } while (0)
        lds_p kb = ring + lane * 16, eb = ring + 2304 + r32 * 8, vb = ring + 1024 + (h * 20 + (r32 < 19 ? r32 : 19)) * 16;
        const f32x16 Z = {};
        unsigned mA = 0, mB = 0;
        f32x16 OA, OB;
        for (int pass = 0; pass < 2; ++pass) {
            const unsigned cb = pass * 32;
            KV_LOAD(0); KV_PUT(0); arrive(L, C_RW0 + kh, lane);
            KV_LOAD(1); KV_PUT(1); arrive(L, C_RW0 + kh, lane);
            KV_LOAD(2);
            if (pass == 0) {
                const bf16x8 km = BC8(__builtin_amdgcn_raw_buffer_load_b128(rs_ws, WS_KV + lane * 16, 0, AUX_SC1));
                const bf16x4 ke = __builtin_bit_cast(bf16x4, __builtin_amdgcn_raw_buffer_load_b64(rs_ws, WS_KV + 2304 + r32 * 8, 0, AUX_SC1));
                const u32x2 ea = {h ? 0u : qeA, 0u}, ebq = {h ? 0u : qeB, 0u};
                f32x16 sa = MF32(km, qmA, Z), sb = MF32(km, qmB, Z);
                sa = MF32E(ke, __builtin_bit_cast(bf16x4, ea), sa); sb = MF32E(ke, __builtin_bit_cast(bf16x4, ebq), sb);
                float ma = sa[0], mb = sb[0];
#pragma unroll
                for (int i = 1; i < 16; ++i) { ma = fmaxf(ma, sa[i]); mb = fmaxf(mb, sb[i]); }
                ma = fmaxf(ma, __shfl_xor(ma, 32)); mb = fmaxf(mb, __shfl_xor(mb, 32));
                const float ah = bflo(cvtpk(ma, 0.f)), bh = bflo(cvtpk(mb, 0.f));
                mA = cvtpk(-ah, ah - ma); mB = cvtpk(-bh, bh - mb);
            }
            const u32x2 ea = {h ? 0u : qeA, h ? 0u : mA}, ebq = {h ? 0u : qeB, h ? 0u : mB};
            const bf16x4 qxA = __builtin_bit_cast(bf16x4, ea), qxB = __builtin_bit_cast(bf16x4, ebq);
            waitc(L, C_RW0 + kh, cb + 4);
            f32x16 SA, SB; u32x4 pa0, pa1, pb0 = {}, pb1 = {}; bf16x8 km, v0, v1; bf16x4 ke;
            OA = Z; OB = Z;
            km = ldsr<bf16x8>(kb); ke = ldsr<bf16x4>(eb);
            SA = MF32(km, qmA, Z); SA = MF32E(ke, qxA, SA);
            v0 = ldsr<bf16x8>(vb); v1 = ldsr<bf16x8>(vb + 640);
#define PH(SC, SN, QM, QX, ON, pp0, pp1, pn0, pn1, LOADS1, LOADS2) do { \
            ON = MF32(v0, BC8(pp0), ON); \
            SC[0] = EXP2(SC[0]); SC[1] = EXP2(SC[1]); SC[2] = EXP2(SC[2]); SC[3] = EXP2(SC[3]); SBAR(); \
            SN = MF32(km, QM, Z); SBAR(); \
            SC[4] = EXP2(SC[4]); SC[5] = EXP2(SC[5]); pn0[0] = cvtpk(SC[0], SC[1]); SC[6] = EXP2(SC[6]); SC[7] = EXP2(SC[7]); pn0[1] = cvtpk(SC[2], SC[3]); SBAR(); \
            ON = MF32(v1, BC8(pp1), ON); SBAR(); \
            LOADS1; \
            SC[8] = EXP2(SC[8]); SC[9] = EXP2(SC[9]); pn0[2] = cvtpk(SC[4], SC[5]); SC[10] = EXP2(SC[10]); SC[11] = EXP2(SC[11]); pn0[3] = cvtpk(SC[6], SC[7]); SBAR(); \
            SN = MF32E(ke, QX, SN); SBAR(); \
            LOADS2; \
            SC[12] = EXP2(SC[12]); SC[13] = EXP2(SC[13]); pn1[0] = cvtpk(SC[8], SC[9]); SC[14] = EXP2(SC[14]); SC[15] = EXP2(SC[15]); pn1[1] = cvtpk(SC[10], SC[11]); \
            pn1[2] = cvtpk(SC[12], SC[13]); pn1[3] = cvtpk(SC[14], SC[15]); PIN(pn0); PIN(pn1); SBAR(); \
        } while (0)
#define TILE(t) \
            PH(SA, SB, qmB, qxB, OB, pb0, pb1, pa0, pa1, (v0 = ldsr<bf16x8>(vb + (t) * KVT), v1 = ldsr<bf16x8>(vb + (t) * KVT + 640)), \
               (km = ldsr<bf16x8>(kb + (((t) + 1) & 7) * KVT), ke = ldsr<bf16x4>(eb + (((t) + 1) & 7) * KVT))); \
            PH(SB, SA, qmA, qxA, OA, pa0, pa1, pb0, pb1, (void)0, (void)0);
            for (int it = 0; it < 4; ++it) {
                const int c = 2 * it;
                TILE(0) TILE(1)
                if (it > 0) { waitc(L, C_RR0 + kh, cb + 4 * c); KV_PUT(c + 1); arrive(L, C_RW0 + kh, lane); if (it < 3) KV_LOAD(c + 2); }
                TILE(2)
                waitc(L, C_RW0 + kh, cb + 4 * (c + 2));
                TILE(3)
                arrive(L, C_RR0 + kh, lane);
                TILE(4) TILE(5)
                if (it < 3) { waitc(L, C_RR0 + kh, cb + 4 * (c + 1)); KV_PUT(c + 2); arrive(L, C_RW0 + kh, lane); KV_LOAD(c + 3); }
                TILE(6)
                if (it < 3) waitc(L, C_RW0 + kh, cb + 4 * (c + 3));
                TILE(7)
                arrive(L, C_RR0 + kh, lane);
            }
            OB = MF32(v0, BC8(pb0), OB); OB = MF32(v1, BC8(pb1), OB);
#undef TILE
#undef PH
            STAMP(4);
            waitc(L, C_QR, 8);
            lds_p xo = (w < 4 ? L + L_X1 : L + L_Q) + wq4 * 3072, xi = (w < 4 ? L + L_Q : L + L_X1) + wq4 * 3072;
            {
                const f32x16& S = kh ? OA : OB;
#pragma unroll
                for (int g = 0; g < 3; ++g) ldsw(xo + g * 1024 + lane * 16, (f32x4){S[4 * g], S[4 * g + 1], S[4 * g + 2], S[4 * g + 3]});
            }
            arrive(L, C_X0 + wq4, lane); waitc(L, C_X0 + wq4, 2 * (pass + 1));
            float o[12];
#pragma unroll
            for (int g = 0; g < 3; ++g) {
                const f32x4 t = ldsr<f32x4>(xi + g * 1024 + lane * 16);
#pragma unroll
                for (int i = 0; i < 4; ++i) o[4 * g + i] = (kh ? OB[4 * g + i] : OA[4 * g + i]) + t[i];
            }
            const float lsum = __shfl(o[10], r32);
            const float rl = 1.f / lsum;
            bool bad = !(lsum < 1.2676506e30f);
#pragma unroll
            for (int i = 0; i < 8; ++i) bad |= !(fabsf(o[i]) < 3.0e38f);
            if (h == 0) bad |= !(fabsf(o[8]) < 3.0e38f) || !(fabsf(o[9]) < 3.0e38f);
            {
                lds_p os = xi;
                lds_p ob = os + (r32 & 15) * 72 + (r32 >> 4) * 36 + 8 * h;
                asm volatile("s_waitcnt lgkmcnt(0)" ::: "memory");
                ldsw(ob, cvtpk(o[0] * rl, o[1] * rl)); ldsw(ob + 4, cvtpk(o[2] * rl, o[3] * rl));
                ldsw(ob + 16, cvtpk(o[4] * rl, o[5] * rl)); ldsw(ob + 20, cvtpk(o[6] * rl, o[7] * rl));
                if (h == 0) ldsw(ob + 32, cvtpk(o[8] * rl, o[9] * rl));
                const int tok0 = 128 * tb + 16 * w;
#pragma unroll
                for (int i = 0; i < 3; ++i) {
                    const int e = lane + 64 * i;
                    if (e < 144) {
                        const int tk = e / 9, pc = e - 9 * tk;
                        const u32x2 v = ldsr<u32x2>(os + tk * 72 + pc * 8);
                        __builtin_amdgcn_raw_buffer_store_b64(v, rs_ws, WS_O + (tok0 + tk) * 1152 + 72 * pi + pc * 8, 0, AUX_SC1);
                    }
                }
            }
            if (pass == 0 && __any(bad) && lane == 0) ((volatile LAS unsigned*)(L + L_C))[C_BAD] = 1u;
            DRAIN();
            arrive(L, C_O, lane); waitc(L, C_O, 8 * (pass + 1));
            const bool redo = pass == 0 && __builtin_amdgcn_readfirstlane(((volatile LAS unsigned*)(L + L_C))[C_BAD]) != 0;
            if (!redo) break;
            {
                const u32x2 ea0 = {h ? 0u : qeA, 0u}, eb0 = {h ? 0u : qeB, 0u};
                float ma = -INFINITY, mb = -INFINITY;
                for (int t = 0; t < 64; ++t) {
                    const u32x4 kmv = __builtin_amdgcn_raw_buffer_load_b128(rs_ws, WS_KV + t * KVT + lane * 16, 0, AUX_SC1);
                    const u32x2 kev = __builtin_amdgcn_raw_buffer_load_b64(rs_ws, WS_KV + t * KVT + 2304 + r32 * 8, 0, AUX_SC1);
                    f32x16 sa = MF32(BC8(kmv), qmA, Z), sb = MF32(BC8(kmv), qmB, Z);
                    sa = MF32E(__builtin_bit_cast(bf16x4, kev), __builtin_bit_cast(bf16x4, ea0), sa);
                    sb = MF32E(__builtin_bit_cast(bf16x4, kev), __builtin_bit_cast(bf16x4, eb0), sb);
#pragma unroll
                    for (int i = 0; i < 16; ++i) { ma = fmaxf(ma, sa[i]); mb = fmaxf(mb, sb[i]); }
                }
                ma = fmaxf(ma, __shfl_xor(ma, 32)); mb = fmaxf(mb, __shfl_xor(mb, 32));
                const float ah = bflo(cvtpk(ma, 0.f)), bh = bflo(cvtpk(mb, 0.f));
                mA = cvtpk(-ah, ah - ma); mB = cvtpk(-bh, bh - mb);
            }
        }
#undef KV_LOAD
#undef KV_PUT
        if (MODE == 0 && w == 0 && lane == 0) gstore_sc1((unsigned*)(p.ws + WS_FLAG2 + tb * 128 + pi * 4), tag);
        STAMP(5);
    }

    if (MODE == 0 || MODE == 3) {
        if (MODE == 0) {
            if (w == 7) {
                const unsigned* f = (const unsigned*)(p.ws + WS_FLAG2 + tb * 128) + (lane & 15);
                for (unsigned spins = 0; spins < SPIN_GLB; ++spins) {
                    if (__all(gload_sc1(f) == tag)) break;
                    __builtin_amdgcn_s_sleep(2);
                }
                __builtin_amdgcn_fence(__ATOMIC_ACQUIRE, "wavefront");
                arrive(L, C_F2, lane);
            }
            waitc(L, C_F2, 1);
        }
        STAMP(6);
        const int Tt = 128 * tb + 64 * (pi & 1) + 16 * wq4, F0 = 72 * (pi >> 1);
        u32x4 of[9];
#pragma unroll
        for (int i = 0; i < 9; ++i) of[i] = __builtin_amdgcn_raw_buffer_load_b128(rs_ws, WS_O + (Tt + c16) * 1152 + (9 * kh + i) * 64 + 16 * kq, 0, AUX_SC1);
        waitc(L, C_WO, 4);
        f32x4 D[5] = {};
        lds_p wb = L + L_S + (16 * (kq >> 1) + 4 * (kq & 1) + q4) * 160 + 8 * p4 + kh * (9 * 32 * 160);
#pragma unroll
        for (int i = 0; i < 9; ++i)
#pragma unroll
            for (int T = 0; T < 5; ++T)
                D[T] = MF16(cat8(ldtr(wb + i * 5120 + 32 * T), ldtr(wb + i * 5120 + 1280 + 32 * T)), BC8(of[i]), D[T]);
        lds_p xo = L + L_W + w * 3072, xi = L + L_W + (w ^ 4) * 3072;
        if (kh == 0) { ldsw(xo + lane * 16, D[3]); ldsw(xo + 1024 + lane * 16, D[4]); }
        else { ldsw(xo + lane * 16, D[0]); ldsw(xo + 1024 + lane * 16, D[1]); ldsw(xo + 2048 + lane * 16, D[2]); }
        arrive(L, C_Y0 + wq4, lane); waitc(L, C_Y0 + wq4, 2);
        unsigned short* op = p.out + (size_t)(Tt + c16) * (2 * DM) + F0 + 4 * kq;
        if (kh == 0) {
#pragma unroll
            for (int T = 0; T < 3; ++T) {
                const f32x4 v = D[T] + ldsr<f32x4>(xi + T * 1024 + lane * 16);
                *(u32x2*)(op + 16 * T) = (u32x2){cvtpk(v[0], v[1]), cvtpk(v[2], v[3])};
            }
        } else {
#pragma unroll
            for (int T = 3; T < 5; ++T) {
                const f32x4 v = D[T] + ldsr<f32x4>(xi + (T - 3) * 1024 + lane * 16);
                if (T == 3 || kq < 2) *(u32x2*)(op + 16 * T) = (u32x2){cvtpk(v[0], v[1]), cvtpk(v[2], v[3])};
            }
        }
        STAMP(7);
        if (MODE == 0 && bid == 0 && w == 7) {
            const unsigned* f = (const unsigned*)(p.ws + WS_FLAG2);
            for (unsigned spins = 0; spins < SPIN_GLB; ++spins) {
                bool ok = true;
#pragma unroll
                for (int i = 0; i < 4; ++i) { const int e = lane + 64 * i; ok &= gload_sc1(f + (e >> 4) * 32 + (e & 15)) == tag; }
                if (__all(ok)) break;
                __builtin_amdgcn_s_sleep(2);
            }
            if (lane == 0) gstore_sc1((unsigned*)(p.ws + WS_EPOCH), tag);
        }
    }
}

}

extern "C" void kernel_launch(void* const* d_in, const int* in_sizes, int n_in, void* d_out, int out_size, void* d_ws, size_t ws_size,
                              hipStream_t stream) {
    Params p;
    p.x = (const unsigned short*)d_in[0]; p.wq = (const unsigned short*)d_in[1]; p.wk = (const unsigned short*)d_in[2];
    p.wv = (const unsigned short*)d_in[3]; p.wo = (const unsigned short*)d_in[4]; p.u = (const unsigned short*)d_in[5];
    p.are = (const unsigned short*)d_in[6]; p.aim = (const unsigned short*)d_in[7]; p.bre = (const unsigned short*)d_in[8];
    p.bim = (const unsigned short*)d_in[9]; p.cre = (const unsigned short*)d_in[10]; p.cim = (const unsigned short*)d_in[11];
    p.ldt = (const unsigned short*)d_in[12]; p.dsk = (const unsigned short*)d_in[13];
    p.out = (unsigned short*)d_out; p.ws = (char*)d_ws;
    hipFuncSetAttribute((const void*)fused_kernel<0>, hipFuncAttributeMaxDynamicSharedMemorySize, L_END);
    fused_kernel<0><<<256, 768, L_END, stream>>>(p);
}
```

```cpp
#include <hip/hip_runtime.h>
#include <hip/hip_bf16.h>
#include <stdint.h>

#define U_BY 3
#define S_PRIO0 0
#define S_PRIOB 0
#define S_PRIOC 3
#define MK_TOUCH 0
#define MK_NOSSM 0
#define MK_STAMPS 0

namespace {

typedef __attribute__((ext_vector_type(16))) float f32x16;
typedef __attribute__((ext_vector_type(4))) float f32x4;
typedef __attribute__((ext_vector_type(2))) float f32x2;
typedef __attribute__((ext_vector_type(8))) short bf16x8;
typedef __attribute__((ext_vector_type(4))) short bf16x4;
typedef __attribute__((ext_vector_type(2))) __bf16 bf16x2_t;
typedef __attribute__((ext_vector_type(4))) unsigned u32x4;
typedef __attribute__((ext_vector_type(2))) unsigned u32x2;
typedef short v4i16_t __attribute__((ext_vector_type(4)));
#define LAS __attribute__((address_space(3)))
#define GAS __attribute__((address_space(1)))
typedef LAS char* lds_p;
typedef const unsigned short* cbf;

constexpr int SEQ = 2048, DM = 576;
constexpr float QSCALE = 0.34005040627f;
constexpr float LOG2E = 1.4426950408889634f;
constexpr float INV2PI = 0.15915494309189535f;

constexpr int WS_EPOCH = 0;
constexpr int WS_FLAG1 = 4096;
constexpr int WS_FLAG2 = WS_FLAG1 + 16384;
constexpr int WS_KV = 32768;
constexpr int KVT = 2560;
constexpr int WS_O = WS_KV + 64 * KVT;
constexpr int WS_BYTES = WS_O + SEQ * DM * 2;
constexpr int WS_STAMP = 8 * 1024 * 1024;

constexpr int L_W = 0;
constexpr int L_X1 = 40960;
constexpr int L_S = 55296;
constexpr int L_Q = L_S + 87360;
constexpr int L_C = L_Q + 16384;
constexpr int L_END = L_C + 256;
constexpr int SCH = 21840;
constexpr int SCH_U = 16640;
enum { C_W = 0, C_P, C_Q, C_F1, C_RW, C_B, C_BAD, C_U, C_S2, C_D, C_NUM };

constexpr int RSRC_W3 = 0x00020000;
constexpr int AUX_SC1 = 16;
constexpr unsigned SPIN_LDS = 1u << 20, SPIN_GLB = 1u << 16;

#define SBAR() __builtin_amdgcn_sched_barrier(0)
#define PIN(x) asm volatile("" : "+v"(x))
#define CBAR() asm volatile("" ::: "memory")
#define DRAIN() asm volatile("s_waitcnt vmcnt(0)" ::: "memory")
#define MF32(a, b, c) __builtin_amdgcn_mfma_f32_32x32x16_bf16(a, b, c, 0, 0, 0)
#define MF32E(a, b, c) __builtin_amdgcn_mfma_f32_32x32x8bf16_1k(a, b, c, 0, 0, 0)
#define MF16(a, b, c) __builtin_amdgcn_mfma_f32_16x16x32_bf16(a, b, c, 0, 0, 0)
#define EXP2(x) __builtin_amdgcn_exp2f(x)
#define BC8(x) __builtin_bit_cast(bf16x8, x)
#define DI __device__ __forceinline__

DI unsigned cvtpk(float lo, float hi) { f32x2 v = {lo, hi}; return __builtin_bit_cast(unsigned, __builtin_convertvector(v, bf16x2_t)); }
DI float bflo(unsigned x) { return __builtin_bit_cast(float, x << 16); }
DI float bfhi(unsigned x) { return __builtin_bit_cast(float, x & 0xffff0000u); }
DI float bf1(unsigned short x) { return __builtin_bit_cast(float, (unsigned)x << 16); }
template <class T> DI T ldsr(lds_p p) { return *(const LAS T*)p; }
template <class T> DI void ldsw(lds_p p, T v) { *(LAS T*)p = v; }
DI bf16x4 ldtr(lds_p p) { return __builtin_bit_cast(bf16x4, __builtin_amdgcn_ds_read_tr16_b64_v4i16((LAS v4i16_t*)p)); }
DI bf16x8 cat8(bf16x4 a, bf16x4 b) { return __builtin_shufflevector(a, b, 0, 1, 2, 3, 4, 5, 6, 7); }
DI int swap23(int k) { return (k & ~12) | ((k & 4) << 1) | ((k & 8) >> 1); }
DI void dma16(__amdgpu_buffer_rsrc_t rs, int voff, unsigned lds_base) {
    unsigned keep;
    asm volatile("s_mov_b32 %0, m0\n\ts_mov_b32 m0, %3\n\ts_nop 0\n\tbuffer_load_dwordx4 %1, %2, 0 offen lds\n\ts_mov_b32 m0, %0"
                 : "=&s"(keep) : "v"(voff), "s"(rs), "s"(lds_base) : "memory");
}
DI void dma16_sc1(__amdgpu_buffer_rsrc_t rs, int voff, unsigned lds_base) {
    unsigned keep;
    asm volatile("s_mov_b32 %0, m0\n\ts_mov_b32 m0, %3\n\ts_nop 0\n\tbuffer_load_dwordx4 %1, %2, 0 offen sc1 lds\n\ts_mov_b32 m0, %0"
                 : "=&s"(keep) : "v"(voff), "s"(rs), "s"(lds_base) : "memory");
}
#define BARRIER() asm volatile("s_waitcnt lgkmcnt(0)\n\ts_barrier" ::: "memory")
DI void arrive(lds_p L, int c, int lane) {
    CBAR();
    if (lane == 0) (void)__hip_atomic_fetch_add((LAS unsigned*)(L + L_C) + c, 1u, __ATOMIC_RELAXED, __HIP_MEMORY_SCOPE_WORKGROUP);
    CBAR();
}
DI void waitc(lds_p L, int c, unsigned target) {
    CBAR();
    volatile LAS unsigned* p = (volatile LAS unsigned*)(L + L_C) + c;
    for (unsigned spins = 0; spins < SPIN_LDS; ++spins) {
        if ((unsigned)__builtin_amdgcn_readfirstlane(*p) >= target) break;
        __builtin_amdgcn_s_sleep(1);
    }
    CBAR();
}
DI unsigned peek(lds_p L, int c) { return *((volatile LAS unsigned*)(L + L_C) + c); }
DI void waitp(lds_p L, int c, unsigned seen, unsigned target) { if ((unsigned)__builtin_amdgcn_readfirstlane(seen) < target) waitc(L, c, target); CBAR(); }
DI unsigned gload_sc1(const unsigned* p) { return __hip_atomic_load((GAS unsigned*)p, __ATOMIC_RELAXED, __HIP_MEMORY_SCOPE_AGENT); }
DI void gstore_sc1(unsigned* p, unsigned v) { __hip_atomic_store((GAS unsigned*)p, v, __ATOMIC_RELAXED, __HIP_MEMORY_SCOPE_AGENT); }

struct cplx2 { f32x2 re, im; };
DI cplx2 cmul(const cplx2& a, const cplx2& b) { cplx2 r; r.re = a.re * b.re - a.im * b.im; r.im = a.re * b.im + a.im * b.re; return r; }
DI cplx2 cexp(f32x2 ar2, f32x2 airev, float e) {
    f32x2 m = ar2 * e, t = airev * e; cplx2 r;
#pragma unroll
    for (int i = 0; i < 2; ++i) {
        const float mag = EXP2(m[i]), fr = __builtin_amdgcn_fractf(t[i]);
        r.re[i] = mag * __builtin_amdgcn_cosf(fr); r.im[i] = mag * __builtin_amdgcn_sinf(fr);
    }
    return r;
}

struct Params {
    const unsigned short *x, *wq, *wk, *wv, *wo, *u, *are, *aim, *bre, *bim, *cre, *cim, *ldt, *dsk;
    unsigned short* out;
    char* ws;
};

#define STAMP(i) do {} while (0)
#define CSTAMP(i) do {} while (0)
#define STAMP_DECL do {} while (0)
#define STAMP_DUMP() do {} while (0)
#define STAMP_ARG
#define STAMP_PASS

template <int G> DI void conv_group(lds_p L, lds_p R, f32x4 (&acc)[8], int pa0, int pb0) {
    if ((unsigned)__builtin_amdgcn_readfirstlane(peek(L, C_Q)) >= 8u) __builtin_amdgcn_s_setprio(S_PRIOC);
    lds_p a1 = R + pa0 - 64 * (8 * G), b1 = R + pb0 - 64 * (8 * G), a2 = R + pa0 - 64 * (63 - 8 * G), b2 = R + pb0 - 64 * (63 - 8 * G);
    PIN(a1); PIN(b1); PIN(a2); PIN(b2);
#define RDA(ptr) cat8(*(const volatile LAS bf16x4*)(ptr), *(const volatile LAS bf16x4*)((ptr) + 8))
    bf16x8 P1 = RDA(a1), P2 = RDA(a2), Q1, Q2, B1[8 - G], B2[G + 1];
#pragma unroll
    for (int n = 0; n < 8 - G; ++n) B1[n] = ldsr<bf16x8>(b1 + 512 * (G + n));
#pragma unroll
    for (int n = 0; n < G + 1; ++n) B2[n] = ldsr<bf16x8>(b2 + 512 * (7 - G + n));
#define CONV_STEP(C1, C2, N1, N2) do { \
        a1 -= 64; b1 -= 64; a2 += 64; b2 += 64; PIN(a1); PIN(b1); PIN(a2); PIN(b2); \
        N1 = RDA(a1); N2 = RDA(a2); SBAR(); \
        _Pragma("unroll") for (int n = 0; n < 8 - G; ++n) { acc[G + n] = MF16(C1, B1[n], acc[G + n]); SBAR(); B1[n] = ldsr<bf16x8>(b1 + 512 * (G + n)); SBAR(); } \
        _Pragma("unroll") for (int n = 0; n < G + 1; ++n) { acc[7 - G + n] = MF16(C2, B2[n], acc[7 - G + n]); SBAR(); B2[n] = ldsr<bf16x8>(b2 + 512 * (7 - G + n)); SBAR(); } \
    } while (0)
#pragma unroll 1
    for (int i = 0; i < 4; ++i) { CONV_STEP(P1, P2, Q1, Q2); CONV_STEP(Q1, Q2, P1, P2); }
#undef CONV_STEP
#undef RDA
}

DI void ssm_wave(const Params& p, lds_p L, int s, int lane, int ch0 STAMP_ARG) {
    const int d = ch0 + s; const int w = 8 + s; (void)w;
    __builtin_amdgcn_s_setprio(S_PRIO0);
    lds_p R = L + L_S + s * SCH;
    const int pb = d * 128 + lane;
    const f32x2 are = {bf1(p.are[pb]), bf1(p.are[pb + 64])}, aim = {bf1(p.aim[pb]), bf1(p.aim[pb + 64])};
    const f32x2 bre = {bf1(p.bre[pb]), bf1(p.bre[pb + 64])}, bim = {bf1(p.bim[pb]), bf1(p.bim[pb + 64])};
    const f32x2 cre = {bf1(p.cre[pb]), bf1(p.cre[pb + 64])}, cim = {bf1(p.cim[pb]), bf1(p.cim[pb + 64])};
    const float ldt = bf1(p.ldt[d]);
    const u32x2 dsk = *(const u32x2*)(p.dsk + ch0);
    const int t0 = 512 * s + 8 * lane;
    const float dt = bflo(cvtpk(expf(ldt), 0.f));
    const f32x2 ar2 = (are * dt) * LOG2E, airev = (aim * dt) * INV2PI;
    cplx2 dB; dB.re = bre * dt; dB.im = bim * dt;
    cplx2 C; C.re = cre; C.im = cim;
    const cplx2 CB = cmul(C, dB);
    const cplx2 w1 = cexp(ar2, airev, 1.f), w8 = cexp(ar2, airev, 8.f), w128 = cexp(ar2, airev, 128.f);

    STAMP(3);
    lds_p st0 = R + lane * 16, st1 = R + 4224 + lane * 16;
    const int kq = lane >> 4, q4 = (lane >> 2) & 3, p4 = lane & 3;
    lds_p trb = R + (p4 >> 1) * 4224 + (16 * (kq >> 1) + 4 * (kq & 1) + q4) * 16 + (p4 & 1) * 8;
#define PUT16(V, NEG) do { \
        u32x4 a0, a1, b0, b1, c0, c1, d0, d1; \
        _Pragma("unroll") for (int j = 0; j < 4; ++j) { \
            a0[j] = cvtpk(V[2 * j].re[0], V[2 * j + 1].re[0]); a1[j] = cvtpk(V[8 + 2 * j].re[0], V[9 + 2 * j].re[0]); \
            b0[j] = cvtpk(V[2 * j].re[1], V[2 * j + 1].re[1]); b1[j] = cvtpk(V[8 + 2 * j].re[1], V[9 + 2 * j].re[1]); \
            c0[j] = cvtpk(NEG V[2 * j].im[0], NEG V[2 * j + 1].im[0]); c1[j] = cvtpk(NEG V[8 + 2 * j].im[0], NEG V[9 + 2 * j].im[0]); \
            d0[j] = cvtpk(NEG V[2 * j].im[1], NEG V[2 * j + 1].im[1]); d1[j] = cvtpk(NEG V[8 + 2 * j].im[1], NEG V[9 + 2 * j].im[1]); } \
        ldsw(st0, a0); ldsw(st1, a1); ldsw(st0 + 1024, b0); ldsw(st1 + 1024, b1); \
        ldsw(st0 + 2048, c0); ldsw(st1 + 2048, c1); ldsw(st0 + 3072, d0); ldsw(st1 + 3072, d1); } while (0)
#define GETF(ks) cat8(ldtr(trb + (ks) * 512), ldtr(trb + (ks) * 512 + 128))
    bf16x8 yf[8];
    {
        cplx2 V[16];
        V[0].re = (f32x2){1.f, 1.f}; V[0].im = (f32x2){0.f, 0.f}; V[1].re = w8.re; V[1].im = -w8.im;
        V[2] = cmul(V[1], V[1]); V[3] = cmul(V[2], V[1]); V[4] = cmul(V[2], V[2]);
        V[5] = cmul(V[4], V[1]); V[6] = cmul(V[4], V[2]); V[7] = cmul(V[4], V[3]); V[8] = cmul(V[4], V[4]);
#pragma unroll
        for (int j = 9; j < 16; ++j) V[j] = cmul(V[8], V[j - 8]);
        PUT16(V, +);
#pragma unroll
        for (int ks = 0; ks < 8; ++ks) yf[ks] = GETF(ks);
    }
    STAMP(9);
    const cplx2 w2 = cmul(w1, w1), w3 = cmul(w2, w1), w4 = cmul(w2, w2), w5 = cmul(w4, w1), w6 = cmul(w4, w2), w7 = cmul(w4, w3);
    f32x4 D0 = {}, D1 = {}, D2 = {};
    cplx2 G = cmul(CB, w128);
    {
        cplx2 V[16];
        V[0] = CB; V[1] = cmul(CB, w1); V[2] = cmul(CB, w2); V[3] = cmul(CB, w3); V[4] = cmul(CB, w4); V[5] = cmul(CB, w5); V[6] = cmul(CB, w6); V[7] = cmul(CB, w7);
        V[8] = G; V[9] = cmul(G, w1); V[10] = cmul(G, w2); V[11] = cmul(G, w3); V[12] = cmul(G, w4); V[13] = cmul(G, w5); V[14] = cmul(G, w6); V[15] = cmul(G, w7);
        PUT16(V, +);
        bf16x8 xf[8];
#pragma unroll
        for (int ks = 0; ks < 8; ++ks) xf[ks] = GETF(ks);
        SBAR();
#pragma unroll
        for (int ks = 0; ks < 8; ++ks) D0 = MF16(xf[ks], yf[ks], D0);
    }
    PIN(D0); STAMP(10);
    {
        cplx2 V[16];
        G = cmul(G, w128); V[0] = G; V[1] = cmul(G, w2); V[2] = cmul(G, w4); V[3] = cmul(G, w6);
        G = cmul(G, w128); V[4] = G; V[5] = cmul(G, w2); V[6] = cmul(G, w4); V[7] = cmul(G, w6);
#pragma unroll
        for (int i = 0; i < 4; ++i) { G = cmul(G, w128); V[8 + 2 * i] = G; V[9 + 2 * i] = cmul(G, w4); }
        PUT16(V, +);
        bf16x8 xf[8];
#pragma unroll
        for (int ks = 0; ks < 8; ++ks) xf[ks] = GETF(ks);
        SBAR();
#pragma unroll
        for (int ks = 0; ks < 8; ++ks) D1 = MF16(xf[ks], yf[ks], D1);
    }
    {
        cplx2 V[16];
#pragma unroll
        for (int i = 0; i < 9; ++i) { G = cmul(G, w128); V[i] = G; }
#pragma unroll
        for (int i = 9; i < 16; ++i) V[i] = G;
        PUT16(V, +);
        bf16x8 xf[8];
#pragma unroll
        for (int ks = 0; ks < 8; ++ks) xf[ks] = GETF(ks);
        SBAR();
#pragma unroll
        for (int ks = 0; ks < 8; ++ks) D2 = MF16(yf[ks], xf[ks], D2);
    }
#undef PUT16
#undef GETF
    PIN(D2); STAMP(4);
    lds_p tab = R + 8448;
    {
        const int j = lane & 15, rq = lane >> 4;
        ldsw(tab + 2 * (8 * j + 4 * (rq & 1) + 128 * (rq >> 1)), (u32x2){cvtpk(D0[0], D0[1]), cvtpk(D0[2], D0[3])});
        const int e0 = rq < 2 ? 256 + 64 * rq + 4 * j : 384 + 64 * (rq - 2) + 2 * j, e1 = rq < 2 ? e0 + 2 : e0 + 32;
        ldsw(tab + 2 * e0, cvtpk(D1[0], D1[1])); ldsw(tab + 2 * e1, cvtpk(D1[2], D1[3]));
        if (j < 8 || lane == 8) ldsw(tab + 2 * (512 + 16 * j + 4 * rq), (u32x2){cvtpk(D2[0], D2[1]), cvtpk(D2[2], D2[3])});
    }
#define PK(hi_src, hi_half, lo_src, lo_half) __builtin_amdgcn_perm(hi_src, lo_src, ((hi_half) ? 0x07060000u : 0x05040000u) | ((lo_half) ? 0x0302u : 0x0100u))
    {
#pragma unroll
        for (int i = 0; i < 2; ++i) {
            const int g = lane + 64 * i, m = 255 - g;
            const unsigned lo = ldsr<unsigned short>(tab + 2 * (384 + m)), hi = ldsr<unsigned short>(tab + 2 * (385 + m));
            const unsigned mid = (m & 1) ? hi : lo, ll = PK(lo, 0, lo, 0);
            ldsw(R + 16 * g, (u32x4){PK(hi, 0, hi, 0), PK(mid, 0, hi, 0), ll, ll});
        }
        {
            const int g = 128 + lane, m = 255 - g;
            const unsigned ab = ldsr<unsigned>(tab + 2 * (256 + 2 * m)), c = ldsr<unsigned short>(tab + 2 * (258 + 2 * m));
            ldsw(R + 16 * g, (u32x4){PK(c, 0, c, 0), PK(ab, 1, ab, 1), PK(ab, 0, ab, 1), PK(ab, 0, ab, 0)});
        }
        {
            const int g = 192 + lane, m = 255 - g;
            u32x4 v;
            if (lane < 32) {
                const u32x2 q = ldsr<u32x2>(tab + 2 * (128 + 4 * m)); const unsigned e = ldsr<unsigned short>(tab + 2 * (132 + 4 * m));
                v = (u32x4){PK(q[1], 1, e, 0), PK(q[1], 0, q[1], 0), PK(q[0], 1, q[1], 0), PK(q[0], 0, q[0], 0)};
            } else {
                const u32x4 q = ldsr<u32x4>(tab + 16 * m);
                v = (u32x4){PK(q[3], 0, q[3], 1), PK(q[2], 0, q[2], 1), PK(q[1], 0, q[1], 1), PK(q[0], 0, q[0], 1)};
            }
            ldsw(R + 16 * g, v);
        }
        if (lane < 4) ldsw(R + 16 * (256 + lane), (u32x4){0u, 0u, 0u, 0u});
    }
#undef PK
    if (lane < 32) ldsw(R + SCH_U + 16 * lane, (u32x4){0u, 0u, 0u, 0u});
#pragma unroll
    for (int i = 0; i < 5; ++i) {
        const int g = lane + 64 * i;
        if (i < 4 || lane < 4) {
            const u32x4 a = ldsr<u32x4>(R + 16 * g); const u32x2 b = ldsr<u32x2>(R + 16 * g + 16);
            const unsigned a0 = __builtin_amdgcn_alignbit(a[1], a[0], 16), a1 = __builtin_amdgcn_alignbit(a[2], a[1], 16), a2 = __builtin_amdgcn_alignbit(a[3], a[2], 16),
                           a3 = __builtin_amdgcn_alignbit(b[0], a[3], 16), a4 = __builtin_amdgcn_alignbit(b[1], b[0], 16);
            ldsw(R + 4160 + 16 * g, (u32x4){a0, a1, a2, a3});
            ldsw(R + 8320 + 16 * g, (u32x4){a[1], a[2], a[3], b[0]});
            ldsw(R + 12480 + 16 * g, (u32x4){a1, a2, a3, a4});
        }
    }
    STAMP(5);
    waitc(L, C_U, 8);
    STAMP(6);
    f32x4 acc[8] = {};
    {
        const int b = lane & 15, kq = lane >> 4, c = (3 - b) & 3;
        const int pa0 = c * 4160 + 2 * (2031 - b + 8 * kq - c), pb0 = SCH_U + 480 + 32 * b + 16 * kq;
        __builtin_amdgcn_s_setprio(S_PRIOB);
        conv_group<0>(L, R, acc, pa0, pb0); conv_group<1>(L, R, acc, pa0, pb0); conv_group<2>(L, R, acc, pa0, pb0); conv_group<3>(L, R, acc, pa0, pb0);
        __builtin_amdgcn_s_setprio(3);
    }
    PIN(acc[0]); PIN(acc[7]); STAMP(7);
#pragma unroll
    for (int nu = 0; nu < 8; ++nu)
        ldsw(R + 512 * nu + 32 * (lane & 15) + 8 * (lane >> 4), (u32x2){cvtpk(acc[nu][0], acc[nu][1]), cvtpk(acc[nu][2], acc[nu][3])});
    arrive(L, C_S2, lane); waitc(L, C_S2, 4);
    {
        u32x4 yv[4], uv4[4];
#pragma unroll
        for (int c = 0; c < 4; ++c) {
            yv[c] = ldsr<u32x4>(L + L_S + c * SCH + 2 * t0);
            uv4[c] = ldsr<u32x4>(L + L_S + c * SCH + SCH_U + 512 + 2 * t0);
        }
        const float dk[4] = {bflo(dsk[0]), bfhi(dsk[0]), bflo(dsk[1]), bfhi(dsk[1])};
        u32x4 res[4];
#pragma unroll
        for (int c = 0; c < 4; ++c)
#pragma unroll
            for (int e = 0; e < 4; ++e) {
                const unsigned sk = cvtpk(bflo(uv4[c][e]) * dk[c], bfhi(uv4[c][e]) * dk[c]);
                res[c][e] = cvtpk(bflo(yv[c][e]) + bflo(sk), bfhi(yv[c][e]) + bfhi(sk));
            }
#pragma unroll
        for (int e = 0; e < 4; ++e) {
            unsigned short* op = p.out + (size_t)(t0 + 2 * e) * (2 * DM) + DM + ch0;
            *(u32x2*)op = (u32x2){__builtin_amdgcn_perm(res[1][e], res[0][e], 0x05040100u), __builtin_amdgcn_perm(res[3][e], res[2][e], 0x05040100u)};
            *(u32x2*)(op + 2 * DM) = (u32x2){__builtin_amdgcn_perm(res[1][e], res[0][e], 0x07060302u), __builtin_amdgcn_perm(res[3][e], res[2][e], 0x07060302u)};
        }
    }
    STAMP(8);

}

__global__ __launch_bounds__(768) void fused_kernel(Params p) {
    extern __shared__ __attribute__((aligned(16))) char smem[];
    lds_p L = (lds_p)smem;
    const unsigned lds0 = (unsigned)(uintptr_t)smem;
    const int tid = threadIdx.x, lane = tid & 63, w = __builtin_amdgcn_readfirstlane(tid >> 6);
    const int bid = blockIdx.x, xcd = bid & 7, idx = bid >> 3, tb = 2 * xcd + (idx & 1), pi = idx >> 1;
    if (tid < 64) ((LAS unsigned*)(L + L_C))[tid] = 0u;
    __syncthreads();
    STAMP_DECL;
    STAMP(0);
    const __amdgpu_buffer_rsrc_t rs_ws = __builtin_amdgcn_make_buffer_rsrc((void*)p.ws, 0, WS_BYTES, RSRC_W3);

    if (w >= 8) {
        if (idx >= 14 && !MK_NOSSM) ssm_wave(p, L, w - 8, lane, 72 * xcd + 4 * (idx - 14) STAMP_PASS);
        STAMP(1);
        STAMP_DUMP();
        return;
    }

    const unsigned tag = (unsigned)__builtin_amdgcn_readfirstlane(gload_sc1((const unsigned*)(p.ws + WS_EPOCH))) + 1u;
    const int c16 = lane & 15, kq = lane >> 4, r32 = lane & 31, h = lane >> 5;
    const int q4 = (lane >> 2) & 3, p4 = lane & 3;
    const int kh = w >> 2, wq4 = w & 3;

    bf16x8 qmA, qmB; unsigned qeA, qeB;
    {
        const int T0 = 128 * tb + 16 * w;
        const bool has_kv = pi < 5;
        const bool has_ssm = idx >= 14 && !MK_NOSSM;
        {
            const __amdgpu_buffer_rsrc_t rs_wq = __builtin_amdgcn_make_buffer_rsrc((void*)p.wq, 0, DM * DM * 2, RSRC_W3);
            int kl = (lane * 43) >> 8, g = lane - 6 * kl;
            const unsigned dst = lds0 + L_W + w * 6912;
#pragma unroll
            for (int i = 0; i < 7; ++i) {
                const int vo = swap23(72 * w + kl) * 1152 + 72 * pi + 16 * g;
                if (i < 6 || lane < 48) dma16(rs_wq, vo, dst + 1024 * i);
                g += 4; kl += 10; if (g >= 6) { g -= 6; kl += 1; }
            }
        }
        u32x2 kp0 = {}, vp0 = {}, kp1 = {}, vp1 = {};
        const int kr0 = 72 * w + lane, kr1 = 72 * w + 64 + (lane & 7);
        if (has_kv) {
            const __amdgpu_buffer_rsrc_t rs_wk = __builtin_amdgcn_make_buffer_rsrc((void*)p.wk, 0, DM * 18 * 2, RSRC_W3);
            const __amdgpu_buffer_rsrc_t rs_wv = __builtin_amdgcn_make_buffer_rsrc((void*)p.wv, 0, DM * 18 * 2, RSRC_W3);
            const int o0 = swap23(kr0) * 36 + 8 * pi, o1 = swap23(kr1) * 36 + 8 * pi;
            kp0 = __builtin_amdgcn_raw_buffer_load_b64(rs_wk, o0, 0, 0); vp0 = __builtin_amdgcn_raw_buffer_load_b64(rs_wv, o0, 0, 0);
            kp1 = __builtin_amdgcn_raw_buffer_load_b64(rs_wk, o1, 0, 0); vp1 = __builtin_amdgcn_raw_buffer_load_b64(rs_wv, o1, 0, 0);
        }
        SBAR();
        arrive(L, C_D, lane);
        waitc(L, C_D, 8);
        u32x4 xa[9], xb[9];
        {
            const unsigned short* xp = p.x + (size_t)(T0 + (lane >> 3)) * DM + 8 * (lane & 7);
#pragma unroll
            for (int s = 0; s < 9; ++s) { xa[s] = *(const u32x4*)(xp + 64 * s); xb[s] = *(const u32x4*)(xp + 64 * s + 8 * DM); }
        }
        SBAR();
        asm volatile("s_waitcnt vmcnt(18)" ::: "memory");
        SBAR();
        if (has_kv) {
            ldsw(L + L_W + kr0 * 96 + 80, (u32x4){kp0[0], kp0[1], vp0[0], vp0[1]});
            if (lane < 8) ldsw(L + L_W + kr1 * 96 + 80, (u32x4){kp1[0], kp1[1], vp1[0], vp1[1]});
        }
        arrive(L, C_W, lane);
        f32x4 D0 = {}, D1 = {}, D2 = {}, D2t = {};
        bf16x8 xf[18];
        lds_p wb = L + L_W + (16 * (kq >> 1) + 4 * (kq & 1) + q4) * 96 + 8 * p4;
        {
            lds_p xs = L + L_Q + w * 2048;
            lds_p xw = xs + (lane >> 3) * 128 + 16 * ((lane & 7) ^ (lane >> 4));
            lds_p xw2 = xs + 1024 + (lane >> 3) * 128 + 16 * ((lane & 7) ^ (4 + (lane >> 4)));
            lds_p xr0 = xs + c16 * 128 + 16 * (kq ^ ((c16 >> 1) & 7)), xr1 = xs + c16 * 128 + 16 * ((4 + kq) ^ ((c16 >> 1) & 7));
            ldsw(xw, xa[0]); ldsw(xw2, xb[0]); xf[0] = ldsr<bf16x8>(xr0); xf[1] = ldsr<bf16x8>(xr1);
            waitc(L, C_W, 8);
            STAMP(1);
#pragma unroll
            for (int s = 0; s < 9; ++s) {
                if (s < 8) { ldsw(xw, xa[s + 1]); ldsw(xw2, xb[s + 1]); xf[2 * s + 2] = ldsr<bf16x8>(xr0); xf[2 * s + 3] = ldsr<bf16x8>(xr1); }
#pragma unroll
                for (int e = 0; e < 2; ++e) {
                    const int ks = 2 * s + e;
                    const bf16x8 w2 = cat8(ldtr(wb + ks * 3072 + 64), ldtr(wb + ks * 3072 + 768 + 64));
                    D2 = MF16(w2, xf[ks], D2);
                    D2t = MF16(xf[ks], w2, D2t);
                }
                SBAR();
            }
        }
        STAMP(2);
        if (has_kv) {
            const int kt = 4 * tb + (w >> 1), rr = 16 * (w & 1) + c16, s16 = w & 1, tbase = WS_KV + kt * KVT;
            if (kq == 2) {
                u32x2 v = {cvtpk(D2[0], D2[1]), pi == 4 ? 0x3F803F80u : cvtpk(D2[2], D2[3])};
                const int off = pi == 4 ? 2304 + rr * 8 : ((pi >> 1) * 32 + rr) * 16 + (pi & 1) * 8;
                __builtin_amdgcn_raw_buffer_store_b64(v, rs_ws, tbase + off, 0, AUX_SC1);
            }
            if (c16 >= 12) {
                const int dv = 4 * pi + c16 - 12;
                u32x2 v = {cvtpk(D2t[0], D2t[1]), cvtpk(D2t[2], D2t[3])};
                if (dv == 18) v = (u32x2){0x3F803F80u, 0x3F803F80u};
                if (dv == 19) v = (u32x2){0u, 0u};
                __builtin_amdgcn_raw_buffer_store_b64(v, rs_ws, tbase + 1024 + ((s16 * 2 + (kq & 1)) * 20 + dv) * 16 + (kq >> 1) * 8, 0, AUX_SC1);
            }
        }
        if (has_kv) {
            DRAIN(); arrive(L, C_P, lane);
            if (w == 0) {
                waitc(L, C_P, 8);
                if (lane < 8) gstore_sc1((unsigned*)(p.ws + WS_FLAG1 + lane * 2048 + tb * 128 + pi * 4), tag);
            }
        }
        STAMP(8);
        SBAR();
#pragma unroll
        for (int ks = 0; ks < 18; ++ks) {
            const bf16x8 w0 = cat8(ldtr(wb + ks * 3072), ldtr(wb + ks * 3072 + 768));
            const bf16x8 w1 = cat8(ldtr(wb + ks * 3072 + 32), ldtr(wb + ks * 3072 + 768 + 32));
            D0 = MF16(w0, xf[ks], D0); D1 = MF16(w1, xf[ks], D1);
        }
        {
            lds_p qs = L + L_Q + w * 2048;
            ldsw(qs + c16 * 48 + 8 * kq, (u32x2){cvtpk(D0[0] * QSCALE, D0[1] * QSCALE), cvtpk(D0[2] * QSCALE, D0[3] * QSCALE)});
            const int a0 = kq == 0 ? c16 * 48 + 32 : (16 + c16) * 48 + 2 * (4 * kq - 2);
            const int a1 = (16 + c16) * 48 + 8 * kq;
            ldsw(qs + a0, cvtpk(D1[0] * QSCALE, D1[1] * QSCALE));
            ldsw(qs + a1, cvtpk(D1[2] * QSCALE, D1[3] * QSCALE));
            if (kq == 0) {
                ldsw(qs + (16 + c16) * 48 + 28, cvtpk(D2[0] * QSCALE, D2[1] * QSCALE));
                ldsw(qs + (16 + c16) * 48 + 32, cvtpk(D2[2] * QSCALE, D2[3] * QSCALE));
            }
        }
        arrive(L, C_Q, lane);
        if (has_ssm) {
            u32x2 uu[4];
#pragma unroll
            for (int e = 0; e < 4; ++e) uu[e] = *(const u32x2*)(p.u + (size_t)(256 * w + 4 * lane + e) * DM + 72 * xcd + 4 * (idx - 14));
            const int uo = SCH_U + 512 + 2 * (256 * w + 4 * lane);
#pragma unroll
            for (int c = 0; c < 4; ++c) {
                const unsigned sel = (c & 1) ? 0x07060302u : 0x05040100u;
                ldsw(L + L_S + c * SCH + uo, (u32x2){__builtin_amdgcn_perm(uu[1][c >> 1], uu[0][c >> 1], sel), __builtin_amdgcn_perm(uu[3][c >> 1], uu[2][c >> 1], sel)});
            }
            arrive(L, C_U, lane);
        }
        {
            if (w == 7) {
                const unsigned* f = (const unsigned*)(p.ws + WS_FLAG1 + xcd * 2048);
                const int i0 = lane, i1 = 64 + (lane & 15);
                const unsigned* f0 = f + (i0 / 5) * 32 + i0 % 5; const unsigned* f1 = f + (i1 / 5) * 32 + i1 % 5;
                for (unsigned spins = 0; spins < SPIN_GLB; ++spins) {
                    const unsigned a0 = gload_sc1(f0), a1 = gload_sc1(f1); const bool ok = (a0 == tag) & (a1 == tag);
                    if (__all(ok)) break;
                    __builtin_amdgcn_s_sleep(2);
                }
                __builtin_amdgcn_fence(__ATOMIC_ACQUIRE, "wavefront");
                arrive(L, C_F1, lane);
            }
        }
        waitc(L, C_Q, 8);
        {
            lds_p qa = L + L_Q + wq4 * 2048 + r32 * 48, qb = qa + 4 * 2048;
            qmA = ldsr<bf16x8>(qa + 16 * h); qmB = ldsr<bf16x8>(qb + 16 * h);
            qeA = ldsr<unsigned>(qa + 32); qeB = ldsr<unsigned>(qb + 32);
        }
        waitc(L, C_F1, 1);
        STAMP(3);
    }

    {
        lds_p ring = L + L_W + kh * (8 * KVT);
        const int dmo = WS_KV + (32 * kh + wq4) * KVT + lane * 16;
        const unsigned dmd = lds0 + L_W + kh * (8 * KVT) + wq4 * KVT;
#define KV_DMA_A(cc) do { dma16_sc1(rs_ws, dmo + (cc) * (4 * KVT), dmd + ((cc) & 1) * (4 * KVT)); dma16_sc1(rs_ws, dmo + (cc) * (4 * KVT) + 1024, dmd + ((cc) & 1) * (4 * KVT) + 1024); } while (0)
#define KV_DMA_B(cc) do { if (lane < 32) dma16_sc1(rs_ws, dmo + (cc) * (4 * KVT) + 2048, dmd + ((cc) & 1) * (4 * KVT) + 2048); } while (0)
        lds_p kb = ring + lane * 16, eb = ring + 2304 + r32 * 8, vb = ring + 1024 + (h * 20 + (r32 < 19 ? r32 : 19)) * 16;
        const f32x16 Z = {};
        unsigned mA = 0, mB = 0;
        f32x16 OA, OB;
        for (int pass = 0; pass < 2; ++pass) {
            KV_DMA_A(0); KV_DMA_B(0); KV_DMA_A(1); KV_DMA_B(1);
            asm volatile("s_waitcnt vmcnt(3)" ::: "memory");
            arrive(L, C_RW, lane); waitc(L, C_RW, 8 * (pass + 1));
            STAMP(9); CSTAMP(10);
            if (pass == 0) {
                const bf16x8 km = ldsr<bf16x8>(L + L_W + lane * 16); const bf16x4 ke = ldsr<bf16x4>(L + L_W + 2304 + r32 * 8);
                const u32x2 ea = {h ? 0u : qeA, 0u}, ebq = {h ? 0u : qeB, 0u};
                f32x16 sa = MF32(km, qmA, Z), sb = MF32(km, qmB, Z);
                sa = MF32E(ke, __builtin_bit_cast(bf16x4, ea), sa); sb = MF32E(ke, __builtin_bit_cast(bf16x4, ebq), sb);
                float ma = sa[0], mb = sb[0];
#pragma unroll
                for (int i = 1; i < 16; ++i) { ma = fmaxf(ma, sa[i]); mb = fmaxf(mb, sb[i]); }
                auto ra = __builtin_amdgcn_permlane32_swap(__float_as_uint(ma), __float_as_uint(ma), false, false);
                auto rb = __builtin_amdgcn_permlane32_swap(__float_as_uint(mb), __float_as_uint(mb), false, false);
                ma = fmaxf(__uint_as_float(ra[0]), __uint_as_float(ra[1])); mb = fmaxf(__uint_as_float(rb[0]), __uint_as_float(rb[1]));
                const float ah = bflo(cvtpk(ma, 0.f)), bh = bflo(cvtpk(mb, 0.f));
                mA = cvtpk(-ah, ah - ma); mB = cvtpk(-bh, bh - mb);
            }
            const u32x2 ea = {h ? 0u : qeA, h ? 0u : mA}, ebq = {h ? 0u : qeB, h ? 0u : mB};
            const bf16x4 qxA = __builtin_bit_cast(bf16x4, ea), qxB = __builtin_bit_cast(bf16x4, ebq);
            f32x16 SA, SB = Z; u32x4 pa0, pa1, pb0 = {}, pb1 = {}; bf16x8 km, vx0, vx1, vy0, vy1; bf16x4 ke;
            OA = Z; OB = Z;
            km = ldsr<bf16x8>(kb); ke = ldsr<bf16x4>(eb);
            SA = MF32(km, qmA, Z); SA = MF32E(ke, qxA, SA);
            vx0 = ldsr<bf16x8>(vb); vx1 = ldsr<bf16x8>(vb + 640);
#define PH(SC, SN, QM, QX, ON, pp0, pp1, pn0, pn1, V0, V1, LD1, LD2, LD4) do { \
            ON = MF32(V0, BC8(pp0), ON); SBAR(); \
            LD1; \
            SC[0] = EXP2(SC[0]); SC[1] = EXP2(SC[1]); SC[2] = EXP2(SC[2]); SC[3] = EXP2(SC[3]); SBAR(); \
            SN = MF32(km, QM, Z); SBAR(); \
            LD2; \
            SC[4] = EXP2(SC[4]); SC[5] = EXP2(SC[5]); pn0[0] = cvtpk(SC[0], SC[1]); SC[6] = EXP2(SC[6]); SC[7] = EXP2(SC[7]); pn0[1] = cvtpk(SC[2], SC[3]); SBAR(); \
            ON = MF32(V1, BC8(pp1), ON); SBAR(); \
            SC[8] = EXP2(SC[8]); SC[9] = EXP2(SC[9]); pn0[2] = cvtpk(SC[4], SC[5]); SC[10] = EXP2(SC[10]); SC[11] = EXP2(SC[11]); pn0[3] = cvtpk(SC[6], SC[7]); PIN(pn0); SBAR(); \
            SN = MF32E(ke, QX, SN); SBAR(); \
            LD4; \
            SC[12] = EXP2(SC[12]); SC[13] = EXP2(SC[13]); pn1[0] = cvtpk(SC[8], SC[9]); SC[14] = EXP2(SC[14]); SC[15] = EXP2(SC[15]); pn1[1] = cvtpk(SC[10], SC[11]); \
            pn1[2] = cvtpk(SC[12], SC[13]); pn1[3] = cvtpk(SC[14], SC[15]); PIN(pn1); SBAR(); \
        } while (0)
#define VRD(t, N0, N1) (N0 = ldsr<bf16x8>(vb + (t) * KVT), N1 = ldsr<bf16x8>(vb + (t) * KVT + 640))
#define KMRD(t) (km = ldsr<bf16x8>(kb + (((t) + 1) & 7) * KVT))
#define KERD(t) (ke = ldsr<bf16x4>(eb + (((t) + 1) & 7) * KVT))
#define PHA(P0, P1, LD1, LD2, LD4) PH(SA, SB, qmB, qxB, OB, pb0, pb1, pa0, pa1, P0, P1, LD1, LD2, LD4);
#define PHB(N0, N1, LD1, LD2, LD4) PH(SB, SA, qmA, qxA, OA, pa0, pa1, pb0, pb1, N0, N1, LD1, LD2, LD4);
#define TA(t) PHA(vx0, vx1, VRD(t, vy0, vy1), KMRD(t), KERD(t))
#define TB() PHB(vy0, vy1, (void)0, (void)0, (void)0)
#define UA(t) PHA(vy0, vy1, VRD(t, vx0, vx1), KMRD(t), KERD(t))
#define UB() PHB(vx0, vx1, (void)0, (void)0, (void)0)
#define PRIO(x) __builtin_amdgcn_s_setprio(x);
#define LAST(t, b) \
            PHA(vy0, vy1, VRD(t, vx0, vx1), (void)0, (void)0) \
            asm volatile("s_waitcnt vmcnt(0)" ::: "memory"); \
            if ((b) < 7) { if (it == 0 && pass == 0) { arrive(L, C_B, lane); waitc(L, C_B, 8 * ((b) + 1)); } else __builtin_amdgcn_s_barrier(); } \
            CBAR(); PRIO(3) KMRD(t); KERD(t); SBAR(); \
            PHB(vx0, vx1, (void)0, if ((b) + 2 < 8) KV_DMA_A((b) + 2), if ((b) + 2 < 8) KV_DMA_B((b) + 2))
            PRIO(3)
            for (int it = 0; it < 4; ++it) {
                TA(0) PRIO(2) TB() UA(1) PRIO(1) UB() TA(2) PRIO(0) TB()
                LAST(3, 2 * it)
                if (it == 1 && pass == 0) {
                    const __amdgpu_buffer_rsrc_t rs_wo = __builtin_amdgcn_make_buffer_rsrc((void*)p.wo, 0, DM * DM * 2, RSRC_W3);
                    int kp = 64 * w + lane; const int k9 = (kp * 7282) >> 16; int g = kp - 9 * k9; kp = k9;
                    for (int inst = w; inst < 81; inst += 8) {
                        dma16(rs_wo, swap23(kp) * 1152 + 144 * (pi >> 1) + 16 * g, lds0 + L_S + 1024 * inst);
                        g += 8; kp += 56; if (g >= 9) { g -= 9; kp += 1; }
                    }
                }
                TA(4) PRIO(2) TB() UA(5) PRIO(1) UB() TA(6) PRIO(0) TB()
                LAST(7, 2 * it + 1)
            }
            OB = MF32(vx0, BC8(pb0), OB); OB = MF32(vx1, BC8(pb1), OB);
#undef TA
#undef TB
#undef UA
#undef UB
#undef LAST
#undef PHA
#undef PHB
#undef VRD
#undef KMRD
#undef KERD
#undef PH
            PRIO(0)
            STAMP(4); CSTAMP(11);
            lds_p xo = (w < 4 ? L + L_X1 : L + L_Q) + wq4 * 3072, xi = (w < 4 ? L + L_Q : L + L_X1) + wq4 * 3072;
            {
                const f32x16& S = kh ? OA : OB;
#pragma unroll
                for (int g = 0; g < 3; ++g) ldsw(xo + g * 1024 + lane * 16, (f32x4){S[4 * g], S[4 * g + 1], S[4 * g + 2], S[4 * g + 3]});
            }
            BARRIER();
            float o[12];
#pragma unroll
            for (int g = 0; g < 3; ++g) {
                const f32x4 t = ldsr<f32x4>(xi + g * 1024 + lane * 16);
#pragma unroll
                for (int i = 0; i < 4; ++i) o[4 * g + i] = (kh ? OB[4 * g + i] : OA[4 * g + i]) + t[i];
            }
            const float lsum = __uint_as_float(__builtin_amdgcn_permlane32_swap(__float_as_uint(o[10]), __float_as_uint(o[10]), false, false)[0]);
            const float rl = __builtin_amdgcn_rcpf(lsum);
            bool bad = !(lsum < 1.2676506e30f);
#pragma unroll
            for (int i = 0; i < 8; ++i) bad |= !(fabsf(o[i]) < 3.0e38f);
            if (h == 0) bad |= !(fabsf(o[8]) < 3.0e38f) || !(fabsf(o[9]) < 3.0e38f);
            {
                lds_p os = xi;
                lds_p ob = os + (r32 & 15) * 72 + (r32 >> 4) * 36 + 8 * h;
                ldsw(ob, cvtpk(o[0] * rl, o[1] * rl)); ldsw(ob + 4, cvtpk(o[2] * rl, o[3] * rl));
                ldsw(ob + 16, cvtpk(o[4] * rl, o[5] * rl)); ldsw(ob + 20, cvtpk(o[6] * rl, o[7] * rl));
                if (h == 0) ldsw(ob + 32, cvtpk(o[8] * rl, o[9] * rl));
#pragma unroll
                for (int i = 0; i < 3; ++i) {
                    const int e = lane + 64 * i;
                    if (e < 144) {
                        const int tk = e & 15, pc = e >> 4, k0 = 36 * pi + 4 * pc;
                        const u32x2 v = ldsr<u32x2>(os + tk * 72 + pc * 8);
                        __builtin_amdgcn_raw_buffer_store_b64(v, rs_ws, WS_O + (8 * tb + w) * 18432 + (k0 >> 3) * 256 + tk * 16 + (k0 & 4) * 2, 0, AUX_SC1);
                    }
                }
            }
            if (pass == 0 && __any(bad) && lane == 0) ((volatile LAS unsigned*)(L + L_C))[C_BAD] = 1u;
            DRAIN();
            BARRIER();
            const bool redo = pass == 0 && __builtin_amdgcn_readfirstlane(((volatile LAS unsigned*)(L + L_C))[C_BAD]) != 0;
            if (!redo) break;
            {
                const u32x2 ea0 = {h ? 0u : qeA, 0u}, eb0 = {h ? 0u : qeB, 0u};
                float ma = -INFINITY, mb = -INFINITY;
                for (int t = 0; t < 64; ++t) {
                    const u32x4 kmv = __builtin_amdgcn_raw_buffer_load_b128(rs_ws, WS_KV + t * KVT + lane * 16, 0, AUX_SC1);
                    const u32x2 kev = __builtin_amdgcn_raw_buffer_load_b64(rs_ws, WS_KV + t * KVT + 2304 + r32 * 8, 0, AUX_SC1);
                    f32x16 sa = MF32(BC8(kmv), qmA, Z), sb = MF32(BC8(kmv), qmB, Z);
                    sa = MF32E(__builtin_bit_cast(bf16x4, kev), __builtin_bit_cast(bf16x4, ea0), sa);
                    sb = MF32E(__builtin_bit_cast(bf16x4, kev), __builtin_bit_cast(bf16x4, eb0), sb);
#pragma unroll
                    for (int i = 0; i < 16; ++i) { ma = fmaxf(ma, sa[i]); mb = fmaxf(mb, sb[i]); }
                }
                ma = fmaxf(ma, __shfl_xor(ma, 32)); mb = fmaxf(mb, __shfl_xor(mb, 32));
                const float ah = bflo(cvtpk(ma, 0.f)), bh = bflo(cvtpk(mb, 0.f));
                mA = cvtpk(-ah, ah - ma); mB = cvtpk(-bh, bh - mb);
            }
        }
#undef KV_DMA_A
#undef KV_DMA_B
#undef PRIO
        if (w == 0 && lane == 0) gstore_sc1((unsigned*)(p.ws + WS_FLAG2 + tb * 128 + pi * 4), tag);
        STAMP(5);
    }

    {
        if (w == 7) {
            const unsigned* f = (const unsigned*)(p.ws + WS_FLAG2 + tb * 128) + (lane & 15);
            for (unsigned spins = 0; spins < SPIN_GLB; ++spins) {
                if (__all(gload_sc1(f) == tag)) break;
                __builtin_amdgcn_s_sleep(1);
            }
            __builtin_amdgcn_fence(__ATOMIC_ACQUIRE, "wavefront");
        }
        BARRIER();
        STAMP(6);
        const int Tt = 128 * tb + 64 * (pi & 1) + 16 * wq4, F0 = 72 * (pi >> 1);
        u32x4 of[9];
#pragma unroll
        for (int i = 0; i < 9; ++i) of[i] = __builtin_amdgcn_raw_buffer_load_b128(rs_ws, WS_O + (Tt >> 4) * 18432 + (9 * kh + i) * 1024 + lane * 16, 0, AUX_SC1);
        f32x4 D[5] = {};
        lds_p wb = L + L_S + (16 * (kq >> 1) + 4 * (kq & 1) + q4) * 144 + 8 * p4 + kh * (9 * 32 * 144);
#pragma unroll
        for (int i = 0; i < 9; ++i)
#pragma unroll
            for (int T = 0; T < 5; ++T)
                D[T] = MF16(cat8(ldtr(wb + i * 4608 + 32 * T), ldtr(wb + i * 4608 + 1152 + 32 * T)), BC8(of[i]), D[T]);
        lds_p xo = L + L_W + w * 3072, xi = L + L_W + (w ^ 4) * 3072;
        if (kh == 0) { ldsw(xo + lane * 16, D[3]); ldsw(xo + 1024 + lane * 16, D[4]); }
        else { ldsw(xo + lane * 16, D[0]); ldsw(xo + 1024 + lane * 16, D[1]); ldsw(xo + 2048 + lane * 16, D[2]); }
        BARRIER();
        unsigned short* op = p.out + (size_t)(Tt + c16) * (2 * DM) + F0 + 4 * kq;
        if (kh == 0) {
#pragma unroll
            for (int T = 0; T < 3; ++T) {
                const f32x4 v = D[T] + ldsr<f32x4>(xi + T * 1024 + lane * 16);
                *(u32x2*)(op + 16 * T) = (u32x2){cvtpk(v[0], v[1]), cvtpk(v[2], v[3])};
            }
        } else {
#pragma unroll
            for (int T = 3; T < 5; ++T) {
                const f32x4 v = D[T] + ldsr<f32x4>(xi + (T - 3) * 1024 + lane * 16);
                if (T == 3 || kq < 2) *(u32x2*)(op + 16 * T) = (u32x2){cvtpk(v[0], v[1]), cvtpk(v[2], v[3])};
            }
        }
        STAMP(7);
        STAMP_DUMP();
        if (bid == 0 && w == 7) {
            const unsigned* f = (const unsigned*)(p.ws + WS_FLAG2);
            for (unsigned spins = 0; spins < SPIN_GLB; ++spins) {
                bool ok = true;
#pragma unroll
                for (int i = 0; i < 4; ++i) { const int e = lane + 64 * i; ok &= gload_sc1(f + (e >> 4) * 32 + (e & 15)) == tag; }
                if (__all(ok)) break;
                __builtin_amdgcn_s_sleep(2);
            }
            if (lane == 0) gstore_sc1((unsigned*)(p.ws + WS_EPOCH), tag);
        }
    }
}

}

extern "C" void kernel_launch(void* const* d_in, const int* in_sizes, int n_in, void* d_out, int out_size, void* d_ws, size_t ws_size,
                              hipStream_t stream) {
    Params p;
    p.x = (const unsigned short*)d_in[0]; p.wq = (const unsigned short*)d_in[1]; p.wk = (const unsigned short*)d_in[2];
    p.wv = (const unsigned short*)d_in[3]; p.wo = (const unsigned short*)d_in[4]; p.u = (const unsigned short*)d_in[5];
    p.are = (const unsigned short*)d_in[6]; p.aim = (const unsigned short*)d_in[7]; p.bre = (const unsigned short*)d_in[8];
    p.bim = (const unsigned short*)d_in[9]; p.cre = (const unsigned short*)d_in[10]; p.cim = (const unsigned short*)d_in[11];
    p.ldt = (const unsigned short*)d_in[12]; p.dsk = (const unsigned short*)d_in[13];
    p.out = (unsigned short*)d_out; p.ws = (char*)d_ws;
    (void)hipFuncSetAttribute((const void*)fused_kernel, hipFuncAttributeMaxDynamicSharedMemorySize, L_END);
    fused_kernel<<<256, 768, L_END, stream>>>(p);
}
```

```cpp
#include <hip/hip_runtime.h>
#include <hip/hip_bf16.h>
#include <stdint.h>

#define U_BY 3
#define S_PRIO0 0
#define S_PRIOB 0
#define S_PRIOC 3
#define MK_TOUCH 1
#define MK_NOSSM 0
#define MK_STAMPS 0

namespace {

typedef __attribute__((ext_vector_type(16))) float f32x16;
typedef __attribute__((ext_vector_type(4))) float f32x4;
typedef __attribute__((ext_vector_type(2))) float f32x2;
typedef __attribute__((ext_vector_type(8))) short bf16x8;
typedef __attribute__((ext_vector_type(4))) short bf16x4;
typedef __attribute__((ext_vector_type(2))) __bf16 bf16x2_t;
typedef __attribute__((ext_vector_type(4))) unsigned u32x4;
typedef __attribute__((ext_vector_type(2))) unsigned u32x2;
typedef short v4i16_t __attribute__((ext_vector_type(4)));
#define LAS __attribute__((address_space(3)))
#define GAS __attribute__((address_space(1)))
typedef LAS char* lds_p;
typedef const unsigned short* cbf;

constexpr int SEQ = 2048, DM = 576;
constexpr float QSCALE = 0.34005040627f;
constexpr float LOG2E = 1.4426950408889634f;
constexpr float INV2PI = 0.15915494309189535f;

constexpr int WS_EPOCH = 0;
constexpr int WS_FLAG1 = 4096;
constexpr int WS_FLAG2 = WS_FLAG1 + 16384;
constexpr int WS_KV = 32768;
constexpr int KVT = 2560;
constexpr int WS_O = WS_KV + 64 * KVT;
constexpr int WS_BYTES = WS_O + SEQ * DM * 2;
constexpr int WS_STAMP = 8 * 1024 * 1024;

constexpr int L_W = 0;
constexpr int L_X1 = 40960;
constexpr int L_S = 55296;
constexpr int L_Q = L_S + 87360;
constexpr int L_C = L_Q + 16384;
constexpr int L_END = L_C + 256;
constexpr int SCH = 21840;
constexpr int SCH_U = 16640;
enum { C_W = 0, C_P, C_Q, C_F1, C_RW, C_B, C_BAD, C_U, C_S2, C_D, C_NUM };

constexpr int RSRC_W3 = 0x00020000;
constexpr int AUX_SC1 = 16;
constexpr unsigned SPIN_LDS = 1u << 20, SPIN_GLB = 1u << 16;

#define SBAR() __builtin_amdgcn_sched_barrier(0)
#define PIN(x) asm volatile("" : "+v"(x))
#define CBAR() asm volatile("" ::: "memory")
#define DRAIN() asm volatile("s_waitcnt vmcnt(0)" ::: "memory")
#define MF32(a, b, c) __builtin_amdgcn_mfma_f32_32x32x16_bf16(a, b, c, 0, 0, 0)
#define MF32E(a, b, c) __builtin_amdgcn_mfma_f32_32x32x8bf16_1k(a, b, c, 0, 0, 0)
#define MF16(a, b, c) __builtin_amdgcn_mfma_f32_16x16x32_bf16(a, b, c, 0, 0, 0)
#define EXP2(x) __builtin_amdgcn_exp2f(x)
#define BC8(x) __builtin_bit_cast(bf16x8, x)
#define DI __device__ __forceinline__

DI unsigned cvtpk(float lo, float hi) { f32x2 v = {lo, hi}; return __builtin_bit_cast(unsigned, __builtin_convertvector(v, bf16x2_t)); }
DI float bflo(unsigned x) { return __builtin_bit_cast(float, x << 16); }
DI float bfhi(unsigned x) { return __builtin_bit_cast(float, x & 0xffff0000u); }
DI float bf1(unsigned short x) { return __builtin_bit_cast(float, (unsigned)x << 16); }
template <class T> DI T ldsr(lds_p p) { return *(const LAS T*)p; }
template <class T> DI void ldsw(lds_p p, T v) { *(LAS T*)p = v; }
DI bf16x4 ldtr(lds_p p) { return __builtin_bit_cast(bf16x4, __builtin_amdgcn_ds_read_tr16_b64_v4i16((LAS v4i16_t*)p)); }
DI bf16x8 cat8(bf16x4 a, bf16x4 b) { return __builtin_shufflevector(a, b, 0, 1, 2, 3, 4, 5, 6, 7); }
DI int swap23(int k) { return (k & ~12) | ((k & 4) << 1) | ((k & 8) >> 1); }
DI void dma16(__amdgpu_buffer_rsrc_t rs, int voff, unsigned lds_base) {
    unsigned keep;
    asm volatile("s_mov_b32 %0, m0\n\ts_mov_b32 m0, %3\n\ts_nop 0\n\tbuffer_load_dwordx4 %1, %2, 0 offen lds\n\ts_mov_b32 m0, %0"
                 : "=&s"(keep) : "v"(voff), "s"(rs), "s"(lds_base) : "memory");
}
DI void dma16_sc1(__amdgpu_buffer_rsrc_t rs, int voff, unsigned lds_base) {
    unsigned keep;
    asm volatile("s_mov_b32 %0, m0\n\ts_mov_b32 m0, %3\n\ts_nop 0\n\tbuffer_load_dwordx4 %1, %2, 0 offen sc1 lds\n\ts_mov_b32 m0, %0"
                 : "=&s"(keep) : "v"(voff), "s"(rs), "s"(lds_base) : "memory");
}
#define BARRIER() asm volatile("s_waitcnt lgkmcnt(0)\n\ts_barrier" ::: "memory")
DI void arrive(lds_p L, int c, int lane) {
    CBAR();
    if (lane == 0) (void)__hip_atomic_fetch_add((LAS unsigned*)(L + L_C) + c, 1u, __ATOMIC_RELAXED, __HIP_MEMORY_SCOPE_WORKGROUP);
    CBAR();
}
DI void waitc(lds_p L, int c, unsigned target) {
    CBAR();
    volatile LAS unsigned* p = (volatile LAS unsigned*)(L + L_C) + c;
    for (unsigned spins = 0; spins < SPIN_LDS; ++spins) {
        if ((unsigned)__builtin_amdgcn_readfirstlane(*p) >= target) break;
        __builtin_amdgcn_s_sleep(1);
    }
    CBAR();
}
DI unsigned peek(lds_p L, int c) { return *((volatile LAS unsigned*)(L + L_C) + c); }
DI void waitp(lds_p L, int c, unsigned seen, unsigned target) { if ((unsigned)__builtin_amdgcn_readfirstlane(seen) < target) waitc(L, c, target); CBAR(); }
DI unsigned gload_sc1(const unsigned* p) { return __hip_atomic_load((GAS unsigned*)p, __ATOMIC_RELAXED, __HIP_MEMORY_SCOPE_AGENT); }
DI void gstore_sc1(unsigned* p, unsigned v) { __hip_atomic_store((GAS unsigned*)p, v, __ATOMIC_RELAXED, __HIP_MEMORY_SCOPE_AGENT); }

struct cplx2 { f32x2 re, im; };
DI cplx2 cmul(const cplx2& a, const cplx2& b) { cplx2 r; r.re = a.re * b.re - a.im * b.im; r.im = a.re * b.im + a.im * b.re; return r; }
DI cplx2 cexp(f32x2 ar2, f32x2 airev, float e) {
    f32x2 m = ar2 * e, t = airev * e; cplx2 r;
#pragma unroll
    for (int i = 0; i < 2; ++i) {
        const float mag = EXP2(m[i]), fr = __builtin_amdgcn_fractf(t[i]);
        r.re[i] = mag * __builtin_amdgcn_cosf(fr); r.im[i] = mag * __builtin_amdgcn_sinf(fr);
    }
    return r;
}

struct Params {
    const unsigned short *x, *wq, *wk, *wv, *wo, *u, *are, *aim, *bre, *bim, *cre, *cim, *ldt, *dsk;
    unsigned short* out;
    char* ws;
};

#define STAMP(i) do {} while (0)
#define CSTAMP(i) do {} while (0)
#define STAMP_DECL do {} while (0)
#define STAMP_DUMP() do {} while (0)
#define STAMP_ARG
#define STAMP_PASS

template <int G> DI void conv_group(lds_p L, lds_p R, f32x4 (&acc)[8], int pa0, int pb0) {
    if ((unsigned)__builtin_amdgcn_readfirstlane(peek(L, C_Q)) >= 8u) __builtin_amdgcn_s_setprio(S_PRIOC);
    lds_p a1 = R + pa0 - 64 * (8 * G), b1 = R + pb0 - 64 * (8 * G), a2 = R + pa0 - 64 * (63 - 8 * G), b2 = R + pb0 - 64 * (63 - 8 * G);
    PIN(a1); PIN(b1); PIN(a2); PIN(b2);
#define RDA(ptr) cat8(*(const volatile LAS bf16x4*)(ptr), *(const volatile LAS bf16x4*)((ptr) + 8))
    bf16x8 P1 = RDA(a1), P2 = RDA(a2), Q1, Q2, B1[8 - G], B2[G + 1];
#pragma unroll
    for (int n = 0; n < 8 - G; ++n) B1[n] = ldsr<bf16x8>(b1 + 512 * (G + n));
#pragma unroll
    for (int n = 0; n < G + 1; ++n) B2[n] = ldsr<bf16x8>(b2 + 512 * (7 - G + n));
#define CONV_STEP(C1, C2, N1, N2) do { \
        a1 -= 64; b1 -= 64; a2 += 64; b2 += 64; PIN(a1); PIN(b1); PIN(a2); PIN(b2); \
        N1 = RDA(a1); N2 = RDA(a2); SBAR(); \
        _Pragma("unroll") for (int n = 0; n < 8 - G; ++n) { acc[G + n] = MF16(C1, B1[n], acc[G + n]); SBAR(); B1[n] = ldsr<bf16x8>(b1 + 512 * (G + n)); SBAR(); } \
        _Pragma("unroll") for (int n = 0; n < G + 1; ++n) { acc[7 - G + n] = MF16(C2, B2[n], acc[7 - G + n]); SBAR(); B2[n] = ldsr<bf16x8>(b2 + 512 * (7 - G + n)); SBAR(); } \
    } while (0)
#pragma unroll 1
    for (int i = 0; i < 4; ++i) { CONV_STEP(P1, P2, Q1, Q2); CONV_STEP(Q1, Q2, P1, P2); }
#undef CONV_STEP
#undef RDA
}

DI void ssm_wave(const Params& p, lds_p L, int s, int lane, int ch0 STAMP_ARG) {
    const int d = ch0 + s; const int w = 8 + s; (void)w;
    __builtin_amdgcn_s_setprio(S_PRIO0);
    lds_p R = L + L_S + s * SCH;
    const int pb = d * 128 + lane;
    const f32x2 are = {bf1(p.are[pb]), bf1(p.are[pb + 64])}, aim = {bf1(p.aim[pb]), bf1(p.aim[pb + 64])};
    const f32x2 bre = {bf1(p.bre[pb]), bf1(p.bre[pb + 64])}, bim = {bf1(p.bim[pb]), bf1(p.bim[pb + 64])};
    const f32x2 cre = {bf1(p.cre[pb]), bf1(p.cre[pb + 64])}, cim = {bf1(p.cim[pb]), bf1(p.cim[pb + 64])};
    const float ldt = bf1(p.ldt[d]);
    const u32x2 dsk = *(const u32x2*)(p.dsk + ch0);
    const int t0 = 512 * s + 8 * lane;
    const float dt = bflo(cvtpk(expf(ldt), 0.f));
    const f32x2 ar2 = (are * dt) * LOG2E, airev = (aim * dt) * INV2PI;
    cplx2 dB; dB.re = bre * dt; dB.im = bim * dt;
    cplx2 C; C.re = cre; C.im = cim;
    const cplx2 CB = cmul(C, dB);
    const cplx2 w1 = cexp(ar2, airev, 1.f), w8 = cexp(ar2, airev, 8.f), w128 = cexp(ar2, airev, 128.f);

    STAMP(3);
    lds_p st0 = R + lane * 16, st1 = R + 4224 + lane * 16;
    const int kq = lane >> 4, q4 = (lane >> 2) & 3, p4 = lane & 3;
    lds_p trb = R + (p4 >> 1) * 4224 + (16 * (kq >> 1) + 4 * (kq & 1) + q4) * 16 + (p4 & 1) * 8;
#define PUT16(V, NEG) do { \
        u32x4 a0, a1, b0, b1, c0, c1, d0, d1; \
        _Pragma("unroll") for (int j = 0; j < 4; ++j) { \
            a0[j] = cvtpk(V[2 * j].re[0], V[2 * j + 1].re[0]); a1[j] = cvtpk(V[8 + 2 * j].re[0], V[9 + 2 * j].re[0]); \
            b0[j] = cvtpk(V[2 * j].re[1], V[2 * j + 1].re[1]); b1[j] = cvtpk(V[8 + 2 * j].re[1], V[9 + 2 * j].re[1]); \
            c0[j] = cvtpk(NEG V[2 * j].im[0], NEG V[2 * j + 1].im[0]); c1[j] = cvtpk(NEG V[8 + 2 * j].im[0], NEG V[9 + 2 * j].im[0]); \
            d0[j] = cvtpk(NEG V[2 * j].im[1], NEG V[2 * j + 1].im[1]); d1[j] = cvtpk(NEG V[8 + 2 * j].im[1], NEG V[9 + 2 * j].im[1]); } \
        ldsw(st0, a0); ldsw(st1, a1); ldsw(st0 + 1024, b0); ldsw(st1 + 1024, b1); \
        ldsw(st0 + 2048, c0); ldsw(st1 + 2048, c1); ldsw(st0 + 3072, d0); ldsw(st1 + 3072, d1); } while (0)
#define GETF(ks) cat8(ldtr(trb + (ks) * 512), ldtr(trb + (ks) * 512 + 128))
    bf16x8 yf[8];
    {
        cplx2 V[16];
        V[0].re = (f32x2){1.f, 1.f}; V[0].im = (f32x2){0.f, 0.f}; V[1].re = w8.re; V[1].im = -w8.im;
        V[2] = cmul(V[1], V[1]); V[3] = cmul(V[2], V[1]); V[4] = cmul(V[2], V[2]);
        V[5] = cmul(V[4], V[1]); V[6] = cmul(V[4], V[2]); V[7] = cmul(V[4], V[3]); V[8] = cmul(V[4], V[4]);
#pragma unroll
        for (int j = 9; j < 16; ++j) V[j] = cmul(V[8], V[j - 8]);
        PUT16(V, +);
#pragma unroll
        for (int ks = 0; ks < 8; ++ks) yf[ks] = GETF(ks);
    }
    STAMP(9);
    const cplx2 w2 = cmul(w1, w1), w3 = cmul(w2, w1), w4 = cmul(w2, w2), w5 = cmul(w4, w1), w6 = cmul(w4, w2), w7 = cmul(w4, w3);
    f32x4 D0 = {}, D1 = {}, D2 = {};
    cplx2 G = cmul(CB, w128);
    {
        cplx2 V[16];
        V[0] = CB; V[1] = cmul(CB, w1); V[2] = cmul(CB, w2); V[3] = cmul(CB, w3); V[4] = cmul(CB, w4); V[5] = cmul(CB, w5); V[6] = cmul(CB, w6); V[7] = cmul(CB, w7);
        V[8] = G; V[9] = cmul(G, w1); V[10] = cmul(G, w2); V[11] = cmul(G, w3); V[12] = cmul(G, w4); V[13] = cmul(G, w5); V[14] = cmul(G, w6); V[15] = cmul(G, w7);
        PUT16(V, +);
        bf16x8 xf[8];
#pragma unroll
        for (int ks = 0; ks < 8; ++ks) xf[ks] = GETF(ks);
        SBAR();
#pragma unroll
        for (int ks = 0; ks < 8; ++ks) D0 = MF16(xf[ks], yf[ks], D0);
    }
    PIN(D0); STAMP(10);
    {
        cplx2 V[16];
        G = cmul(G, w128); V[0] = G; V[1] = cmul(G, w2); V[2] = cmul(G, w4); V[3] = cmul(G, w6);
        G = cmul(G, w128); V[4] = G; V[5] = cmul(G, w2); V[6] = cmul(G, w4); V[7] = cmul(G, w6);
#pragma unroll
        for (int i = 0; i < 4; ++i) { G = cmul(G, w128); V[8 + 2 * i] = G; V[9 + 2 * i] = cmul(G, w4); }
        PUT16(V, +);
        bf16x8 xf[8];
#pragma unroll
        for (int ks = 0; ks < 8; ++ks) xf[ks] = GETF(ks);
        SBAR();
#pragma unroll
        for (int ks = 0; ks < 8; ++ks) D1 = MF16(xf[ks], yf[ks], D1);
    }
    {
        cplx2 V[16];
#pragma unroll
        for (int i = 0; i < 9; ++i) { G = cmul(G, w128); V[i] = G; }
#pragma unroll
        for (int i = 9; i < 16; ++i) V[i] = G;
        PUT16(V, +);
        bf16x8 xf[8];
#pragma unroll
        for (int ks = 0; ks < 8; ++ks) xf[ks] = GETF(ks);
        SBAR();
#pragma unroll
        for (int ks = 0; ks < 8; ++ks) D2 = MF16(yf[ks], xf[ks], D2);
    }
#undef PUT16
#undef GETF
    PIN(D2); STAMP(4);
    lds_p tab = R + 8448;
    {
        const int j = lane & 15, rq = lane >> 4;
        ldsw(tab + 2 * (8 * j + 4 * (rq & 1) + 128 * (rq >> 1)), (u32x2){cvtpk(D0[0], D0[1]), cvtpk(D0[2], D0[3])});
        const int e0 = rq < 2 ? 256 + 64 * rq + 4 * j : 384 + 64 * (rq - 2) + 2 * j, e1 = rq < 2 ? e0 + 2 : e0 + 32;
        ldsw(tab + 2 * e0, cvtpk(D1[0], D1[1])); ldsw(tab + 2 * e1, cvtpk(D1[2], D1[3]));
        if (j < 8 || lane == 8) ldsw(tab + 2 * (512 + 16 * j + 4 * rq), (u32x2){cvtpk(D2[0], D2[1]), cvtpk(D2[2], D2[3])});
    }
#define PK(hi_src, hi_half, lo_src, lo_half) __builtin_amdgcn_perm(hi_src, lo_src, ((hi_half) ? 0x07060000u : 0x05040000u) | ((lo_half) ? 0x0302u : 0x0100u))
    {
#pragma unroll
        for (int i = 0; i < 2; ++i) {
            const int g = lane + 64 * i, m = 255 - g;
            const unsigned lo = ldsr<unsigned short>(tab + 2 * (384 + m)), hi = ldsr<unsigned short>(tab + 2 * (385 + m));
            const unsigned mid = (m & 1) ? hi : lo, ll = PK(lo, 0, lo, 0);
            ldsw(R + 16 * g, (u32x4){PK(hi, 0, hi, 0), PK(mid, 0, hi, 0), ll, ll});
        }
        {
            const int g = 128 + lane, m = 255 - g;
            const unsigned ab = ldsr<unsigned>(tab + 2 * (256 + 2 * m)), c = ldsr<unsigned short>(tab + 2 * (258 + 2 * m));
            ldsw(R + 16 * g, (u32x4){PK(c, 0, c, 0), PK(ab, 1, ab, 1), PK(ab, 0, ab, 1), PK(ab, 0, ab, 0)});
        }
        {
            const int g = 192 + lane, m = 255 - g;
            u32x4 v;
            if (lane < 32) {
                const u32x2 q = ldsr<u32x2>(tab + 2 * (128 + 4 * m)); const unsigned e = ldsr<unsigned short>(tab + 2 * (132 + 4 * m));
                v = (u32x4){PK(q[1], 1, e, 0), PK(q[1], 0, q[1], 0), PK(q[0], 1, q[1], 0), PK(q[0], 0, q[0], 0)};
            } else {
                const u32x4 q = ldsr<u32x4>(tab + 16 * m);
                v = (u32x4){PK(q[3], 0, q[3], 1), PK(q[2], 0, q[2], 1), PK(q[1], 0, q[1], 1), PK(q[0], 0, q[0], 1)};
            }
            ldsw(R + 16 * g, v);
        }
        if (lane < 4) ldsw(R + 16 * (256 + lane), (u32x4){0u, 0u, 0u, 0u});
    }
#undef PK
    if (lane < 32) ldsw(R + SCH_U + 16 * lane, (u32x4){0u, 0u, 0u, 0u});
#pragma unroll
    for (int i = 0; i < 5; ++i) {
        const int g = lane + 64 * i;
        if (i < 4 || lane < 4) {
            const u32x4 a = ldsr<u32x4>(R + 16 * g); const u32x2 b = ldsr<u32x2>(R + 16 * g + 16);
            const unsigned a0 = __builtin_amdgcn_alignbit(a[1], a[0], 16), a1 = __builtin_amdgcn_alignbit(a[2], a[1], 16), a2 = __builtin_amdgcn_alignbit(a[3], a[2], 16),
                           a3 = __builtin_amdgcn_alignbit(b[0], a[3], 16), a4 = __builtin_amdgcn_alignbit(b[1], b[0], 16);
            ldsw(R + 4160 + 16 * g, (u32x4){a0, a1, a2, a3});
            ldsw(R + 8320 + 16 * g, (u32x4){a[1], a[2], a[3], b[0]});
            ldsw(R + 12480 + 16 * g, (u32x4){a1, a2, a3, a4});
        }
    }
    STAMP(5);
    waitc(L, C_U, 8);
    STAMP(6);
    f32x4 acc[8] = {};
    {
        const int b = lane & 15, kq = lane >> 4, c = (3 - b) & 3;
        const int pa0 = c * 4160 + 2 * (2031 - b + 8 * kq - c), pb0 = SCH_U + 480 + 32 * b + 16 * kq;
        __builtin_amdgcn_s_setprio(S_PRIOB);
        conv_group<0>(L, R, acc, pa0, pb0); conv_group<1>(L, R, acc, pa0, pb0); conv_group<2>(L, R, acc, pa0, pb0); conv_group<3>(L, R, acc, pa0, pb0);
        __builtin_amdgcn_s_setprio(3);
    }
    PIN(acc[0]); PIN(acc[7]); STAMP(7);
#pragma unroll
    for (int nu = 0; nu < 8; ++nu)
        ldsw(R + 512 * nu + 32 * (lane & 15) + 8 * (lane >> 4), (u32x2){cvtpk(acc[nu][0], acc[nu][1]), cvtpk(acc[nu][2], acc[nu][3])});
    arrive(L, C_S2, lane); waitc(L, C_S2, 4);
    {
        u32x4 yv[4], uv4[4];
#pragma unroll
        for (int c = 0; c < 4; ++c) {
            yv[c] = ldsr<u32x4>(L + L_S + c * SCH + 2 * t0);
            uv4[c] = ldsr<u32x4>(L + L_S + c * SCH + SCH_U + 512 + 2 * t0);
        }
        const float dk[4] = {bflo(dsk[0]), bfhi(dsk[0]), bflo(dsk[1]), bfhi(dsk[1])};
        u32x4 res[4];
#pragma unroll
        for (int c = 0; c < 4; ++c)
#pragma unroll
            for (int e = 0; e < 4; ++e) {
                const unsigned sk = cvtpk(bflo(uv4[c][e]) * dk[c], bfhi(uv4[c][e]) * dk[c]);
                res[c][e] = cvtpk(bflo(yv[c][e]) + bflo(sk), bfhi(yv[c][e]) + bfhi(sk));
            }
#pragma unroll
        for (int e = 0; e < 4; ++e) {
            unsigned short* op = p.out + (size_t)(t0 + 2 * e) * (2 * DM) + DM + ch0;
            *(u32x2*)op = (u32x2){__builtin_amdgcn_perm(res[1][e], res[0][e], 0x05040100u), __builtin_amdgcn_perm(res[3][e], res[2][e], 0x05040100u)};
            *(u32x2*)(op + 2 * DM) = (u32x2){__builtin_amdgcn_perm(res[1][e], res[0][e], 0x07060302u), __builtin_amdgcn_perm(res[3][e], res[2][e], 0x07060302u)};
        }
    }
    STAMP(8);

}

__global__ __launch_bounds__(768) void fused_kernel(Params p) {
    extern __shared__ __attribute__((aligned(16))) char smem[];
    lds_p L = (lds_p)smem;
    const unsigned lds0 = (unsigned)(uintptr_t)smem;
    const int tid = threadIdx.x, lane = tid & 63, w = __builtin_amdgcn_readfirstlane(tid >> 6);
    const int bid = blockIdx.x, xcd = bid & 7, idx = bid >> 3, tb = 2 * xcd + (idx & 1), pi = idx >> 1;
    if (tid < 64) ((LAS unsigned*)(L + L_C))[tid] = 0u;
    __syncthreads();
    STAMP_DECL;
    STAMP(0);
    const __amdgpu_buffer_rsrc_t rs_ws = __builtin_amdgcn_make_buffer_rsrc((void*)p.ws, 0, WS_BYTES, RSRC_W3);

    if (w >= 8) {
        if (w == 11 && idx < 14) {
            const char* wsb = p.ws; const char* ob = (const char*)p.out; const char* wob = (const char*)p.wo;
            unsigned a = 0;
            if (lane < 40) a ^= *(const volatile unsigned*)(wsb + WS_KV + 4096 * lane);
            if (lane < 36) a ^= *(const volatile unsigned*)(wsb + WS_O + tb * 147456 + 4096 * lane);
            if (lane < 36) a ^= *(const volatile unsigned*)(ob + (size_t)(128 * tb + 64 * (pi & 1)) * 2304 + 4096 * lane);
#pragma unroll
            for (int i = 0; i < 3; ++i) if (lane + 64 * i < 162) a ^= *(const volatile unsigned*)(wob + 4096 * (lane + 64 * i));
            asm volatile("" :: "v"(a));
        }
        if (idx >= 14 && !MK_NOSSM) ssm_wave(p, L, w - 8, lane, 72 * xcd + 4 * (idx - 14) STAMP_PASS);
        STAMP(1);
        STAMP_DUMP();
        return;
    }

    const unsigned tag = (unsigned)__builtin_amdgcn_readfirstlane(gload_sc1((const unsigned*)(p.ws + WS_EPOCH))) + 1u;
    const int c16 = lane & 15, kq = lane >> 4, r32 = lane & 31, h = lane >> 5;
    const int q4 = (lane >> 2) & 3, p4 = lane & 3;
    const int kh = w >> 2, wq4 = w & 3;

    bf16x8 qmA, qmB; unsigned qeA, qeB;
    {
        const int T0 = 128 * tb + 16 * w;
        const bool has_kv = pi < 5;
        const bool has_ssm = idx >= 14 && !MK_NOSSM;
        {
            const __amdgpu_buffer_rsrc_t rs_wq = __builtin_amdgcn_make_buffer_rsrc((void*)p.wq, 0, DM * DM * 2, RSRC_W3);
            int kl = (lane * 43) >> 8, g = lane - 6 * kl;
            const unsigned dst = lds0 + L_W + w * 6912;
#pragma unroll
            for (int i = 0; i < 7; ++i) {
                const int vo = swap23(72 * w + kl) * 1152 + 72 * pi + 16 * g;
                if (i < 6 || lane < 48) dma16(rs_wq, vo, dst + 1024 * i);
                g += 4; kl += 10; if (g >= 6) { g -= 6; kl += 1; }
            }
        }
        u32x2 kp0 = {}, vp0 = {}, kp1 = {}, vp1 = {};
        const int kr0 = 72 * w + lane, kr1 = 72 * w + 64 + (lane & 7);
        if (has_kv) {
            const __amdgpu_buffer_rsrc_t rs_wk = __builtin_amdgcn_make_buffer_rsrc((void*)p.wk, 0, DM * 18 * 2, RSRC_W3);
            const __amdgpu_buffer_rsrc_t rs_wv = __builtin_amdgcn_make_buffer_rsrc((void*)p.wv, 0, DM * 18 * 2, RSRC_W3);
            const int o0 = swap23(kr0) * 36 + 8 * pi, o1 = swap23(kr1) * 36 + 8 * pi;
            kp0 = __builtin_amdgcn_raw_buffer_load_b64(rs_wk, o0, 0, 0); vp0 = __builtin_amdgcn_raw_buffer_load_b64(rs_wv, o0, 0, 0);
            kp1 = __builtin_amdgcn_raw_buffer_load_b64(rs_wk, o1, 0, 0); vp1 = __builtin_amdgcn_raw_buffer_load_b64(rs_wv, o1, 0, 0);
        }
        SBAR();
        arrive(L, C_D, lane);
        waitc(L, C_D, 8);
        u32x4 xa[9], xb[9];
        {
            const unsigned short* xp = p.x + (size_t)(T0 + (lane >> 3)) * DM + 8 * (lane & 7);
#pragma unroll
            for (int s = 0; s < 9; ++s) { xa[s] = *(const u32x4*)(xp + 64 * s); xb[s] = *(const u32x4*)(xp + 64 * s + 8 * DM); }
        }
        SBAR();
        asm volatile("s_waitcnt vmcnt(18)" ::: "memory");
        SBAR();
        if (has_kv) {
            ldsw(L + L_W + kr0 * 96 + 80, (u32x4){kp0[0], kp0[1], vp0[0], vp0[1]});
            if (lane < 8) ldsw(L + L_W + kr1 * 96 + 80, (u32x4){kp1[0], kp1[1], vp1[0], vp1[1]});
        }
        arrive(L, C_W, lane);
        f32x4 D0 = {}, D1 = {}, D2 = {}, D2t = {};
        bf16x8 xf[18];
        lds_p wb = L + L_W + (16 * (kq >> 1) + 4 * (kq & 1) + q4) * 96 + 8 * p4;
        {
            lds_p xs = L + L_Q + w * 2048;
            lds_p xw = xs + (lane >> 3) * 128 + 16 * ((lane & 7) ^ (lane >> 4));
            lds_p xw2 = xs + 1024 + (lane >> 3) * 128 + 16 * ((lane & 7) ^ (4 + (lane >> 4)));
            lds_p xr0 = xs + c16 * 128 + 16 * (kq ^ ((c16 >> 1) & 7)), xr1 = xs + c16 * 128 + 16 * ((4 + kq) ^ ((c16 >> 1) & 7));
            ldsw(xw, xa[0]); ldsw(xw2, xb[0]); xf[0] = ldsr<bf16x8>(xr0); xf[1] = ldsr<bf16x8>(xr1);
            waitc(L, C_W, 8);
            STAMP(1);
#pragma unroll
            for (int s = 0; s < 9; ++s) {
                if (s < 8) { ldsw(xw, xa[s + 1]); ldsw(xw2, xb[s + 1]); xf[2 * s + 2] = ldsr<bf16x8>(xr0); xf[2 * s + 3] = ldsr<bf16x8>(xr1); }
#pragma unroll
                for (int e = 0; e < 2; ++e) {
                    const int ks = 2 * s + e;
                    const bf16x8 w2 = cat8(ldtr(wb + ks * 3072 + 64), ldtr(wb + ks * 3072 + 768 + 64));
                    D2 = MF16(w2, xf[ks], D2);
                    D2t = MF16(xf[ks], w2, D2t);
                }
                SBAR();
            }
        }
        STAMP(2);
        if (has_kv) {
            const int kt = 4 * tb + (w >> 1), rr = 16 * (w & 1) + c16, s16 = w & 1, tbase = WS_KV + kt * KVT;
            if (kq == 2) {
                u32x2 v = {cvtpk(D2[0], D2[1]), pi == 4 ? 0x3F803F80u : cvtpk(D2[2], D2[3])};
                const int off = pi == 4 ? 2304 + rr * 8 : ((pi >> 1) * 32 + rr) * 16 + (pi & 1) * 8;
                __builtin_amdgcn_raw_buffer_store_b64(v, rs_ws, tbase + off, 0, AUX_SC1);
            }
            if (c16 >= 12) {
                const int dv = 4 * pi + c16 - 12;
                u32x2 v = {cvtpk(D2t[0], D2t[1]), cvtpk(D2t[2], D2t[3])};
                if (dv == 18) v = (u32x2){0x3F803F80u, 0x3F803F80u};
                if (dv == 19) v = (u32x2){0u, 0u};
                __builtin_amdgcn_raw_buffer_store_b64(v, rs_ws, tbase + 1024 + ((s16 * 2 + (kq & 1)) * 20 + dv) * 16 + (kq >> 1) * 8, 0, AUX_SC1);
            }
        }
        if (has_kv) {
            DRAIN(); arrive(L, C_P, lane);
            if (w == 0) {
                waitc(L, C_P, 8);
                if (lane < 8) gstore_sc1((unsigned*)(p.ws + WS_FLAG1 + lane * 2048 + tb * 128 + pi * 4), tag);
            }
        }
        STAMP(8);
        SBAR();
#pragma unroll
        for (int ks = 0; ks < 18; ++ks) {
            const bf16x8 w0 = cat8(ldtr(wb + ks * 3072), ldtr(wb + ks * 3072 + 768));
            const bf16x8 w1 = cat8(ldtr(wb + ks * 3072 + 32), ldtr(wb + ks * 3072 + 768 + 32));
            D0 = MF16(w0, xf[ks], D0); D1 = MF16(w1, xf[ks], D1);
        }
        {
            lds_p qs = L + L_Q + w * 2048;
            ldsw(qs + c16 * 48 + 8 * kq, (u32x2){cvtpk(D0[0] * QSCALE, D0[1] * QSCALE), cvtpk(D0[2] * QSCALE, D0[3] * QSCALE)});
            const int a0 = kq == 0 ? c16 * 48 + 32 : (16 + c16) * 48 + 2 * (4 * kq - 2);
            const int a1 = (16 + c16) * 48 + 8 * kq;
            ldsw(qs + a0, cvtpk(D1[0] * QSCALE, D1[1] * QSCALE));
            ldsw(qs + a1, cvtpk(D1[2] * QSCALE, D1[3] * QSCALE));
            if (kq == 0) {
                ldsw(qs + (16 + c16) * 48 + 28, cvtpk(D2[0] * QSCALE, D2[1] * QSCALE));
                ldsw(qs + (16 + c16) * 48 + 32, cvtpk(D2[2] * QSCALE, D2[3] * QSCALE));
            }
        }
        arrive(L, C_Q, lane);
        if (has_ssm) {
            u32x2 uu[4];
#pragma unroll
            for (int e = 0; e < 4; ++e) uu[e] = *(const u32x2*)(p.u + (size_t)(256 * w + 4 * lane + e) * DM + 72 * xcd + 4 * (idx - 14));
            const int uo = SCH_U + 512 + 2 * (256 * w + 4 * lane);
#pragma unroll
            for (int c = 0; c < 4; ++c) {
                const unsigned sel = (c & 1) ? 0x07060302u : 0x05040100u;
                ldsw(L + L_S + c * SCH + uo, (u32x2){__builtin_amdgcn_perm(uu[1][c >> 1], uu[0][c >> 1], sel), __builtin_amdgcn_perm(uu[3][c >> 1], uu[2][c >> 1], sel)});
            }
            arrive(L, C_U, lane);
        }
        {
            if (w == 7) {
                const unsigned* f = (const unsigned*)(p.ws + WS_FLAG1 + xcd * 2048);
                const int i0 = lane, i1 = 64 + (lane & 15);
                const unsigned* f0 = f + (i0 / 5) * 32 + i0 % 5; const unsigned* f1 = f + (i1 / 5) * 32 + i1 % 5;
                for (unsigned spins = 0; spins < SPIN_GLB; ++spins) {
                    const unsigned a0 = gload_sc1(f0), a1 = gload_sc1(f1); const bool ok = (a0 == tag) & (a1 == tag);
                    if (__all(ok)) break;
                    __builtin_amdgcn_s_sleep(2);
                }
                __builtin_amdgcn_fence(__ATOMIC_ACQUIRE, "wavefront");
                arrive(L, C_F1, lane);
            }
        }
        waitc(L, C_Q, 8);
        {
            lds_p qa = L + L_Q + wq4 * 2048 + r32 * 48, qb = qa + 4 * 2048;
            qmA = ldsr<bf16x8>(qa + 16 * h); qmB = ldsr<bf16x8>(qb + 16 * h);
            qeA = ldsr<unsigned>(qa + 32); qeB = ldsr<unsigned>(qb + 32);
        }
        waitc(L, C_F1, 1);
        STAMP(3);
    }

    {
        lds_p ring = L + L_W + kh * (8 * KVT);
        const int dmo = WS_KV + (32 * kh + wq4) * KVT + lane * 16;
        const unsigned dmd = lds0 + L_W + kh * (8 * KVT) + wq4 * KVT;
#define KV_DMA_A(cc) do { dma16_sc1(rs_ws, dmo + (cc) * (4 * KVT), dmd + ((cc) & 1) * (4 * KVT)); dma16_sc1(rs_ws, dmo + (cc) * (4 * KVT) + 1024, dmd + ((cc) & 1) * (4 * KVT) + 1024); } while (0)
#define KV_DMA_B(cc) do { if (lane < 32) dma16_sc1(rs_ws, dmo + (cc) * (4 * KVT) + 2048, dmd + ((cc) & 1) * (4 * KVT) + 2048); } while (0)
        lds_p kb = ring + lane * 16, eb = ring + 2304 + r32 * 8, vb = ring + 1024 + (h * 20 + (r32 < 19 ? r32 : 19)) * 16;
        const f32x16 Z = {};
        unsigned mA = 0, mB = 0;
        f32x16 OA, OB;
        for (int pass = 0; pass < 2; ++pass) {
            KV_DMA_A(0); KV_DMA_B(0); KV_DMA_A(1); KV_DMA_B(1);
            asm volatile("s_waitcnt vmcnt(3)" ::: "memory");
            arrive(L, C_RW, lane); waitc(L, C_RW, 8 * (pass + 1));
            STAMP(9); CSTAMP(10);
            if (pass == 0) {
                const bf16x8 km = ldsr<bf16x8>(L + L_W + lane * 16); const bf16x4 ke = ldsr<bf16x4>(L + L_W + 2304 + r32 * 8);
                const u32x2 ea = {h ? 0u : qeA, 0u}, ebq = {h ? 0u : qeB, 0u};
                f32x16 sa = MF32(km, qmA, Z), sb = MF32(km, qmB, Z);
                sa = MF32E(ke, __builtin_bit_cast(bf16x4, ea), sa); sb = MF32E(ke, __builtin_bit_cast(bf16x4, ebq), sb);
                float ma = sa[0], mb = sb[0];
#pragma unroll
                for (int i = 1; i < 16; ++i) { ma = fmaxf(ma, sa[i]); mb = fmaxf(mb, sb[i]); }
                auto ra = __builtin_amdgcn_permlane32_swap(__float_as_uint(ma), __float_as_uint(ma), false, false);
                auto rb = __builtin_amdgcn_permlane32_swap(__float_as_uint(mb), __float_as_uint(mb), false, false);
                ma = fmaxf(__uint_as_float(ra[0]), __uint_as_float(ra[1])); mb = fmaxf(__uint_as_float(rb[0]), __uint_as_float(rb[1]));
                const float ah = bflo(cvtpk(ma, 0.f)), bh = bflo(cvtpk(mb, 0.f));
                mA = cvtpk(-ah, ah - ma); mB = cvtpk(-bh, bh - mb);
            }
            const u32x2 ea = {h ? 0u : qeA, h ? 0u : mA}, ebq = {h ? 0u : qeB, h ? 0u : mB};
            const bf16x4 qxA = __builtin_bit_cast(bf16x4, ea), qxB = __builtin_bit_cast(bf16x4, ebq);
            f32x16 SA, SB = Z; u32x4 pa0, pa1, pb0 = {}, pb1 = {}; bf16x8 km, vx0, vx1, vy0, vy1; bf16x4 ke;
            OA = Z; OB = Z;
            km = ldsr<bf16x8>(kb); ke = ldsr<bf16x4>(eb);
            SA = MF32(km, qmA, Z); SA = MF32E(ke, qxA, SA);
            vx0 = ldsr<bf16x8>(vb); vx1 = ldsr<bf16x8>(vb + 640);
#define PH(SC, SN, QM, QX, ON, pp0, pp1, pn0, pn1, V0, V1, LD1, LD2, LD4) do { \
            ON = MF32(V0, BC8(pp0), ON); SBAR(); \
            LD1; \
            SC[0] = EXP2(SC[0]); SC[1] = EXP2(SC[1]); SC[2] = EXP2(SC[2]); SC[3] = EXP2(SC[3]); SBAR(); \
            SN = MF32(km, QM, Z); SBAR(); \
            LD2; \
            SC[4] = EXP2(SC[4]); SC[5] = EXP2(SC[5]); pn0[0] = cvtpk(SC[0], SC[1]); SC[6] = EXP2(SC[6]); SC[7] = EXP2(SC[7]); pn0[1] = cvtpk(SC[2], SC[3]); SBAR(); \
            ON = MF32(V1, BC8(pp1), ON); SBAR(); \
            SC[8] = EXP2(SC[8]); SC[9] = EXP2(SC[9]); pn0[2] = cvtpk(SC[4], SC[5]); SC[10] = EXP2(SC[10]); SC[11] = EXP2(SC[11]); pn0[3] = cvtpk(SC[6], SC[7]); PIN(pn0); SBAR(); \
            SN = MF32E(ke, QX, SN); SBAR(); \
            LD4; \
            SC[12] = EXP2(SC[12]); SC[13] = EXP2(SC[13]); pn1[0] = cvtpk(SC[8], SC[9]); SC[14] = EXP2(SC[14]); SC[15] = EXP2(SC[15]); pn1[1] = cvtpk(SC[10], SC[11]); \
            pn1[2] = cvtpk(SC[12], SC[13]); pn1[3] = cvtpk(SC[14], SC[15]); PIN(pn1); SBAR(); \
        } while (0)
#define VRD(t, N0, N1) (N0 = ldsr<bf16x8>(vb + (t) * KVT), N1 = ldsr<bf16x8>(vb + (t) * KVT + 640))
#define KMRD(t) (km = ldsr<bf16x8>(kb + (((t) + 1) & 7) * KVT))
#define KERD(t) (ke = ldsr<bf16x4>(eb + (((t) + 1) & 7) * KVT))
#define PHA(P0, P1, LD1, LD2, LD4) PH(SA, SB, qmB, qxB, OB, pb0, pb1, pa0, pa1, P0, P1, LD1, LD2, LD4);
#define PHB(N0, N1, LD1, LD2, LD4) PH(SB, SA, qmA, qxA, OA, pa0, pa1, pb0, pb1, N0, N1, LD1, LD2, LD4);
#define TA(t) PHA(vx0, vx1, VRD(t, vy0, vy1), KMRD(t), KERD(t))
#define TB() PHB(vy0, vy1, (void)0, (void)0, (void)0)
#define UA(t) PHA(vy0, vy1, VRD(t, vx0, vx1), KMRD(t), KERD(t))
#define UB() PHB(vx0, vx1, (void)0, (void)0, (void)0)
#define PRIO(x) __builtin_amdgcn_s_setprio(x);
#define LAST(t, b) \
            PHA(vy0, vy1, VRD(t, vx0, vx1), (void)0, (void)0) \
            asm volatile("s_waitcnt vmcnt(0)" ::: "memory"); \
            if ((b) < 7) { if (it == 0 && pass == 0) { arrive(L, C_B, lane); waitc(L, C_B, 8 * ((b) + 1)); } else __builtin_amdgcn_s_barrier(); } \
            CBAR(); PRIO(3) KMRD(t); KERD(t); SBAR(); \
            PHB(vx0, vx1, (void)0, if ((b) + 2 < 8) KV_DMA_A((b) + 2), if ((b) + 2 < 8) KV_DMA_B((b) + 2))
            PRIO(3)
            for (int it = 0; it < 4; ++it) {
                TA(0) PRIO(2) TB() UA(1) PRIO(1) UB() TA(2) PRIO(0) TB()
                LAST(3, 2 * it)
                if (it == 1 && pass == 0) {
                    const __amdgpu_buffer_rsrc_t rs_wo = __builtin_amdgcn_make_buffer_rsrc((void*)p.wo, 0, DM * DM * 2, RSRC_W3);
                    int kp = 64 * w + lane; const int k9 = (kp * 7282) >> 16; int g = kp - 9 * k9; kp = k9;
                    for (int inst = w; inst < 81; inst += 8) {
                        dma16(rs_wo, swap23(kp) * 1152 + 144 * (pi >> 1) + 16 * g, lds0 + L_S + 1024 * inst);
                        g += 8; kp += 56; if (g >= 9) { g -= 9; kp += 1; }
                    }
                }
                TA(4) PRIO(2) TB() UA(5) PRIO(1) UB() TA(6) PRIO(0) TB()
                LAST(7, 2 * it + 1)
            }
            OB = MF32(vx0, BC8(pb0), OB); OB = MF32(vx1, BC8(pb1), OB);
#undef TA
#undef TB
#undef UA
#undef UB
#undef LAST
#undef PHA
#undef PHB
#undef VRD
#undef KMRD
#undef KERD
#undef PH
            PRIO(0)
            STAMP(4); CSTAMP(11);
            lds_p xo = (w < 4 ? L + L_X1 : L + L_Q) + wq4 * 3072, xi = (w < 4 ? L + L_Q : L + L_X1) + wq4 * 3072;
            {
                const f32x16& S = kh ? OA : OB;
#pragma unroll
                for (int g = 0; g < 3; ++g) ldsw(xo + g * 1024 + lane * 16, (f32x4){S[4 * g], S[4 * g + 1], S[4 * g + 2], S[4 * g + 3]});
            }
            BARRIER();
            float o[12];
#pragma unroll
            for (int g = 0; g < 3; ++g) {
                const f32x4 t = ldsr<f32x4>(xi + g * 1024 + lane * 16);
#pragma unroll
                for (int i = 0; i < 4; ++i) o[4 * g + i] = (kh ? OB[4 * g + i] : OA[4 * g + i]) + t[i];
            }
            const float lsum = __uint_as_float(__builtin_amdgcn_permlane32_swap(__float_as_uint(o[10]), __float_as_uint(o[10]), false, false)[0]);
            const float rl = __builtin_amdgcn_rcpf(lsum);
            bool bad = !(lsum < 1.2676506e30f);
#pragma unroll
            for (int i = 0; i < 8; ++i) bad |= !(fabsf(o[i]) < 3.0e38f);
            if (h == 0) bad |= !(fabsf(o[8]) < 3.0e38f) || !(fabsf(o[9]) < 3.0e38f);
            {
                lds_p os = xi;
                lds_p ob = os + (r32 & 15) * 72 + (r32 >> 4) * 36 + 8 * h;
                ldsw(ob, cvtpk(o[0] * rl, o[1] * rl)); ldsw(ob + 4, cvtpk(o[2] * rl, o[3] * rl));
                ldsw(ob + 16, cvtpk(o[4] * rl, o[5] * rl)); ldsw(ob + 20, cvtpk(o[6] * rl, o[7] * rl));
                if (h == 0) ldsw(ob + 32, cvtpk(o[8] * rl, o[9] * rl));
#pragma unroll
                for (int i = 0; i < 3; ++i) {
                    const int e = lane + 64 * i;
                    if (e < 144) {
                        const int tk = e & 15, pc = e >> 4, k0 = 36 * pi + 4 * pc;
                        const u32x2 v = ldsr<u32x2>(os + tk * 72 + pc * 8);
                        __builtin_amdgcn_raw_buffer_store_b64(v, rs_ws, WS_O + (8 * tb + w) * 18432 + (k0 >> 3) * 256 + tk * 16 + (k0 & 4) * 2, 0, AUX_SC1);
                    }
                }
            }
            if (pass == 0 && __any(bad) && lane == 0) ((volatile LAS unsigned*)(L + L_C))[C_BAD] = 1u;
            DRAIN();
            BARRIER();
            const bool redo = pass == 0 && __builtin_amdgcn_readfirstlane(((volatile LAS unsigned*)(L + L_C))[C_BAD]) != 0;
            if (!redo) break;
            {
                const u32x2 ea0 = {h ? 0u : qeA, 0u}, eb0 = {h ? 0u : qeB, 0u};
                float ma = -INFINITY, mb = -INFINITY;
                for (int t = 0; t < 64; ++t) {
                    const u32x4 kmv = __builtin_amdgcn_raw_buffer_load_b128(rs_ws, WS_KV + t * KVT + lane * 16, 0, AUX_SC1);
                    const u32x2 kev = __builtin_amdgcn_raw_buffer_load_b64(rs_ws, WS_KV + t * KVT + 2304 + r32 * 8, 0, AUX_SC1);
                    f32x16 sa = MF32(BC8(kmv), qmA, Z), sb = MF32(BC8(kmv), qmB, Z);
                    sa = MF32E(__builtin_bit_cast(bf16x4, kev), __builtin_bit_cast(bf16x4, ea0), sa);
                    sb = MF32E(__builtin_bit_cast(bf16x4, kev), __builtin_bit_cast(bf16x4, eb0), sb);
#pragma unroll
                    for (int i = 0; i < 16; ++i) { ma = fmaxf(ma, sa[i]); mb = fmaxf(mb, sb[i]); }
                }
                ma = fmaxf(ma, __shfl_xor(ma, 32)); mb = fmaxf(mb, __shfl_xor(mb, 32));
                const float ah = bflo(cvtpk(ma, 0.f)), bh = bflo(cvtpk(mb, 0.f));
                mA = cvtpk(-ah, ah - ma); mB = cvtpk(-bh, bh - mb);
            }
        }
#undef KV_DMA_A
#undef KV_DMA_B
#undef PRIO
        if (w == 0 && lane == 0) gstore_sc1((unsigned*)(p.ws + WS_FLAG2 + tb * 128 + pi * 4), tag);
        STAMP(5);
    }

    {
        if (w == 7) {
            const unsigned* f = (const unsigned*)(p.ws + WS_FLAG2 + tb * 128) + (lane & 15);
            for (unsigned spins = 0; spins < SPIN_GLB; ++spins) {
                if (__all(gload_sc1(f) == tag)) break;
                __builtin_amdgcn_s_sleep(1);
            }
            __builtin_amdgcn_fence(__ATOMIC_ACQUIRE, "wavefront");
        }
        BARRIER();
        STAMP(6);
        const int Tt = 128 * tb + 64 * (pi & 1) + 16 * wq4, F0 = 72 * (pi >> 1);
        u32x4 of[9];
#pragma unroll
        for (int i = 0; i < 9; ++i) of[i] = __builtin_amdgcn_raw_buffer_load_b128(rs_ws, WS_O + (Tt >> 4) * 18432 + (9 * kh + i) * 1024 + lane * 16, 0, AUX_SC1);
        f32x4 D[5] = {};
        lds_p wb = L + L_S + (16 * (kq >> 1) + 4 * (kq & 1) + q4) * 144 + 8 * p4 + kh * (9 * 32 * 144);
#pragma unroll
        for (int i = 0; i < 9; ++i)
#pragma unroll
            for (int T = 0; T < 5; ++T)
                D[T] = MF16(cat8(ldtr(wb + i * 4608 + 32 * T), ldtr(wb + i * 4608 + 1152 + 32 * T)), BC8(of[i]), D[T]);
        lds_p xo = L + L_W + w * 3072, xi = L + L_W + (w ^ 4) * 3072;
        if (kh == 0) { ldsw(xo + lane * 16, D[3]); ldsw(xo + 1024 + lane * 16, D[4]); }
        else { ldsw(xo + lane * 16, D[0]); ldsw(xo + 1024 + lane * 16, D[1]); ldsw(xo + 2048 + lane * 16, D[2]); }
        BARRIER();
        unsigned short* op = p.out + (size_t)(Tt + c16) * (2 * DM) + F0 + 4 * kq;
        if (kh == 0) {
#pragma unroll
            for (int T = 0; T < 3; ++T) {
                const f32x4 v = D[T] + ldsr<f32x4>(xi + T * 1024 + lane * 16);
                *(u32x2*)(op + 16 * T) = (u32x2){cvtpk(v[0], v[1]), cvtpk(v[2], v[3])};
            }
        } else {
#pragma unroll
            for (int T = 3; T < 5; ++T) {
                const f32x4 v = D[T] + ldsr<f32x4>(xi + (T - 3) * 1024 + lane * 16);
                if (T == 3 || kq < 2) *(u32x2*)(op + 16 * T) = (u32x2){cvtpk(v[0], v[1]), cvtpk(v[2], v[3])};
            }
        }
        STAMP(7);
        STAMP_DUMP();
        if (bid == 0 && w == 7) {
            const unsigned* f = (const unsigned*)(p.ws + WS_FLAG2);
            for (unsigned spins = 0; spins < SPIN_GLB; ++spins) {
                bool ok = true;
#pragma unroll
                for (int i = 0; i < 4; ++i) { const int e = lane + 64 * i; ok &= gload_sc1(f + (e >> 4) * 32 + (e & 15)) == tag; }
                if (__all(ok)) break;
                __builtin_amdgcn_s_sleep(2);
            }
            if (lane == 0) gstore_sc1((unsigned*)(p.ws + WS_EPOCH), tag);
        }
    }
}

}

extern "C" void kernel_launch(void* const* d_in, const int* in_sizes, int n_in, void* d_out, int out_size, void* d_ws, size_t ws_size,
                              hipStream_t stream) {
    Params p;
    p.x = (const unsigned short*)d_in[0]; p.wq = (const unsigned short*)d_in[1]; p.wk = (const unsigned short*)d_in[2];
    p.wv = (const unsigned short*)d_in[3]; p.wo = (const unsigned short*)d_in[4]; p.u = (const unsigned short*)d_in[5];
    p.are = (const unsigned short*)d_in[6]; p.aim = (const unsigned short*)d_in[7]; p.bre = (const unsigned short*)d_in[8];
    p.bim = (const unsigned short*)d_in[9]; p.cre = (const unsigned short*)d_in[10]; p.cim = (const unsigned short*)d_in[11];
    p.ldt = (const unsigned short*)d_in[12]; p.dsk = (const unsigned short*)d_in[13];
    p.out = (unsigned short*)d_out; p.ws = (char*)d_ws;
    (void)hipFuncSetAttribute((const void*)fused_kernel, hipFuncAttributeMaxDynamicSharedMemorySize, L_END);
    fused_kernel<<<256, 768, L_END, stream>>>(p);
}
```

```cpp
#include <hip/hip_runtime.h>
#include <hip/hip_bf16.h>
#include <stdint.h>

#define U_BY 3
#define S_PRIO0 0
#define S_PRIOB 0
#define S_PRIOC 3
#define MK_TOUCH 1
#define MK_CODEPF 1
#define CODE_BACK 16384
#define CODE_FWD 11008
#define MK_NOSSM 0
#define MK_STAMPS 0

namespace {

typedef __attribute__((ext_vector_type(16))) float f32x16;
typedef __attribute__((ext_vector_type(4))) float f32x4;
typedef __attribute__((ext_vector_type(2))) float f32x2;
typedef __attribute__((ext_vector_type(8))) short bf16x8;
typedef __attribute__((ext_vector_type(4))) short bf16x4;
typedef __attribute__((ext_vector_type(2))) __bf16 bf16x2_t;
typedef __attribute__((ext_vector_type(4))) unsigned u32x4;
typedef __attribute__((ext_vector_type(2))) unsigned u32x2;
typedef short v4i16_t __attribute__((ext_vector_type(4)));
#define LAS __attribute__((address_space(3)))
#define GAS __attribute__((address_space(1)))
typedef LAS char* lds_p;
typedef const unsigned short* cbf;

constexpr int SEQ = 2048, DM = 576;
constexpr float QSCALE = 0.34005040627f;
constexpr float LOG2E = 1.4426950408889634f;
constexpr float INV2PI = 0.15915494309189535f;

constexpr int WS_EPOCH = 0;
constexpr int WS_FLAG1 = 4096;
constexpr int WS_FLAG2 = WS_FLAG1 + 16384;
constexpr int WS_KV = 32768;
constexpr int KVT = 2560;
constexpr int WS_O = WS_KV + 64 * KVT;
constexpr int WS_BYTES = WS_O + SEQ * DM * 2;
constexpr int WS_STAMP = 8 * 1024 * 1024;

constexpr int L_W = 0;
constexpr int L_X1 = 40960;
constexpr int L_S = 55296;
constexpr int L_Q = L_S + 87360;
constexpr int L_C = L_Q + 16384;
constexpr int L_END = L_C + 256;
constexpr int SCH = 21840;
constexpr int SCH_U = 16640;
enum { C_W = 0, C_P, C_Q, C_F1, C_RW, C_B, C_BAD, C_U, C_S2, C_D, C_NUM };

constexpr int RSRC_W3 = 0x00020000;
constexpr int AUX_SC1 = 16;
constexpr unsigned SPIN_LDS = 1u << 20, SPIN_GLB = 1u << 16;

#define SBAR() __builtin_amdgcn_sched_barrier(0)
#define PIN(x) asm volatile("" : "+v"(x))
#define CBAR() asm volatile("" ::: "memory")
#define DRAIN() asm volatile("s_waitcnt vmcnt(0)" ::: "memory")
#define MF32(a, b, c) __builtin_amdgcn_mfma_f32_32x32x16_bf16(a, b, c, 0, 0, 0)
#define MF32E(a, b, c) __builtin_amdgcn_mfma_f32_32x32x8bf16_1k(a, b, c, 0, 0, 0)
#define MF16(a, b, c) __builtin_amdgcn_mfma_f32_16x16x32_bf16(a, b, c, 0, 0, 0)
#define EXP2(x) __builtin_amdgcn_exp2f(x)
#define BC8(x) __builtin_bit_cast(bf16x8, x)
#define DI __device__ __forceinline__

DI unsigned cvtpk(float lo, float hi) { f32x2 v = {lo, hi}; return __builtin_bit_cast(unsigned, __builtin_convertvector(v, bf16x2_t)); }
DI float bflo(unsigned x) { return __builtin_bit_cast(float, x << 16); }
DI float bfhi(unsigned x) { return __builtin_bit_cast(float, x & 0xffff0000u); }
DI float bf1(unsigned short x) { return __builtin_bit_cast(float, (unsigned)x << 16); }
template <class T> DI T ldsr(lds_p p) { return *(const LAS T*)p; }
template <class T> DI void ldsw(lds_p p, T v) { *(LAS T*)p = v; }
DI bf16x4 ldtr(lds_p p) { return __builtin_bit_cast(bf16x4, __builtin_amdgcn_ds_read_tr16_b64_v4i16((LAS v4i16_t*)p)); }
DI bf16x8 cat8(bf16x4 a, bf16x4 b) { return __builtin_shufflevector(a, b, 0, 1, 2, 3, 4, 5, 6, 7); }
DI int swap23(int k) { return (k & ~12) | ((k & 4) << 1) | ((k & 8) >> 1); }
DI void dma16(__amdgpu_buffer_rsrc_t rs, int voff, unsigned lds_base) {
    unsigned keep;
    asm volatile("s_mov_b32 %0, m0\n\ts_mov_b32 m0, %3\n\ts_nop 0\n\tbuffer_load_dwordx4 %1, %2, 0 offen lds\n\ts_mov_b32 m0, %0"
                 : "=&s"(keep) : "v"(voff), "s"(rs), "s"(lds_base) : "memory");
}
DI void dma16_sc1(__amdgpu_buffer_rsrc_t rs, int voff, unsigned lds_base) {
    unsigned keep;
    asm volatile("s_mov_b32 %0, m0\n\ts_mov_b32 m0, %3\n\ts_nop 0\n\tbuffer_load_dwordx4 %1, %2, 0 offen sc1 lds\n\ts_mov_b32 m0, %0"
                 : "=&s"(keep) : "v"(voff), "s"(rs), "s"(lds_base) : "memory");
}
#define BARRIER() asm volatile("s_waitcnt lgkmcnt(0)\n\ts_barrier" ::: "memory")
DI void arrive(lds_p L, int c, int lane) {
    CBAR();
    if (lane == 0) (void)__hip_atomic_fetch_add((LAS unsigned*)(L + L_C) + c, 1u, __ATOMIC_RELAXED, __HIP_MEMORY_SCOPE_WORKGROUP);
    CBAR();
}
DI void waitc(lds_p L, int c, unsigned target) {
    CBAR();
    volatile LAS unsigned* p = (volatile LAS unsigned*)(L + L_C) + c;
    for (unsigned spins = 0; spins < SPIN_LDS; ++spins) {
        if ((unsigned)__builtin_amdgcn_readfirstlane(*p) >= target) break;
        __builtin_amdgcn_s_sleep(1);
    }
    CBAR();
}
DI unsigned peek(lds_p L, int c) { return *((volatile LAS unsigned*)(L + L_C) + c); }
DI void waitp(lds_p L, int c, unsigned seen, unsigned target) { if ((unsigned)__builtin_amdgcn_readfirstlane(seen) < target) waitc(L, c, target); CBAR(); }
DI unsigned gload_sc1(const unsigned* p) { return __hip_atomic_load((GAS unsigned*)p, __ATOMIC_RELAXED, __HIP_MEMORY_SCOPE_AGENT); }
DI void gstore_sc1(unsigned* p, unsigned v) { __hip_atomic_store((GAS unsigned*)p, v, __ATOMIC_RELAXED, __HIP_MEMORY_SCOPE_AGENT); }

struct cplx2 { f32x2 re, im; };
DI cplx2 cmul(const cplx2& a, const cplx2& b) { cplx2 r; r.re = a.re * b.re - a.im * b.im; r.im = a.re * b.im + a.im * b.re; return r; }
DI cplx2 cexp(f32x2 ar2, f32x2 airev, float e) {
    f32x2 m = ar2 * e, t = airev * e; cplx2 r;
#pragma unroll
    for (int i = 0; i < 2; ++i) {
        const float mag = EXP2(m[i]), fr = __builtin_amdgcn_fractf(t[i]);
        r.re[i] = mag * __builtin_amdgcn_cosf(fr); r.im[i] = mag * __builtin_amdgcn_sinf(fr);
    }
    return r;
}

struct Params {
    const unsigned short *x, *wq, *wk, *wv, *wo, *u, *are, *aim, *bre, *bim, *cre, *cim, *ldt, *dsk;
    unsigned short* out;
    char* ws;
};

#define STAMP(i) do {} while (0)
#define CSTAMP(i) do {} while (0)
#define STAMP_DECL do {} while (0)
#define STAMP_DUMP() do {} while (0)
#define STAMP_ARG
#define STAMP_PASS

template <int G> DI void conv_group(lds_p L, lds_p R, f32x4 (&acc)[8], int pa0, int pb0) {
    if ((unsigned)__builtin_amdgcn_readfirstlane(peek(L, C_Q)) >= 8u) __builtin_amdgcn_s_setprio(S_PRIOC);
    lds_p a1 = R + pa0 - 64 * (8 * G), b1 = R + pb0 - 64 * (8 * G), a2 = R + pa0 - 64 * (63 - 8 * G), b2 = R + pb0 - 64 * (63 - 8 * G);
    PIN(a1); PIN(b1); PIN(a2); PIN(b2);
#define RDA(ptr) cat8(*(const volatile LAS bf16x4*)(ptr), *(const volatile LAS bf16x4*)((ptr) + 8))
    bf16x8 P1 = RDA(a1), P2 = RDA(a2), Q1, Q2, B1[8 - G], B2[G + 1];
#pragma unroll
    for (int n = 0; n < 8 - G; ++n) B1[n] = ldsr<bf16x8>(b1 + 512 * (G + n));
#pragma unroll
    for (int n = 0; n < G + 1; ++n) B2[n] = ldsr<bf16x8>(b2 + 512 * (7 - G + n));
#define CONV_STEP(C1, C2, N1, N2) do { \
        a1 -= 64; b1 -= 64; a2 += 64; b2 += 64; PIN(a1); PIN(b1); PIN(a2); PIN(b2); \
        N1 = RDA(a1); N2 = RDA(a2); SBAR(); \
        _Pragma("unroll") for (int n = 0; n < 8 - G; ++n) { acc[G + n] = MF16(C1, B1[n], acc[G + n]); SBAR(); B1[n] = ldsr<bf16x8>(b1 + 512 * (G + n)); SBAR(); } \
        _Pragma("unroll") for (int n = 0; n < G + 1; ++n) { acc[7 - G + n] = MF16(C2, B2[n], acc[7 - G + n]); SBAR(); B2[n] = ldsr<bf16x8>(b2 + 512 * (7 - G + n)); SBAR(); } \
    } while (0)
#pragma unroll 1
    for (int i = 0; i < 4; ++i) { CONV_STEP(P1, P2, Q1, Q2); CONV_STEP(Q1, Q2, P1, P2); }
#undef CONV_STEP
#undef RDA
}

DI void ssm_wave(const Params& p, lds_p L, int s, int lane, int ch0 STAMP_ARG) {
    const int d = ch0 + s; const int w = 8 + s; (void)w;
    __builtin_amdgcn_s_setprio(S_PRIO0);
    lds_p R = L + L_S + s * SCH;
    const int pb = d * 128 + lane;
    const f32x2 are = {bf1(p.are[pb]), bf1(p.are[pb + 64])}, aim = {bf1(p.aim[pb]), bf1(p.aim[pb + 64])};
    const f32x2 bre = {bf1(p.bre[pb]), bf1(p.bre[pb + 64])}, bim = {bf1(p.bim[pb]), bf1(p.bim[pb + 64])};
    const f32x2 cre = {bf1(p.cre[pb]), bf1(p.cre[pb + 64])}, cim = {bf1(p.cim[pb]), bf1(p.cim[pb + 64])};
    const float ldt = bf1(p.ldt[d]);
    const u32x2 dsk = *(const u32x2*)(p.dsk + ch0);
    const int t0 = 512 * s + 8 * lane;
    const float dt = bflo(cvtpk(expf(ldt), 0.f));
    const f32x2 ar2 = (are * dt) * LOG2E, airev = (aim * dt) * INV2PI;
    cplx2 dB; dB.re = bre * dt; dB.im = bim * dt;
    cplx2 C; C.re = cre; C.im = cim;
    const cplx2 CB = cmul(C, dB);
    const cplx2 w1 = cexp(ar2, airev, 1.f), w8 = cexp(ar2, airev, 8.f), w128 = cexp(ar2, airev, 128.f);

    STAMP(3);
    lds_p st0 = R + lane * 16, st1 = R + 4224 + lane * 16;
    const int kq = lane >> 4, q4 = (lane >> 2) & 3, p4 = lane & 3;
    lds_p trb = R + (p4 >> 1) * 4224 + (16 * (kq >> 1) + 4 * (kq & 1) + q4) * 16 + (p4 & 1) * 8;
#define PUT16(V, NEG) do { \
        u32x4 a0, a1, b0, b1, c0, c1, d0, d1; \
        _Pragma("unroll") for (int j = 0; j < 4; ++j) { \
            a0[j] = cvtpk(V[2 * j].re[0], V[2 * j + 1].re[0]); a1[j] = cvtpk(V[8 + 2 * j].re[0], V[9 + 2 * j].re[0]); \
            b0[j] = cvtpk(V[2 * j].re[1], V[2 * j + 1].re[1]); b1[j] = cvtpk(V[8 + 2 * j].re[1], V[9 + 2 * j].re[1]); \
            c0[j] = cvtpk(NEG V[2 * j].im[0], NEG V[2 * j + 1].im[0]); c1[j] = cvtpk(NEG V[8 + 2 * j].im[0], NEG V[9 + 2 * j].im[0]); \
            d0[j] = cvtpk(NEG V[2 * j].im[1], NEG V[2 * j + 1].im[1]); d1[j] = cvtpk(NEG V[8 + 2 * j].im[1], NEG V[9 + 2 * j].im[1]); } \
        ldsw(st0, a0); ldsw(st1, a1); ldsw(st0 + 1024, b0); ldsw(st1 + 1024, b1); \
        ldsw(st0 + 2048, c0); ldsw(st1 + 2048, c1); ldsw(st0 + 3072, d0); ldsw(st1 + 3072, d1); } while (0)
#define GETF(ks) cat8(ldtr(trb + (ks) * 512), ldtr(trb + (ks) * 512 + 128))
    bf16x8 yf[8];
    {
        cplx2 V[16];
        V[0].re = (f32x2){1.f, 1.f}; V[0].im = (f32x2){0.f, 0.f}; V[1].re = w8.re; V[1].im = -w8.im;
        V[2] = cmul(V[1], V[1]); V[3] = cmul(V[2], V[1]); V[4] = cmul(V[2], V[2]);
        V[5] = cmul(V[4], V[1]); V[6] = cmul(V[4], V[2]); V[7] = cmul(V[4], V[3]); V[8] = cmul(V[4], V[4]);
#pragma unroll
        for (int j = 9; j < 16; ++j) V[j] = cmul(V[8], V[j - 8]);
        PUT16(V, +);
#pragma unroll
        for (int ks = 0; ks < 8; ++ks) yf[ks] = GETF(ks);
    }
    STAMP(9);
    const cplx2 w2 = cmul(w1, w1), w3 = cmul(w2, w1), w4 = cmul(w2, w2), w5 = cmul(w4, w1), w6 = cmul(w4, w2), w7 = cmul(w4, w3);
    f32x4 D0 = {}, D1 = {}, D2 = {};
    cplx2 G = cmul(CB, w128);
    {
        cplx2 V[16];
        V[0] = CB; V[1] = cmul(CB, w1); V[2] = cmul(CB, w2); V[3] = cmul(CB, w3); V[4] = cmul(CB, w4); V[5] = cmul(CB, w5); V[6] = cmul(CB, w6); V[7] = cmul(CB, w7);
        V[8] = G; V[9] = cmul(G, w1); V[10] = cmul(G, w2); V[11] = cmul(G, w3); V[12] = cmul(G, w4); V[13] = cmul(G, w5); V[14] = cmul(G, w6); V[15] = cmul(G, w7);
        PUT16(V, +);
        bf16x8 xf[8];
#pragma unroll
        for (int ks = 0; ks < 8; ++ks) xf[ks] = GETF(ks);
        SBAR();
#pragma unroll
        for (int ks = 0; ks < 8; ++ks) D0 = MF16(xf[ks], yf[ks], D0);
    }
    PIN(D0); STAMP(10);
    {
        cplx2 V[16];
        G = cmul(G, w128); V[0] = G; V[1] = cmul(G, w2); V[2] = cmul(G, w4); V[3] = cmul(G, w6);
        G = cmul(G, w128); V[4] = G; V[5] = cmul(G, w2); V[6] = cmul(G, w4); V[7] = cmul(G, w6);
#pragma unroll
        for (int i = 0; i < 4; ++i) { G = cmul(G, w128); V[8 + 2 * i] = G; V[9 + 2 * i] = cmul(G, w4); }
        PUT16(V, +);
        bf16x8 xf[8];
#pragma unroll
        for (int ks = 0; ks < 8; ++ks) xf[ks] = GETF(ks);
        SBAR();
#pragma unroll
        for (int ks = 0; ks < 8; ++ks) D1 = MF16(xf[ks], yf[ks], D1);
    }
    {
        cplx2 V[16];
#pragma unroll
        for (int i = 0; i < 9; ++i) { G = cmul(G, w128); V[i] = G; }
#pragma unroll
        for (int i = 9; i < 16; ++i) V[i] = G;
        PUT16(V, +);
        bf16x8 xf[8];
#pragma unroll
        for (int ks = 0; ks < 8; ++ks) xf[ks] = GETF(ks);
        SBAR();
#pragma unroll
        for (int ks = 0; ks < 8; ++ks) D2 = MF16(yf[ks], xf[ks], D2);
    }
#undef PUT16
#undef GETF
    PIN(D2); STAMP(4);
    lds_p tab = R + 8448;
    {
        const int j = lane & 15, rq = lane >> 4;
        ldsw(tab + 2 * (8 * j + 4 * (rq & 1) + 128 * (rq >> 1)), (u32x2){cvtpk(D0[0], D0[1]), cvtpk(D0[2], D0[3])});
        const int e0 = rq < 2 ? 256 + 64 * rq + 4 * j : 384 + 64 * (rq - 2) + 2 * j, e1 = rq < 2 ? e0 + 2 : e0 + 32;
        ldsw(tab + 2 * e0, cvtpk(D1[0], D1[1])); ldsw(tab + 2 * e1, cvtpk(D1[2], D1[3]));
        if (j < 8 || lane == 8) ldsw(tab + 2 * (512 + 16 * j + 4 * rq), (u32x2){cvtpk(D2[0], D2[1]), cvtpk(D2[2], D2[3])});
    }
#define PK(hi_src, hi_half, lo_src, lo_half) __builtin_amdgcn_perm(hi_src, lo_src, ((hi_half) ? 0x07060000u : 0x05040000u) | ((lo_half) ? 0x0302u : 0x0100u))
    {
#pragma unroll
        for (int i = 0; i < 2; ++i) {
            const int g = lane + 64 * i, m = 255 - g;
            const unsigned lo = ldsr<unsigned short>(tab + 2 * (384 + m)), hi = ldsr<unsigned short>(tab + 2 * (385 + m));
            const unsigned mid = (m & 1) ? hi : lo, ll = PK(lo, 0, lo, 0);
            ldsw(R + 16 * g, (u32x4){PK(hi, 0, hi, 0), PK(mid, 0, hi, 0), ll, ll});
        }
        {
            const int g = 128 + lane, m = 255 - g;
            const unsigned ab = ldsr<unsigned>(tab + 2 * (256 + 2 * m)), c = ldsr<unsigned short>(tab + 2 * (258 + 2 * m));
            ldsw(R + 16 * g, (u32x4){PK(c, 0, c, 0), PK(ab, 1, ab, 1), PK(ab, 0, ab, 1), PK(ab, 0, ab, 0)});
        }
        {
            const int g = 192 + lane, m = 255 - g;
            u32x4 v;
            if (lane < 32) {
                const u32x2 q = ldsr<u32x2>(tab + 2 * (128 + 4 * m)); const unsigned e = ldsr<unsigned short>(tab + 2 * (132 + 4 * m));
                v = (u32x4){PK(q[1], 1, e, 0), PK(q[1], 0, q[1], 0), PK(q[0], 1, q[1], 0), PK(q[0], 0, q[0], 0)};
            } else {
                const u32x4 q = ldsr<u32x4>(tab + 16 * m);
                v = (u32x4){PK(q[3], 0, q[3], 1), PK(q[2], 0, q[2], 1), PK(q[1], 0, q[1], 1), PK(q[0], 0, q[0], 1)};
            }
            ldsw(R + 16 * g, v);
        }
        if (lane < 4) ldsw(R + 16 * (256 + lane), (u32x4){0u, 0u, 0u, 0u});
    }
#undef PK
    if (lane < 32) ldsw(R + SCH_U + 16 * lane, (u32x4){0u, 0u, 0u, 0u});
#pragma unroll
    for (int i = 0; i < 5; ++i) {
        const int g = lane + 64 * i;
        if (i < 4 || lane < 4) {
            const u32x4 a = ldsr<u32x4>(R + 16 * g); const u32x2 b = ldsr<u32x2>(R + 16 * g + 16);
            const unsigned a0 = __builtin_amdgcn_alignbit(a[1], a[0], 16), a1 = __builtin_amdgcn_alignbit(a[2], a[1], 16), a2 = __builtin_amdgcn_alignbit(a[3], a[2], 16),
                           a3 = __builtin_amdgcn_alignbit(b[0], a[3], 16), a4 = __builtin_amdgcn_alignbit(b[1], b[0], 16);
            ldsw(R + 4160 + 16 * g, (u32x4){a0, a1, a2, a3});
            ldsw(R + 8320 + 16 * g, (u32x4){a[1], a[2], a[3], b[0]});
            ldsw(R + 12480 + 16 * g, (u32x4){a1, a2, a3, a4});
        }
    }
    STAMP(5);
    waitc(L, C_U, 8);
    STAMP(6);
    f32x4 acc[8] = {};
    {
        const int b = lane & 15, kq = lane >> 4, c = (3 - b) & 3;
        const int pa0 = c * 4160 + 2 * (2031 - b + 8 * kq - c), pb0 = SCH_U + 480 + 32 * b + 16 * kq;
        __builtin_amdgcn_s_setprio(S_PRIOB);
        conv_group<0>(L, R, acc, pa0, pb0); conv_group<1>(L, R, acc, pa0, pb0); conv_group<2>(L, R, acc, pa0, pb0); conv_group<3>(L, R, acc, pa0, pb0);
        __builtin_amdgcn_s_setprio(3);
    }
    PIN(acc[0]); PIN(acc[7]); STAMP(7);
#pragma unroll
    for (int nu = 0; nu < 8; ++nu)
        ldsw(R + 512 * nu + 32 * (lane & 15) + 8 * (lane >> 4), (u32x2){cvtpk(acc[nu][0], acc[nu][1]), cvtpk(acc[nu][2], acc[nu][3])});
    arrive(L, C_S2, lane); waitc(L, C_S2, 4);
    {
        u32x4 yv[4], uv4[4];
#pragma unroll
        for (int c = 0; c < 4; ++c) {
            yv[c] = ldsr<u32x4>(L + L_S + c * SCH + 2 * t0);
            uv4[c] = ldsr<u32x4>(L + L_S + c * SCH + SCH_U + 512 + 2 * t0);
        }
        const float dk[4] = {bflo(dsk[0]), bfhi(dsk[0]), bflo(dsk[1]), bfhi(dsk[1])};
        u32x4 res[4];
#pragma unroll
        for (int c = 0; c < 4; ++c)
#pragma unroll
            for (int e = 0; e < 4; ++e) {
                const unsigned sk = cvtpk(bflo(uv4[c][e]) * dk[c], bfhi(uv4[c][e]) * dk[c]);
                res[c][e] = cvtpk(bflo(yv[c][e]) + bflo(sk), bfhi(yv[c][e]) + bfhi(sk));
            }
#pragma unroll
        for (int e = 0; e < 4; ++e) {
            unsigned short* op = p.out + (size_t)(t0 + 2 * e) * (2 * DM) + DM + ch0;
            *(u32x2*)op = (u32x2){__builtin_amdgcn_perm(res[1][e], res[0][e], 0x05040100u), __builtin_amdgcn_perm(res[3][e], res[2][e], 0x05040100u)};
            *(u32x2*)(op + 2 * DM) = (u32x2){__builtin_amdgcn_perm(res[1][e], res[0][e], 0x07060302u), __builtin_amdgcn_perm(res[3][e], res[2][e], 0x07060302u)};
        }
    }
    STAMP(8);

}

__global__ __launch_bounds__(768) void fused_kernel(Params p) {
    extern __shared__ __attribute__((aligned(16))) char smem[];
    lds_p L = (lds_p)smem;
    const unsigned lds0 = (unsigned)(uintptr_t)smem;
    const int tid = threadIdx.x, lane = tid & 63, w = __builtin_amdgcn_readfirstlane(tid >> 6);
    const int bid = blockIdx.x, xcd = bid & 7, idx = bid >> 3, tb = 2 * xcd + (idx & 1), pi = idx >> 1;
    if (tid < 64) ((LAS unsigned*)(L + L_C))[tid] = 0u;
    __syncthreads();
    STAMP_DECL;
    STAMP(0);
    const __amdgpu_buffer_rsrc_t rs_ws = __builtin_amdgcn_make_buffer_rsrc((void*)p.ws, 0, WS_BYTES, RSRC_W3);

    if (w >= 8) {
        if (w == 10 && idx < 14) {
            unsigned long long pc;
            asm volatile("s_getpc_b64 %0" : "=s"(pc));
            const char* cb = (const char*)(pc & ~127ull) - CODE_BACK;
            unsigned a = 0;
#pragma unroll
            for (int i = 0; i < 4; ++i) if (128 * (lane + 64 * i) < CODE_BACK + CODE_FWD) a ^= *(const volatile unsigned*)(cb + 128 * (lane + 64 * i));
            asm volatile("" :: "v"(a));
        }
        if (w == 11 && idx < 14) {
            const char* wsb = p.ws; const char* ob = (const char*)p.out; const char* wob = (const char*)p.wo;
            unsigned a = 0;
            if (lane < 40) a ^= *(const volatile unsigned*)(wsb + WS_KV + 4096 * lane);
            if (lane < 36) a ^= *(const volatile unsigned*)(wsb + WS_O + tb * 147456 + 4096 * lane);
            if (lane < 36) a ^= *(const volatile unsigned*)(ob + (size_t)(128 * tb + 64 * (pi & 1)) * 2304 + 4096 * lane);
#pragma unroll
            for (int i = 0; i < 3; ++i) if (lane + 64 * i < 162) a ^= *(const volatile unsigned*)(wob + 4096 * (lane + 64 * i));
            asm volatile("" :: "v"(a));
        }
        if (idx >= 14 && !MK_NOSSM) ssm_wave(p, L, w - 8, lane, 72 * xcd + 4 * (idx - 14) STAMP_PASS);
        STAMP(1);
        STAMP_DUMP();
        return;
    }

    const unsigned tag = (unsigned)__builtin_amdgcn_readfirstlane(gload_sc1((const unsigned*)(p.ws + WS_EPOCH))) + 1u;
    const int c16 = lane & 15, kq = lane >> 4, r32 = lane & 31, h = lane >> 5;
    const int q4 = (lane >> 2) & 3, p4 = lane & 3;
    const int kh = w >> 2, wq4 = w & 3;

    bf16x8 qmA, qmB; unsigned qeA, qeB;
    {
        const int T0 = 128 * tb + 16 * w;
        const bool has_kv = pi < 5;
        const bool has_ssm = idx >= 14 && !MK_NOSSM;
        {
            const __amdgpu_buffer_rsrc_t rs_wq = __builtin_amdgcn_make_buffer_rsrc((void*)p.wq, 0, DM * DM * 2, RSRC_W3);
            int kl = (lane * 43) >> 8, g = lane - 6 * kl;
            const unsigned dst = lds0 + L_W + w * 6912;
#pragma unroll
            for (int i = 0; i < 7; ++i) {
                const int vo = swap23(72 * w + kl) * 1152 + 72 * pi + 16 * g;
                if (i < 6 || lane < 48) dma16(rs_wq, vo, dst + 1024 * i);
                g += 4; kl += 10; if (g >= 6) { g -= 6; kl += 1; }
            }
        }
        u32x2 kp0 = {}, vp0 = {}, kp1 = {}, vp1 = {};
        const int kr0 = 72 * w + lane, kr1 = 72 * w + 64 + (lane & 7);
        if (has_kv) {
            const __amdgpu_buffer_rsrc_t rs_wk = __builtin_amdgcn_make_buffer_rsrc((void*)p.wk, 0, DM * 18 * 2, RSRC_W3);
            const __amdgpu_buffer_rsrc_t rs_wv = __builtin_amdgcn_make_buffer_rsrc((void*)p.wv, 0, DM * 18 * 2, RSRC_W3);
            const int o0 = swap23(kr0) * 36 + 8 * pi, o1 = swap23(kr1) * 36 + 8 * pi;
            kp0 = __builtin_amdgcn_raw_buffer_load_b64(rs_wk, o0, 0, 0); vp0 = __builtin_amdgcn_raw_buffer_load_b64(rs_wv, o0, 0, 0);
            kp1 = __builtin_amdgcn_raw_buffer_load_b64(rs_wk, o1, 0, 0); vp1 = __builtin_amdgcn_raw_buffer_load_b64(rs_wv, o1, 0, 0);
        }
        SBAR();
        arrive(L, C_D, lane);
        waitc(L, C_D, 8);
        u32x4 xa[9], xb[9];
        {
            const unsigned short* xp = p.x + (size_t)(T0 + (lane >> 3)) * DM + 8 * (lane & 7);
#pragma unroll
            for (int s = 0; s < 9; ++s) { xa[s] = *(const u32x4*)(xp + 64 * s); xb[s] = *(const u32x4*)(xp + 64 * s + 8 * DM); }
        }
        SBAR();
        asm volatile("s_waitcnt vmcnt(18)" ::: "memory");
        SBAR();
        if (has_kv) {
            ldsw(L + L_W + kr0 * 96 + 80, (u32x4){kp0[0], kp0[1], vp0[0], vp0[1]});
            if (lane < 8) ldsw(L + L_W + kr1 * 96 + 80, (u32x4){kp1[0], kp1[1], vp1[0], vp1[1]});
        }
        arrive(L, C_W, lane);
        f32x4 D0 = {}, D1 = {}, D2 = {}, D2t = {};
        bf16x8 xf[18];
        lds_p wb = L + L_W + (16 * (kq >> 1) + 4 * (kq & 1) + q4) * 96 + 8 * p4;
        {
            lds_p xs = L + L_Q + w * 2048;
            lds_p xw = xs + (lane >> 3) * 128 + 16 * ((lane & 7) ^ (lane >> 4));
            lds_p xw2 = xs + 1024 + (lane >> 3) * 128 + 16 * ((lane & 7) ^ (4 + (lane >> 4)));
            lds_p xr0 = xs + c16 * 128 + 16 * (kq ^ ((c16 >> 1) & 7)), xr1 = xs + c16 * 128 + 16 * ((4 + kq) ^ ((c16 >> 1) & 7));
            ldsw(xw, xa[0]); ldsw(xw2, xb[0]); xf[0] = ldsr<bf16x8>(xr0); xf[1] = ldsr<bf16x8>(xr1);
            waitc(L, C_W, 8);
            STAMP(1);
#pragma unroll
            for (int s = 0; s < 9; ++s) {
                if (s < 8) { ldsw(xw, xa[s + 1]); ldsw(xw2, xb[s + 1]); xf[2 * s + 2] = ldsr<bf16x8>(xr0); xf[2 * s + 3] = ldsr<bf16x8>(xr1); }
#pragma unroll
                for (int e = 0; e < 2; ++e) {
                    const int ks = 2 * s + e;
                    const bf16x8 w2 = cat8(ldtr(wb + ks * 3072 + 64), ldtr(wb + ks * 3072 + 768 + 64));
                    D2 = MF16(w2, xf[ks], D2);
                    D2t = MF16(xf[ks], w2, D2t);
                }
                SBAR();
            }
        }
        STAMP(2);
        if (has_kv) {
            const int kt = 4 * tb + (w >> 1), rr = 16 * (w & 1) + c16, s16 = w & 1, tbase = WS_KV + kt * KVT;
            if (kq == 2) {
                u32x2 v = {cvtpk(D2[0], D2[1]), pi == 4 ? 0x3F803F80u : cvtpk(D2[2], D2[3])};
                const int off = pi == 4 ? 2304 + rr * 8 : ((pi >> 1) * 32 + rr) * 16 + (pi & 1) * 8;
                __builtin_amdgcn_raw_buffer_store_b64(v, rs_ws, tbase + off, 0, AUX_SC1);
            }
            if (c16 >= 12) {
                const int dv = 4 * pi + c16 - 12;
                u32x2 v = {cvtpk(D2t[0], D2t[1]), cvtpk(D2t[2], D2t[3])};
                if (dv == 18) v = (u32x2){0x3F803F80u, 0x3F803F80u};
                if (dv == 19) v = (u32x2){0u, 0u};
                __builtin_amdgcn_raw_buffer_store_b64(v, rs_ws, tbase + 1024 + ((s16 * 2 + (kq & 1)) * 20 + dv) * 16 + (kq >> 1) * 8, 0, AUX_SC1);
            }
        }
        if (has_kv) {
            DRAIN(); arrive(L, C_P, lane);
            if (w == 0) {
                waitc(L, C_P, 8);
                if (lane < 8) gstore_sc1((unsigned*)(p.ws + WS_FLAG1 + lane * 2048 + tb * 128 + pi * 4), tag);
            }
        }
        STAMP(8);
        SBAR();
#pragma unroll
        for (int ks = 0; ks < 18; ++ks) {
            const bf16x8 w0 = cat8(ldtr(wb + ks * 3072), ldtr(wb + ks * 3072 + 768));
            const bf16x8 w1 = cat8(ldtr(wb + ks * 3072 + 32), ldtr(wb + ks * 3072 + 768 + 32));
            D0 = MF16(w0, xf[ks], D0); D1 = MF16(w1, xf[ks], D1);
        }
        {
            lds_p qs = L + L_Q + w * 2048;
            ldsw(qs + c16 * 48 + 8 * kq, (u32x2){cvtpk(D0[0] * QSCALE, D0[1] * QSCALE), cvtpk(D0[2] * QSCALE, D0[3] * QSCALE)});
            const int a0 = kq == 0 ? c16 * 48 + 32 : (16 + c16) * 48 + 2 * (4 * kq - 2);
            const int a1 = (16 + c16) * 48 + 8 * kq;
            ldsw(qs + a0, cvtpk(D1[0] * QSCALE, D1[1] * QSCALE));
            ldsw(qs + a1, cvtpk(D1[2] * QSCALE, D1[3] * QSCALE));
            if (kq == 0) {
                ldsw(qs + (16 + c16) * 48 + 28, cvtpk(D2[0] * QSCALE, D2[1] * QSCALE));
                ldsw(qs + (16 + c16) * 48 + 32, cvtpk(D2[2] * QSCALE, D2[3] * QSCALE));
            }
        }
        arrive(L, C_Q, lane);
        if (has_ssm) {
            u32x2 uu[4];
#pragma unroll
            for (int e = 0; e < 4; ++e) uu[e] = *(const u32x2*)(p.u + (size_t)(256 * w + 4 * lane + e) * DM + 72 * xcd + 4 * (idx - 14));
            const int uo = SCH_U + 512 + 2 * (256 * w + 4 * lane);
#pragma unroll
            for (int c = 0; c < 4; ++c) {
                const unsigned sel = (c & 1) ? 0x07060302u : 0x05040100u;
                ldsw(L + L_S + c * SCH + uo, (u32x2){__builtin_amdgcn_perm(uu[1][c >> 1], uu[0][c >> 1], sel), __builtin_amdgcn_perm(uu[3][c >> 1], uu[2][c >> 1], sel)});
            }
            arrive(L, C_U, lane);
        }
        {
            if (w == 7) {
                const unsigned* f = (const unsigned*)(p.ws + WS_FLAG1 + xcd * 2048);
                const int i0 = lane, i1 = 64 + (lane & 15);
                const unsigned* f0 = f + (i0 / 5) * 32 + i0 % 5; const unsigned* f1 = f + (i1 / 5) * 32 + i1 % 5;
                for (unsigned spins = 0; spins < SPIN_GLB; ++spins) {
                    const unsigned a0 = gload_sc1(f0), a1 = gload_sc1(f1); const bool ok = (a0 == tag) & (a1 == tag);
                    if (__all(ok)) break;
                    __builtin_amdgcn_s_sleep(2);
                }
                __builtin_amdgcn_fence(__ATOMIC_ACQUIRE, "wavefront");
                arrive(L, C_F1, lane);
            }
        }
        waitc(L, C_Q, 8);
        {
            lds_p qa = L + L_Q + wq4 * 2048 + r32 * 48, qb = qa + 4 * 2048;
            qmA = ldsr<bf16x8>(qa + 16 * h); qmB = ldsr<bf16x8>(qb + 16 * h);
            qeA = ldsr<unsigned>(qa + 32); qeB = ldsr<unsigned>(qb + 32);
        }
        waitc(L, C_F1, 1);
        STAMP(3);
    }

    {
        lds_p ring = L + L_W + kh * (8 * KVT);
        const int dmo = WS_KV + (32 * kh + wq4) * KVT + lane * 16;
        const unsigned dmd = lds0 + L_W + kh * (8 * KVT) + wq4 * KVT;
#define KV_DMA_A(cc) do { dma16_sc1(rs_ws, dmo + (cc) * (4 * KVT), dmd + ((cc) & 1) * (4 * KVT)); dma16_sc1(rs_ws, dmo + (cc) * (4 * KVT) + 1024, dmd + ((cc) & 1) * (4 * KVT) + 1024); } while (0)
#define KV_DMA_B(cc) do { if (lane < 32) dma16_sc1(rs_ws, dmo + (cc) * (4 * KVT) + 2048, dmd + ((cc) & 1) * (4 * KVT) + 2048); } while (0)
        lds_p kb = ring + lane * 16, eb = ring + 2304 + r32 * 8, vb = ring + 1024 + (h * 20 + (r32 < 19 ? r32 : 19)) * 16;
        const f32x16 Z = {};
        unsigned mA = 0, mB = 0;
        f32x16 OA, OB;
        for (int pass = 0; pass < 2; ++pass) {
            KV_DMA_A(0); KV_DMA_B(0); KV_DMA_A(1); KV_DMA_B(1);
            asm volatile("s_waitcnt vmcnt(3)" ::: "memory");
            arrive(L, C_RW, lane); waitc(L, C_RW, 8 * (pass + 1));
            STAMP(9); CSTAMP(10);
            if (pass == 0) {
                const bf16x8 km = ldsr<bf16x8>(L + L_W + lane * 16); const bf16x4 ke = ldsr<bf16x4>(L + L_W + 2304 + r32 * 8);
                const u32x2 ea = {h ? 0u : qeA, 0u}, ebq = {h ? 0u : qeB, 0u};
                f32x16 sa = MF32(km, qmA, Z), sb = MF32(km, qmB, Z);
                sa = MF32E(ke, __builtin_bit_cast(bf16x4, ea), sa); sb = MF32E(ke, __builtin_bit_cast(bf16x4, ebq), sb);
                float ma = sa[0], mb = sb[0];
#pragma unroll
                for (int i = 1; i < 16; ++i) { ma = fmaxf(ma, sa[i]); mb = fmaxf(mb, sb[i]); }
                auto ra = __builtin_amdgcn_permlane32_swap(__float_as_uint(ma), __float_as_uint(ma), false, false);
                auto rb = __builtin_amdgcn_permlane32_swap(__float_as_uint(mb), __float_as_uint(mb), false, false);
                ma = fmaxf(__uint_as_float(ra[0]), __uint_as_float(ra[1])); mb = fmaxf(__uint_as_float(rb[0]), __uint_as_float(rb[1]));
                const float ah = bflo(cvtpk(ma, 0.f)), bh = bflo(cvtpk(mb, 0.f));
                mA = cvtpk(-ah, ah - ma); mB = cvtpk(-bh, bh - mb);
            }
            const u32x2 ea = {h ? 0u : qeA, h ? 0u : mA}, ebq = {h ? 0u : qeB, h ? 0u : mB};
            const bf16x4 qxA = __builtin_bit_cast(bf16x4, ea), qxB = __builtin_bit_cast(bf16x4, ebq);
            f32x16 SA, SB = Z; u32x4 pa0, pa1, pb0 = {}, pb1 = {}; bf16x8 km, vx0, vx1, vy0, vy1; bf16x4 ke;
            OA = Z; OB = Z;
            km = ldsr<bf16x8>(kb); ke = ldsr<bf16x4>(eb);
            SA = MF32(km, qmA, Z); SA = MF32E(ke, qxA, SA);
            vx0 = ldsr<bf16x8>(vb); vx1 = ldsr<bf16x8>(vb + 640);
#define PH(SC, SN, QM, QX, ON, pp0, pp1, pn0, pn1, V0, V1, LD1, LD2, LD4) do { \
            ON = MF32(V0, BC8(pp0), ON); SBAR(); \
            LD1; \
            SC[0] = EXP2(SC[0]); SC[1] = EXP2(SC[1]); SC[2] = EXP2(SC[2]); SC[3] = EXP2(SC[3]); SBAR(); \
            SN = MF32(km, QM, Z); SBAR(); \
            LD2; \
            SC[4] = EXP2(SC[4]); SC[5] = EXP2(SC[5]); pn0[0] = cvtpk(SC[0], SC[1]); SC[6] = EXP2(SC[6]); SC[7] = EXP2(SC[7]); pn0[1] = cvtpk(SC[2], SC[3]); SBAR(); \
            ON = MF32(V1, BC8(pp1), ON); SBAR(); \
            SC[8] = EXP2(SC[8]); SC[9] = EXP2(SC[9]); pn0[2] = cvtpk(SC[4], SC[5]); SC[10] = EXP2(SC[10]); SC[11] = EXP2(SC[11]); pn0[3] = cvtpk(SC[6], SC[7]); PIN(pn0); SBAR(); \
            SN = MF32E(ke, QX, SN); SBAR(); \
            LD4; \
            SC[12] = EXP2(SC[12]); SC[13] = EXP2(SC[13]); pn1[0] = cvtpk(SC[8], SC[9]); SC[14] = EXP2(SC[14]); SC[15] = EXP2(SC[15]); pn1[1] = cvtpk(SC[10], SC[11]); \
            pn1[2] = cvtpk(SC[12], SC[13]); pn1[3] = cvtpk(SC[14], SC[15]); PIN(pn1); SBAR(); \
        } while (0)
#define VRD(t, N0, N1) (N0 = ldsr<bf16x8>(vb + (t) * KVT), N1 = ldsr<bf16x8>(vb + (t) * KVT + 640))
#define KMRD(t) (km = ldsr<bf16x8>(kb + (((t) + 1) & 7) * KVT))
#define KERD(t) (ke = ldsr<bf16x4>(eb + (((t) + 1) & 7) * KVT))
#define PHA(P0, P1, LD1, LD2, LD4) PH(SA, SB, qmB, qxB, OB, pb0, pb1, pa0, pa1, P0, P1, LD1, LD2, LD4);
#define PHB(N0, N1, LD1, LD2, LD4) PH(SB, SA, qmA, qxA, OA, pa0, pa1, pb0, pb1, N0, N1, LD1, LD2, LD4);
#define TA(t) PHA(vx0, vx1, VRD(t, vy0, vy1), KMRD(t), KERD(t))
#define TB() PHB(vy0, vy1, (void)0, (void)0, (void)0)
#define UA(t) PHA(vy0, vy1, VRD(t, vx0, vx1), KMRD(t), KERD(t))
#define UB() PHB(vx0, vx1, (void)0, (void)0, (void)0)
#define PRIO(x) __builtin_amdgcn_s_setprio(x);
#define LAST(t, b) \
            PHA(vy0, vy1, VRD(t, vx0, vx1), (void)0, (void)0) \
            asm volatile("s_waitcnt vmcnt(0)" ::: "memory"); \
            if ((b) < 7) { if (it == 0 && pass == 0) { arrive(L, C_B, lane); waitc(L, C_B, 8 * ((b) + 1)); } else __builtin_amdgcn_s_barrier(); } \
            CBAR(); PRIO(3) KMRD(t); KERD(t); SBAR(); \
            PHB(vx0, vx1, (void)0, if ((b) + 2 < 8) KV_DMA_A((b) + 2), if ((b) + 2 < 8) KV_DMA_B((b) + 2))
            PRIO(3)
            for (int it = 0; it < 4; ++it) {
                TA(0) PRIO(2) TB() UA(1) PRIO(1) UB() TA(2) PRIO(0) TB()
                LAST(3, 2 * it)
                if (it == 1 && pass == 0) {
                    const __amdgpu_buffer_rsrc_t rs_wo = __builtin_amdgcn_make_buffer_rsrc((void*)p.wo, 0, DM * DM * 2, RSRC_W3);
                    int kp = 64 * w + lane; const int k9 = (kp * 7282) >> 16; int g = kp - 9 * k9; kp = k9;
                    for (int inst = w; inst < 81; inst += 8) {
                        dma16(rs_wo, swap23(kp) * 1152 + 144 * (pi >> 1) + 16 * g, lds0 + L_S + 1024 * inst);
                        g += 8; kp += 56; if (g >= 9) { g -= 9; kp += 1; }
                    }
                }
                TA(4) PRIO(2) TB() UA(5) PRIO(1) UB() TA(6) PRIO(0) TB()
                LAST(7, 2 * it + 1)
            }
            OB = MF32(vx0, BC8(pb0), OB); OB = MF32(vx1, BC8(pb1), OB);
#undef TA
#undef TB
#undef UA
#undef UB
#undef LAST
#undef PHA
#undef PHB
#undef VRD
#undef KMRD
#undef KERD
#undef PH
            PRIO(0)
            STAMP(4); CSTAMP(11);
            lds_p xo = (w < 4 ? L + L_X1 : L + L_Q) + wq4 * 3072, xi = (w < 4 ? L + L_Q : L + L_X1) + wq4 * 3072;
            {
                const f32x16& S = kh ? OA : OB;
#pragma unroll
                for (int g = 0; g < 3; ++g) ldsw(xo + g * 1024 + lane * 16, (f32x4){S[4 * g], S[4 * g + 1], S[4 * g + 2], S[4 * g + 3]});
            }
            BARRIER();
            float o[12];
#pragma unroll
            for (int g = 0; g < 3; ++g) {
                const f32x4 t = ldsr<f32x4>(xi + g * 1024 + lane * 16);
#pragma unroll
                for (int i = 0; i < 4; ++i) o[4 * g + i] = (kh ? OB[4 * g + i] : OA[4 * g + i]) + t[i];
            }
            const float lsum = __uint_as_float(__builtin_amdgcn_permlane32_swap(__float_as_uint(o[10]), __float_as_uint(o[10]), false, false)[0]);
            const float rl = __builtin_amdgcn_rcpf(lsum);
            bool bad = !(lsum < 1.2676506e30f);
#pragma unroll
            for (int i = 0; i < 8; ++i) bad |= !(fabsf(o[i]) < 3.0e38f);
            if (h == 0) bad |= !(fabsf(o[8]) < 3.0e38f) || !(fabsf(o[9]) < 3.0e38f);
            {
                lds_p os = xi;
                lds_p ob = os + (r32 & 15) * 72 + (r32 >> 4) * 36 + 8 * h;
                ldsw(ob, cvtpk(o[0] * rl, o[1] * rl)); ldsw(ob + 4, cvtpk(o[2] * rl, o[3] * rl));
                ldsw(ob + 16, cvtpk(o[4] * rl, o[5] * rl)); ldsw(ob + 20, cvtpk(o[6] * rl, o[7] * rl));
                if (h == 0) ldsw(ob + 32, cvtpk(o[8] * rl, o[9] * rl));
#pragma unroll
                for (int i = 0; i < 3; ++i) {
                    const int e = lane + 64 * i;
                    if (e < 144) {
                        const int tk = e & 15, pc = e >> 4, k0 = 36 * pi + 4 * pc;
                        const u32x2 v = ldsr<u32x2>(os + tk * 72 + pc * 8);
                        __builtin_amdgcn_raw_buffer_store_b64(v, rs_ws, WS_O + (8 * tb + w) * 18432 + (k0 >> 3) * 256 + tk * 16 + (k0 & 4) * 2, 0, AUX_SC1);
                    }
                }
            }
            if (pass == 0 && __any(bad) && lane == 0) ((volatile LAS unsigned*)(L + L_C))[C_BAD] = 1u;
            DRAIN();
            BARRIER();
            const bool redo = pass == 0 && __builtin_amdgcn_readfirstlane(((volatile LAS unsigned*)(L + L_C))[C_BAD]) != 0;
            if (!redo) break;
            {
                const u32x2 ea0 = {h ? 0u : qeA, 0u}, eb0 = {h ? 0u : qeB, 0u};
                float ma = -INFINITY, mb = -INFINITY;
                for (int t = 0; t < 64; ++t) {
                    const u32x4 kmv = __builtin_amdgcn_raw_buffer_load_b128(rs_ws, WS_KV + t * KVT + lane * 16, 0, AUX_SC1);
                    const u32x2 kev = __builtin_amdgcn_raw_buffer_load_b64(rs_ws, WS_KV + t * KVT + 2304 + r32 * 8, 0, AUX_SC1);
                    f32x16 sa = MF32(BC8(kmv), qmA, Z), sb = MF32(BC8(kmv), qmB, Z);
                    sa = MF32E(__builtin_bit_cast(bf16x4, kev), __builtin_bit_cast(bf16x4, ea0), sa);
                    sb = MF32E(__builtin_bit_cast(bf16x4, kev), __builtin_bit_cast(bf16x4, eb0), sb);
#pragma unroll
                    for (int i = 0; i < 16; ++i) { ma = fmaxf(ma, sa[i]); mb = fmaxf(mb, sb[i]); }
                }
                ma = fmaxf(ma, __shfl_xor(ma, 32)); mb = fmaxf(mb, __shfl_xor(mb, 32));
                const float ah = bflo(cvtpk(ma, 0.f)), bh = bflo(cvtpk(mb, 0.f));
                mA = cvtpk(-ah, ah - ma); mB = cvtpk(-bh, bh - mb);
            }
        }
#undef KV_DMA_A
#undef KV_DMA_B
#undef PRIO
        if (w == 0 && lane == 0) gstore_sc1((unsigned*)(p.ws + WS_FLAG2 + tb * 128 + pi * 4), tag);
        STAMP(5);
    }

    {
        if (w == 7) {
            const unsigned* f = (const unsigned*)(p.ws + WS_FLAG2 + tb * 128) + (lane & 15);
            for (unsigned spins = 0; spins < SPIN_GLB; ++spins) {
                if (__all(gload_sc1(f) == tag)) break;
                __builtin_amdgcn_s_sleep(1);
            }
            __builtin_amdgcn_fence(__ATOMIC_ACQUIRE, "wavefront");
        }
        BARRIER();
        STAMP(6);
        const int Tt = 128 * tb + 64 * (pi & 1) + 16 * wq4, F0 = 72 * (pi >> 1);
        u32x4 of[9];
#pragma unroll
        for (int i = 0; i < 9; ++i) of[i] = __builtin_amdgcn_raw_buffer_load_b128(rs_ws, WS_O + (Tt >> 4) * 18432 + (9 * kh + i) * 1024 + lane * 16, 0, AUX_SC1);
        f32x4 D[5] = {};
        lds_p wb = L + L_S + (16 * (kq >> 1) + 4 * (kq & 1) + q4) * 144 + 8 * p4 + kh * (9 * 32 * 144);
#pragma unroll
        for (int i = 0; i < 9; ++i)
#pragma unroll
            for (int T = 0; T < 5; ++T)
                D[T] = MF16(cat8(ldtr(wb + i * 4608 + 32 * T), ldtr(wb + i * 4608 + 1152 + 32 * T)), BC8(of[i]), D[T]);
        lds_p xo = L + L_W + w * 3072, xi = L + L_W + (w ^ 4) * 3072;
        if (kh == 0) { ldsw(xo + lane * 16, D[3]); ldsw(xo + 1024 + lane * 16, D[4]); }
        else { ldsw(xo + lane * 16, D[0]); ldsw(xo + 1024 + lane * 16, D[1]); ldsw(xo + 2048 + lane * 16, D[2]); }
        BARRIER();
        unsigned short* op = p.out + (size_t)(Tt + c16) * (2 * DM) + F0 + 4 * kq;
        if (kh == 0) {
#pragma unroll
            for (int T = 0; T < 3; ++T) {
                const f32x4 v = D[T] + ldsr<f32x4>(xi + T * 1024 + lane * 16);
                *(u32x2*)(op + 16 * T) = (u32x2){cvtpk(v[0], v[1]), cvtpk(v[2], v[3])};
            }
        } else {
#pragma unroll
            for (int T = 3; T < 5; ++T) {
                const f32x4 v = D[T] + ldsr<f32x4>(xi + (T - 3) * 1024 + lane * 16);
                if (T == 3 || kq < 2) *(u32x2*)(op + 16 * T) = (u32x2){cvtpk(v[0], v[1]), cvtpk(v[2], v[3])};
            }
        }
        STAMP(7);
        STAMP_DUMP();
        if (bid == 0 && w == 7) {
            const unsigned* f = (const unsigned*)(p.ws + WS_FLAG2);
            for (unsigned spins = 0; spins < SPIN_GLB; ++spins) {
                bool ok = true;
#pragma unroll
                for (int i = 0; i < 4; ++i) { const int e = lane + 64 * i; ok &= gload_sc1(f + (e >> 4) * 32 + (e & 15)) == tag; }
                if (__all(ok)) break;
                __builtin_amdgcn_s_sleep(2);
            }
            if (lane == 0) gstore_sc1((unsigned*)(p.ws + WS_EPOCH), tag);
        }
    }
}

}

extern "C" void kernel_launch(void* const* d_in, const int* in_sizes, int n_in, void* d_out, int out_size, void* d_ws, size_t ws_size,
                              hipStream_t stream) {
    Params p;
    p.x = (const unsigned short*)d_in[0]; p.wq = (const unsigned short*)d_in[1]; p.wk = (const unsigned short*)d_in[2];
    p.wv = (const unsigned short*)d_in[3]; p.wo = (const unsigned short*)d_in[4]; p.u = (const unsigned short*)d_in[5];
    p.are = (const unsigned short*)d_in[6]; p.aim = (const unsigned short*)d_in[7]; p.bre = (const unsigned short*)d_in[8];
    p.bim = (const unsigned short*)d_in[9]; p.cre = (const unsigned short*)d_in[10]; p.cim = (const unsigned short*)d_in[11];
    p.ldt = (const unsigned short*)d_in[12]; p.dsk = (const unsigned short*)d_in[13];
    p.out = (unsigned short*)d_out; p.ws = (char*)d_ws;
    (void)hipFuncSetAttribute((const void*)fused_kernel, hipFuncAttributeMaxDynamicSharedMemorySize, L_END);
    fused_kernel<<<256, 768, L_END, stream>>>(p);
}
```

```cpp
#include <hip/hip_runtime.h>
#include <hip/hip_bf16.h>
#include <stdint.h>

#define S_PRIO0 0
#define U_BY_S 0
#define S_PRIOB 0
#define S_PRIOC 3
#define MK_NOSSM 0
#define MK_STAMPS 0

namespace {

typedef __attribute__((ext_vector_type(16))) float f32x16;
typedef __attribute__((ext_vector_type(4))) float f32x4;
typedef __attribute__((ext_vector_type(2))) float f32x2;
typedef __attribute__((ext_vector_type(8))) short bf16x8;
typedef __attribute__((ext_vector_type(4))) short bf16x4;
typedef __attribute__((ext_vector_type(2))) __bf16 bf16x2_t;
typedef __attribute__((ext_vector_type(4))) unsigned u32x4;
typedef __attribute__((ext_vector_type(2))) unsigned u32x2;
typedef short v4i16_t __attribute__((ext_vector_type(4)));
#define LAS __attribute__((address_space(3)))
#define GAS __attribute__((address_space(1)))
typedef LAS char* lds_p;
typedef const unsigned short* cbf;

constexpr int SEQ = 2048, DM = 576;
constexpr float QSCALE = 0.34005040627f;
constexpr float LOG2E = 1.4426950408889634f;
constexpr float INV2PI = 0.15915494309189535f;

constexpr int WS_EPOCH = 0;
constexpr int WS_FLAG1 = 4096;
constexpr int WS_FLAG2 = WS_FLAG1 + 32768;
constexpr int WS_KV = WS_FLAG2 + 32768;
constexpr int KVT = 2560;
constexpr int WS_O = WS_KV + 64 * KVT;
constexpr int WS_BYTES = WS_O + SEQ * DM * 2;
constexpr int WS_STAMP = 8 * 1024 * 1024;

constexpr int L_W = 0;
constexpr int L_X1 = 40960;
constexpr int L_S = 55296;
constexpr int L_Q = L_S + 87360;
constexpr int L_C = L_Q + 16384;
constexpr int L_END = L_C + 256;
constexpr int SCH = 21840;
constexpr int SCH_U = 16640;
enum { C_W = 0, C_P, C_Q, C_F1, C_RW, C_B, C_BAD, C_U, C_S2, C_D, C_KW, C_X, C_NUM };

constexpr int RSRC_W3 = 0x00020000;
constexpr int AUX_SC1 = 16;
constexpr unsigned SPIN_LDS = 1u << 20, SPIN_GLB = 1u << 16;

#define SBAR() __builtin_amdgcn_sched_barrier(0)
#define PIN(x) asm volatile("" : "+v"(x))
#define CBAR() asm volatile("" ::: "memory")
#define DRAIN() asm volatile("s_waitcnt vmcnt(0)" ::: "memory")
#define MF32(a, b, c) __builtin_amdgcn_mfma_f32_32x32x16_bf16(a, b, c, 0, 0, 0)
#define MF32E(a, b, c) __builtin_amdgcn_mfma_f32_32x32x8bf16_1k(a, b, c, 0, 0, 0)
#define MF16(a, b, c) __builtin_amdgcn_mfma_f32_16x16x32_bf16(a, b, c, 0, 0, 0)
#define EXP2(x) __builtin_amdgcn_exp2f(x)
#define BC8(x) __builtin_bit_cast(bf16x8, x)
#define DI __device__ __forceinline__

DI unsigned cvtpk(float lo, float hi) { f32x2 v = {lo, hi}; return __builtin_bit_cast(unsigned, __builtin_convertvector(v, bf16x2_t)); }
DI float bflo(unsigned x) { return __builtin_bit_cast(float, x << 16); }
DI float bfhi(unsigned x) { return __builtin_bit_cast(float, x & 0xffff0000u); }
DI float bf1(unsigned short x) { return __builtin_bit_cast(float, (unsigned)x << 16); }
template <class T> DI T ldsr(lds_p p) { return *(const LAS T*)p; }
template <class T> DI void ldsw(lds_p p, T v) { *(LAS T*)p = v; }
DI bf16x4 ldtr(lds_p p) { return __builtin_bit_cast(bf16x4, __builtin_amdgcn_ds_read_tr16_b64_v4i16((LAS v4i16_t*)p)); }
DI bf16x8 cat8(bf16x4 a, bf16x4 b) { return __builtin_shufflevector(a, b, 0, 1, 2, 3, 4, 5, 6, 7); }
DI int swap23(int k) { return (k & ~12) | ((k & 4) << 1) | ((k & 8) >> 1); }
DI void dma16(__amdgpu_buffer_rsrc_t rs, int voff, unsigned lds_base) {
    unsigned keep;
    asm volatile("s_mov_b32 %0, m0\n\ts_mov_b32 m0, %3\n\ts_nop 0\n\tbuffer_load_dwordx4 %1, %2, 0 offen lds\n\ts_mov_b32 m0, %0"
                 : "=&s"(keep) : "v"(voff), "s"(rs), "s"(lds_base) : "memory");
}
DI void dma16_sc1(__amdgpu_buffer_rsrc_t rs, int voff, unsigned lds_base) {
    unsigned keep;
    asm volatile("s_mov_b32 %0, m0\n\ts_mov_b32 m0, %3\n\ts_nop 0\n\tbuffer_load_dwordx4 %1, %2, 0 offen sc1 lds\n\ts_mov_b32 m0, %0"
                 : "=&s"(keep) : "v"(voff), "s"(rs), "s"(lds_base) : "memory");
}
#define BARRIER() asm volatile("s_waitcnt lgkmcnt(0)\n\ts_barrier" ::: "memory")
DI void arrive(lds_p L, int c, int lane) {
    (void)lane;
    unsigned long long keep;
    asm volatile("s_mov_b64 %0, exec\n\ts_mov_b64 exec, 1\n\tds_add_u32 %1, %2\n\ts_mov_b64 exec, %0" : "=&s"(keep) : "v"((unsigned)(uintptr_t)(L + L_C + 4 * c)), "v"(1u) : "memory");
}
DI void waitc(lds_p L, int c, unsigned target) {
    CBAR();
    volatile LAS unsigned* p = (volatile LAS unsigned*)(L + L_C) + c;
    for (unsigned spins = 0; spins < SPIN_LDS; ++spins) {
        if ((unsigned)__builtin_amdgcn_readfirstlane(*p) >= target) break;
        __builtin_amdgcn_s_sleep(1);
    }
    CBAR();
}
DI unsigned peek(lds_p L, int c) { return *((volatile LAS unsigned*)(L + L_C) + c); }
DI void waitp(lds_p L, int c, unsigned seen, unsigned target) { if ((unsigned)__builtin_amdgcn_readfirstlane(seen) < target) waitc(L, c, target); CBAR(); }
DI unsigned gload_sc1(const unsigned* p) { return __hip_atomic_load((GAS unsigned*)p, __ATOMIC_RELAXED, __HIP_MEMORY_SCOPE_AGENT); }
DI void gstore_sc1(unsigned* p, unsigned v) { __hip_atomic_store((GAS unsigned*)p, v, __ATOMIC_RELAXED, __HIP_MEMORY_SCOPE_AGENT); }

struct cplx2 { f32x2 re, im; };
DI cplx2 cmul(const cplx2& a, const cplx2& b) { cplx2 r; r.re = a.re * b.re - a.im * b.im; r.im = a.re * b.im + a.im * b.re; return r; }
DI cplx2 cexp(f32x2 ar2, f32x2 airev, float e) {
    f32x2 m = ar2 * e, t = airev * e; cplx2 r;
#pragma unroll
    for (int i = 0; i < 2; ++i) {
        const float mag = EXP2(m[i]), fr = __builtin_amdgcn_fractf(t[i]);
        r.re[i] = mag * __builtin_amdgcn_cosf(fr); r.im[i] = mag * __builtin_amdgcn_sinf(fr);
    }
    return r;
}

struct Params {
    const unsigned short *x, *wq, *wk, *wv, *wo, *u, *are, *aim, *bre, *bim, *cre, *cim, *ldt, *dsk;
    unsigned short* out;
    char* ws;
};

#define STAMP(i) do {} while (0)
#define CSTAMP(i) do {} while (0)
#define STAMP_DECL do {} while (0)
#define STAMP_DUMP() do {} while (0)
#define STAMP_ARG
#define STAMP_PASS

DI void dma16s(__amdgpu_buffer_rsrc_t rs, int voff, int soff, unsigned lds_base) {
    unsigned keep;
    asm volatile("s_mov_b32 %0, m0\n\ts_mov_b32 m0, %4\n\ts_nop 0\n\tbuffer_load_dwordx4 %1, %2, %3 offen lds\n\ts_mov_b32 m0, %0"
                 : "=&s"(keep) : "v"(voff), "s"(rs), "s"(soff), "s"(lds_base) : "memory");
}
DI void kv_wave(const Params& p, lds_p L, unsigned lds0, __amdgpu_buffer_rsrc_t rs_ws, int s, int lane, int xcd, int tt STAMP_ARG) {
    __builtin_amdgcn_s_setprio(3);
    const unsigned epoch = gload_sc1((const unsigned*)(p.ws + WS_EPOCH));
    const int T0 = 256 * xcd + 16 * tt;
    {
        const __amdgpu_buffer_rsrc_t rs_wk = __builtin_amdgcn_make_buffer_rsrc((void*)p.wk, 0, DM * 18 * 2, RSRC_W3);
        const __amdgpu_buffer_rsrc_t rs_wv = __builtin_amdgcn_make_buffer_rsrc((void*)p.wv, 0, DM * 18 * 2, RSRC_W3);
        int vo[3];
#pragma unroll
        for (int j = 0; j < 3; ++j) { const int sg = 64 * j + lane, kp = (sg * 171) >> 9; vo[j] = swap23(kp) * 36 + 16 * (sg - 3 * kp); }
        for (int b = s; b < 9; b += 4) {
#pragma unroll
            for (int j = 0; j < 3; ++j) {
                dma16s(rs_wk, vo[j], 2304 * b, lds0 + L_S + 3072 * b + 1024 * j);
                dma16s(rs_wv, vo[j], 2304 * b, lds0 + L_S + 27648 + 3072 * b + 1024 * j);
            }
        }
    }
    SBAR();
    u32x4 xa[9], xb[9];
    {
        const unsigned short* xp = p.x + (size_t)(T0 + (lane >> 3)) * DM + 8 * (lane & 7);
#pragma unroll
        for (int i = 0; i < 9; ++i) { xa[i] = *(const u32x4*)(xp + 64 * i); xb[i] = *(const u32x4*)(xp + 64 * i + 8 * DM); }
    }
    SBAR();
    BARRIER();
    arrive(L, C_D, lane);
    STAMP(3);
    asm volatile("s_waitcnt vmcnt(18)" ::: "memory");
    arrive(L, C_KW, lane);
    const int c16 = lane & 15, kq = lane >> 4, q4 = (lane >> 2) & 3, p4 = lane & 3;
    lds_p xs = L + L_S + 55296 + s * 2048;
    lds_p xw = xs + (lane >> 3) * 128 + 16 * ((lane & 7) ^ (lane >> 4)), xw2 = xs + 1024 + (lane >> 3) * 128 + 16 * ((lane & 7) ^ (4 + (lane >> 4)));
    lds_p xr0 = xs + c16 * 128 + 16 * (kq ^ ((c16 >> 1) & 7)), xr1 = xs + c16 * 128 + 16 * ((4 + kq) ^ ((c16 >> 1) & 7));
    const int rowb = (16 * (kq >> 1) + 4 * (kq & 1) + q4) * 48;
    lds_p wk = L + L_S + rowb + 8 * p4, wv = wk + 27648, wx = L + L_S + rowb + 32 + (p4 == 1 ? 27648 : 0);
    bf16x8 f0, f1, g0, g1;
    ldsw(xw, xa[0]); ldsw(xw2, xb[0]); f0 = ldsr<bf16x8>(xr0); f1 = ldsr<bf16x8>(xr1);
    waitc(L, C_KW, 4);
    STAMP(4);
    f32x4 DK = {}, DV = {}, DX = {}, DY = {};
    bf16x8 wa[2], wbv[2], wc[2];
#define KVRD(n, ks) do { wa[n] = cat8(ldtr(wk + (ks) * 1536), ldtr(wk + (ks) * 1536 + 384)); wbv[n] = cat8(ldtr(wv + (ks) * 1536), ldtr(wv + (ks) * 1536 + 384)); \
                         wc[n] = cat8(ldtr(wx + (ks) * 1536), ldtr(wx + (ks) * 1536 + 384)); } while (0)
#define KVMM(n, XF) do { DK = MF16(wa[n], XF, DK); DX = MF16(wc[n], XF, DX);        \
                         DV = MF16(XF, wbv[n], DV); DY = MF16(XF, wc[n], DY);       } while (0)
    KVRD(0, 0);
#pragma unroll
    for (int i = 0; i < 9; ++i) {
        KVRD(1, 2 * i + 1);
        SBAR();
        if (i & 1) { KVMM(0, g0); } else { KVMM(0, f0); }
        SBAR();
        if (i < 8) {
            ldsw(xw, xa[i + 1]); ldsw(xw2, xb[i + 1]);
            if (i & 1) { f0 = ldsr<bf16x8>(xr0); } else { g0 = ldsr<bf16x8>(xr0); }
            KVRD(0, 2 * i + 2);
        }
        SBAR();
        if (i & 1) { KVMM(1, g1); } else { KVMM(1, f1); }
        SBAR();
        if (i < 8) { if (i & 1) { f1 = ldsr<bf16x8>(xr1); } else { g1 = ldsr<bf16x8>(xr1); } }
        SBAR();
    }
#undef KVRD
#undef KVMM
    STAMP(5);
    const int tbase = WS_KV + (8 * xcd + (tt >> 1)) * KVT, rr = 16 * (tt & 1) + c16, s16 = tt & 1;
    __builtin_amdgcn_raw_buffer_store_b64((u32x2){cvtpk(DK[0], DK[1]), cvtpk(DK[2], DK[3])}, rs_ws, tbase + ((kq >> 1) * 32 + rr) * 16 + (kq & 1) * 8, 0, AUX_SC1);
    if (kq == 0) __builtin_amdgcn_raw_buffer_store_b64((u32x2){cvtpk(DX[0], DX[1]), 0x3F803F80u}, rs_ws, tbase + 2304 + rr * 8, 0, AUX_SC1);
    const int vo = tbase + 1024 + (s16 * 2 + (kq & 1)) * 320 + (kq >> 1) * 8;
    __builtin_amdgcn_raw_buffer_store_b64((u32x2){cvtpk(DV[0], DV[1]), cvtpk(DV[2], DV[3])}, rs_ws, vo + c16 * 16, 0, AUX_SC1);
    if (c16 >= 4 && c16 < 8) {
        u32x2 v = {cvtpk(DY[0], DY[1]), cvtpk(DY[2], DY[3])};
        if (c16 == 6) v = (u32x2){0x3F803F80u, 0x3F803F80u};
        if (c16 == 7) v = (u32x2){0u, 0u};
        __builtin_amdgcn_raw_buffer_store_b64(v, rs_ws, vo + (12 + c16) * 16, 0, AUX_SC1);
    }
    const unsigned tag = (unsigned)__builtin_amdgcn_readfirstlane(epoch) + 1u;
    DRAIN();
    arrive(L, C_P, lane);
    if (s == 0) {
        waitc(L, C_P, 4);
        if (lane < 8) gstore_sc1((unsigned*)(p.ws + WS_FLAG1 + lane * 4096 + (4 * xcd + (tt >> 2)) * 128), tag);
    }
    STAMP(6);
}

template <int G> DI void conv_group(lds_p L, lds_p R, f32x4 (&acc)[8], int pa0, int pb0) {
    if ((unsigned)__builtin_amdgcn_readfirstlane(peek(L, C_Q)) >= 8u) __builtin_amdgcn_s_setprio(S_PRIOC);
    lds_p a1 = R + pa0 - 64 * (8 * G + 8), b1 = R + pb0 - 64 * (8 * G + 8), a2 = R + pa0 - 64 * (63 - 8 * G), b2 = R + pb0 - 64 * (63 - 8 * G);
    PIN(a1); PIN(b1); PIN(a2); PIN(b2);
#define RDA(ptr) cat8(*(const volatile LAS bf16x4*)(ptr), *(const volatile LAS bf16x4*)((ptr) + 8))
    bf16x8 P1 = RDA(a1 + 512), P2 = RDA(a2), Q1, Q2, B1[8 - G], B2[G + 1];
#pragma unroll
    for (int n = 0; n < 8 - G; ++n) B1[n] = ldsr<bf16x8>(b1 + 512 + 512 * (G + n));
#pragma unroll
    for (int n = 0; n < G + 1; ++n) B2[n] = ldsr<bf16x8>(b2 + 512 * (7 - G + n));
#define CONV_STEP(i, C1, C2, N1, N2) do { \
        N1 = RDA(a1 + 64 * (7 - (i))); N2 = RDA(a2 + 64 * ((i) + 1)); SBAR(); \
        _Pragma("unroll") for (int n = 0; n < 8 - G; ++n) { acc[G + n] = MF16(C1, B1[n], acc[G + n]); SBAR(); B1[n] = ldsr<bf16x8>(b1 + 64 * (7 - (i)) + 512 * (G + n)); SBAR(); } \
        _Pragma("unroll") for (int n = 0; n < G + 1; ++n) { acc[7 - G + n] = MF16(C2, B2[n], acc[7 - G + n]); SBAR(); B2[n] = ldsr<bf16x8>(b2 + 64 * ((i) + 1) + 512 * (7 - G + n)); SBAR(); } \
    } while (0)
    CONV_STEP(0, P1, P2, Q1, Q2); CONV_STEP(1, Q1, Q2, P1, P2); CONV_STEP(2, P1, P2, Q1, Q2); CONV_STEP(3, Q1, Q2, P1, P2);
    CONV_STEP(4, P1, P2, Q1, Q2); CONV_STEP(5, Q1, Q2, P1, P2); CONV_STEP(6, P1, P2, Q1, Q2); CONV_STEP(7, Q1, Q2, P1, P2);
#undef CONV_STEP
#undef RDA
}

DI void ssm_wave(const Params& p, lds_p L, int s, int lane, int ch0 STAMP_ARG) {
    const int d = ch0 + s; const int w = 8 + s; (void)w;
    __builtin_amdgcn_s_setprio(S_PRIO0);
    lds_p R = L + L_S + s * SCH;
    const int pb = d * 128 + lane;
    unsigned short raw[13];
    raw[0] = p.are[pb]; raw[1] = p.are[pb + 64]; raw[2] = p.aim[pb]; raw[3] = p.aim[pb + 64]; raw[4] = p.bre[pb]; raw[5] = p.bre[pb + 64]; raw[6] = p.bim[pb]; raw[7] = p.bim[pb + 64];
    raw[8] = p.cre[pb]; raw[9] = p.cre[pb + 64]; raw[10] = p.cim[pb]; raw[11] = p.cim[pb + 64]; raw[12] = p.ldt[d];
    const unsigned short dsk = p.dsk[d];
    const int t0 = 512 * s + 8 * lane;
    SBAR();
    BARRIER();
    SBAR();
    const f32x2 are = {bf1(raw[0]), bf1(raw[1])}, aim = {bf1(raw[2]), bf1(raw[3])}, bre = {bf1(raw[4]), bf1(raw[5])}, bim = {bf1(raw[6]), bf1(raw[7])};
    const f32x2 cre = {bf1(raw[8]), bf1(raw[9])}, cim = {bf1(raw[10]), bf1(raw[11])};
    const float ldt = bf1(raw[12]);
    const float dt = bflo(cvtpk(expf(ldt), 0.f));
    const f32x2 ar2 = (are * dt) * LOG2E, airev = (aim * dt) * INV2PI;
    cplx2 dB; dB.re = bre * dt; dB.im = bim * dt;
    cplx2 C; C.re = cre; C.im = cim;
    const cplx2 CB = cmul(C, dB);
    const cplx2 w1 = cexp(ar2, airev, 1.f), w8 = cexp(ar2, airev, 8.f), w128 = cexp(ar2, airev, 128.f);

    STAMP(3);
    lds_p st0 = R + lane * 16, st1 = R + 4224 + lane * 16;
    const int kq = lane >> 4, q4 = (lane >> 2) & 3, p4 = lane & 3;
    lds_p trb = R + (p4 >> 1) * 4224 + (16 * (kq >> 1) + 4 * (kq & 1) + q4) * 16 + (p4 & 1) * 8;
#define PUT16(V, NEG) do { \
        u32x4 a0, a1, b0, b1, c0, c1, d0, d1; \
        _Pragma("unroll") for (int j = 0; j < 4; ++j) { \
            a0[j] = cvtpk(V[2 * j].re[0], V[2 * j + 1].re[0]); a1[j] = cvtpk(V[8 + 2 * j].re[0], V[9 + 2 * j].re[0]); \
            b0[j] = cvtpk(V[2 * j].re[1], V[2 * j + 1].re[1]); b1[j] = cvtpk(V[8 + 2 * j].re[1], V[9 + 2 * j].re[1]); \
            c0[j] = cvtpk(NEG V[2 * j].im[0], NEG V[2 * j + 1].im[0]); c1[j] = cvtpk(NEG V[8 + 2 * j].im[0], NEG V[9 + 2 * j].im[0]); \
            d0[j] = cvtpk(NEG V[2 * j].im[1], NEG V[2 * j + 1].im[1]); d1[j] = cvtpk(NEG V[8 + 2 * j].im[1], NEG V[9 + 2 * j].im[1]); } \
        ldsw(st0, a0); ldsw(st1, a1); ldsw(st0 + 1024, b0); ldsw(st1 + 1024, b1); \
        ldsw(st0 + 2048, c0); ldsw(st1 + 2048, c1); ldsw(st0 + 3072, d0); ldsw(st1 + 3072, d1); } while (0)
#define GETF(ks) cat8(ldtr(trb + (ks) * 512), ldtr(trb + (ks) * 512 + 128))
    bf16x8 yf[8];
    {
        cplx2 V[16];
        V[0].re = (f32x2){1.f, 1.f}; V[0].im = (f32x2){0.f, 0.f}; V[1].re = w8.re; V[1].im = -w8.im;
        V[2] = cmul(V[1], V[1]); V[3] = cmul(V[2], V[1]); V[4] = cmul(V[2], V[2]);
        V[5] = cmul(V[4], V[1]); V[6] = cmul(V[4], V[2]); V[7] = cmul(V[4], V[3]); V[8] = cmul(V[4], V[4]);
#pragma unroll
        for (int j = 9; j < 16; ++j) V[j] = cmul(V[8], V[j - 8]);
        PUT16(V, +);
#pragma unroll
        for (int ks = 0; ks < 8; ++ks) yf[ks] = GETF(ks);
    }
    STAMP(9);
    const cplx2 w2 = cmul(w1, w1), w3 = cmul(w2, w1), w4 = cmul(w2, w2), w5 = cmul(w4, w1), w6 = cmul(w4, w2), w7 = cmul(w4, w3);
    f32x4 D0 = {}, D1 = {}, D2 = {};
    cplx2 G = cmul(CB, w128);
    {
        cplx2 V[16];
        V[0] = CB; V[1] = cmul(CB, w1); V[2] = cmul(CB, w2); V[3] = cmul(CB, w3); V[4] = cmul(CB, w4); V[5] = cmul(CB, w5); V[6] = cmul(CB, w6); V[7] = cmul(CB, w7);
        V[8] = G; V[9] = cmul(G, w1); V[10] = cmul(G, w2); V[11] = cmul(G, w3); V[12] = cmul(G, w4); V[13] = cmul(G, w5); V[14] = cmul(G, w6); V[15] = cmul(G, w7);
        PUT16(V, +);
        bf16x8 xf[8];
#pragma unroll
        for (int ks = 0; ks < 8; ++ks) xf[ks] = GETF(ks);
        SBAR();
#pragma unroll
        for (int ks = 0; ks < 8; ++ks) D0 = MF16(xf[ks], yf[ks], D0);
    }
    PIN(D0); STAMP(10);
    {
        cplx2 V[16];
        G = cmul(G, w128); V[0] = G; V[1] = cmul(G, w2); V[2] = cmul(G, w4); V[3] = cmul(G, w6);
        G = cmul(G, w128); V[4] = G; V[5] = cmul(G, w2); V[6] = cmul(G, w4); V[7] = cmul(G, w6);
#pragma unroll
        for (int i = 0; i < 4; ++i) { G = cmul(G, w128); V[8 + 2 * i] = G; V[9 + 2 * i] = cmul(G, w4); }
        PUT16(V, +);
        bf16x8 xf[8];
#pragma unroll
        for (int ks = 0; ks < 8; ++ks) xf[ks] = GETF(ks);
        SBAR();
#pragma unroll
        for (int ks = 0; ks < 8; ++ks) D1 = MF16(xf[ks], yf[ks], D1);
    }
    {
        cplx2 V[16];
#pragma unroll
        for (int i = 0; i < 9; ++i) { G = cmul(G, w128); V[i] = G; }
#pragma unroll
        for (int i = 9; i < 16; ++i) V[i] = G;
        PUT16(V, +);
        bf16x8 xf[8];
#pragma unroll
        for (int ks = 0; ks < 8; ++ks) xf[ks] = GETF(ks);
        SBAR();
#pragma unroll
        for (int ks = 0; ks < 8; ++ks) D2 = MF16(yf[ks], xf[ks], D2);
    }
#undef PUT16
#undef GETF
    PIN(D2); STAMP(4);
    lds_p tab = R + 8448;
    {
        const int j = lane & 15, rq = lane >> 4;
        if (lane == 0) D0[0] += bf1(dsk);
        ldsw(tab + 2 * (8 * j + 4 * (rq & 1) + 128 * (rq >> 1)), (u32x2){cvtpk(D0[0], D0[1]), cvtpk(D0[2], D0[3])});
        const int e0 = rq < 2 ? 256 + 64 * rq + 4 * j : 384 + 64 * (rq - 2) + 2 * j, e1 = rq < 2 ? e0 + 2 : e0 + 32;
        ldsw(tab + 2 * e0, cvtpk(D1[0], D1[1])); ldsw(tab + 2 * e1, cvtpk(D1[2], D1[3]));
        if (j < 8 || lane == 8) ldsw(tab + 2 * (512 + 16 * j + 4 * rq), (u32x2){cvtpk(D2[0], D2[1]), cvtpk(D2[2], D2[3])});
    }
#define PK(hi_src, hi_half, lo_src, lo_half) __builtin_amdgcn_perm(hi_src, lo_src, ((hi_half) ? 0x07060000u : 0x05040000u) | ((lo_half) ? 0x0302u : 0x0100u))
    {
#pragma unroll
        for (int i = 0; i < 2; ++i) {
            const int g = lane + 64 * i, m = 255 - g;
            const unsigned lo = ldsr<unsigned short>(tab + 2 * (384 + m)), hi = ldsr<unsigned short>(tab + 2 * (385 + m));
            const unsigned mid = (m & 1) ? hi : lo, ll = PK(lo, 0, lo, 0);
            ldsw(R + 16 * g, (u32x4){PK(hi, 0, hi, 0), PK(mid, 0, hi, 0), ll, ll});
        }
        {
            const int g = 128 + lane, m = 255 - g;
            const unsigned ab = ldsr<unsigned>(tab + 2 * (256 + 2 * m)), c = ldsr<unsigned short>(tab + 2 * (258 + 2 * m));
            ldsw(R + 16 * g, (u32x4){PK(c, 0, c, 0), PK(ab, 1, ab, 1), PK(ab, 0, ab, 1), PK(ab, 0, ab, 0)});
        }
        {
            const int g = 192 + lane, m = 255 - g;
            u32x4 v;
            if (lane < 32) {
                const u32x2 q = ldsr<u32x2>(tab + 2 * (128 + 4 * m)); const unsigned e = ldsr<unsigned short>(tab + 2 * (132 + 4 * m));
                v = (u32x4){PK(q[1], 1, e, 0), PK(q[1], 0, q[1], 0), PK(q[0], 1, q[1], 0), PK(q[0], 0, q[0], 0)};
            } else {
                const u32x4 q = ldsr<u32x4>(tab + 16 * m);
                v = (u32x4){PK(q[3], 0, q[3], 1), PK(q[2], 0, q[2], 1), PK(q[1], 0, q[1], 1), PK(q[0], 0, q[0], 1)};
            }
            ldsw(R + 16 * g, v);
        }
        if (lane < 4) ldsw(R + 16 * (256 + lane), (u32x4){0u, 0u, 0u, 0u});
    }
#undef PK
    if (lane < 32) ldsw(R + SCH_U + 16 * lane, (u32x4){0u, 0u, 0u, 0u});
#pragma unroll
    for (int i = 0; i < 5; ++i) {
        const int g = lane + 64 * i;
        if (i < 4 || lane < 4) {
            const u32x4 a = ldsr<u32x4>(R + 16 * g); const u32x2 b = ldsr<u32x2>(R + 16 * g + 16);
            const unsigned a0 = __builtin_amdgcn_alignbit(a[1], a[0], 16), a1 = __builtin_amdgcn_alignbit(a[2], a[1], 16), a2 = __builtin_amdgcn_alignbit(a[3], a[2], 16),
                           a3 = __builtin_amdgcn_alignbit(b[0], a[3], 16), a4 = __builtin_amdgcn_alignbit(b[1], b[0], 16);
            ldsw(R + 4160 + 16 * g, (u32x4){a0, a1, a2, a3});
            ldsw(R + 8320 + 16 * g, (u32x4){a[1], a[2], a[3], b[0]});
            ldsw(R + 12480 + 16 * g, (u32x4){a1, a2, a3, a4});
        }
    }
    STAMP(5);
    waitc(L, C_U, 8);
    STAMP(6);
    f32x4 acc[8] = {};
    {
        const int b = lane & 15, kq = lane >> 4, c = (3 - b) & 3;
        const int pa0 = c * 4160 + 2 * (2031 - b + 8 * kq - c), pb0 = SCH_U + 480 + 32 * b + 16 * kq;
        __builtin_amdgcn_s_setprio(S_PRIOB);
        conv_group<0>(L, R, acc, pa0, pb0); conv_group<1>(L, R, acc, pa0, pb0); conv_group<2>(L, R, acc, pa0, pb0); conv_group<3>(L, R, acc, pa0, pb0);
        __builtin_amdgcn_s_setprio(3);
    }
    PIN(acc[0]); PIN(acc[7]); STAMP(7);
#pragma unroll
    for (int nu = 0; nu < 8; ++nu)
        ldsw(R + 512 * nu + 32 * (lane & 15) + 8 * (lane >> 4), (u32x2){cvtpk(acc[nu][0], acc[nu][1]), cvtpk(acc[nu][2], acc[nu][3])});
    arrive(L, C_S2, lane); waitc(L, C_S2, 4);
    {
        u32x4 res[4];
#pragma unroll
        for (int c = 0; c < 4; ++c) res[c] = ldsr<u32x4>(L + L_S + c * SCH + 2 * t0);
#pragma unroll
        for (int e = 0; e < 4; ++e) {
            unsigned short* op = p.out + (size_t)(t0 + 2 * e) * (2 * DM) + DM + ch0;
            *(u32x2*)op = (u32x2){__builtin_amdgcn_perm(res[1][e], res[0][e], 0x05040100u), __builtin_amdgcn_perm(res[3][e], res[2][e], 0x05040100u)};
            *(u32x2*)(op + 2 * DM) = (u32x2){__builtin_amdgcn_perm(res[1][e], res[0][e], 0x07060302u), __builtin_amdgcn_perm(res[3][e], res[2][e], 0x07060302u)};
        }
    }
    STAMP(8);

}

__global__ __launch_bounds__(768) void fused_kernel(Params p) {
    extern __shared__ __attribute__((aligned(16))) char smem[];
    lds_p L = (lds_p)smem;
    const unsigned lds0 = (unsigned)(uintptr_t)smem;
    const int tid = threadIdx.x, lane = tid & 63, w = __builtin_amdgcn_readfirstlane(tid >> 6);
    const int bid = blockIdx.x, xcd = bid & 7, idx = bid >> 3, tb = 2 * xcd + (idx & 1), pi = idx >> 1;
    if (tid < 64) ((LAS unsigned*)(L + L_C))[tid] = 0u;
    STAMP_DECL;
    STAMP(0);
    const __amdgpu_buffer_rsrc_t rs_ws = __builtin_amdgcn_make_buffer_rsrc((void*)p.ws, 0, WS_BYTES, RSRC_W3);

    if (w >= 8) {
        if (idx >= 14 && !MK_NOSSM) ssm_wave(p, L, w - 8, lane, 72 * xcd + 4 * (idx - 14) STAMP_PASS);
        else if (idx >= 10 && idx < 14) kv_wave(p, L, lds0, rs_ws, w - 8, lane, xcd, 4 * (idx - 10) + w - 8 STAMP_PASS);
        else BARRIER();
        STAMP(1);
        STAMP_DUMP();
        return;
    }

    const unsigned epoch = gload_sc1((const unsigned*)(p.ws + WS_EPOCH));
    SBAR();
    BARRIER();
    const int c16 = lane & 15, kq = lane >> 4, r32 = lane & 31, h = lane >> 5;
    const int q4 = (lane >> 2) & 3, p4 = lane & 3;
    const int kh = w >> 2, wq4 = w & 3;

    unsigned tag;
    bf16x8 qmA, qmB; unsigned qeA, qeB;
    {
        const int T0 = 128 * tb + 16 * w;
        const bool has_ssm = idx >= 14 && !MK_NOSSM;
        if (idx >= 10 && idx < 14) waitc(L, C_D, 4);
        {
            const __amdgpu_buffer_rsrc_t rs_wq = __builtin_amdgcn_make_buffer_rsrc((void*)p.wq, 0, DM * DM * 2, RSRC_W3);
            int kl = (lane * 43) >> 8, g = lane - 6 * kl;
            const unsigned dst = lds0 + L_W + w * 6912;
#pragma unroll
            for (int i = 0; i < 7; ++i) {
                const int vo = swap23(72 * w + kl) * 1152 + ((72 * pi) & ~15) + 16 * g;
                if (g < 5 && (i < 6 || lane < 48)) dma16(rs_wq, vo, dst + 1024 * i);
                g += 4; kl += 10; if (g >= 6) { g -= 6; kl += 1; }
            }
        }
        SBAR();
        arrive(L, C_X, lane); waitc(L, C_X, 8);
        constexpr int XPF = 4;
        u32x4 xa[9], xb[9];
        const unsigned short* xp = p.x + (size_t)(T0 + (lane >> 3)) * DM + 8 * (lane & 7);
#pragma unroll
        for (int s = 0; s < XPF; ++s) { xa[s] = *(const u32x4*)(xp + 64 * s); xb[s] = *(const u32x4*)(xp + 64 * s + 8 * DM); }
        SBAR();
        asm volatile("s_waitcnt vmcnt(8)" ::: "memory");
        SBAR();
        arrive(L, C_W, lane);
        f32x4 D0 = {}, D1 = {}, D2 = {};
        unsigned fl0 = 0;
        const unsigned* flp = (const unsigned*)(p.ws + WS_FLAG1 + xcd * 4096) + (lane & 31) * 32;
        {
            lds_p wb = L + L_W + (16 * (kq >> 1) + 4 * (kq & 1) + q4) * 96 + 8 * p4 + 8 * (pi & 1);
            lds_p xs = L + L_Q + w * 2048;
            lds_p xw = xs + (lane >> 3) * 128 + 16 * ((lane & 7) ^ (lane >> 4));
            lds_p xw2 = xs + 1024 + (lane >> 3) * 128 + 16 * ((lane & 7) ^ (4 + (lane >> 4)));
            lds_p xr0 = xs + c16 * 128 + 16 * (kq ^ ((c16 >> 1) & 7)), xr1 = xs + c16 * 128 + 16 * ((4 + kq) ^ ((c16 >> 1) & 7));
            lds_p wb2 = wb - 8 * p4 + 64;
            bf16x8 f0, f1, g0, g1;
            ldsw(xw, xa[0]); ldsw(xw2, xb[0]); f0 = ldsr<bf16x8>(xr0); f1 = ldsr<bf16x8>(xr1);
            waitc(L, C_W, 8);
            if (w >= 4) __builtin_amdgcn_s_setprio(1);
            STAMP(1);
            bf16x8 wf[2][3];
#define QRD(n, ks) do { wf[n][0] = cat8(ldtr(wb + (ks) * 3072), ldtr(wb + (ks) * 3072 + 768)); wf[n][1] = cat8(ldtr(wb + (ks) * 3072 + 32), ldtr(wb + (ks) * 3072 + 768 + 32)); \
                        wf[n][2] = cat8(ldtr(wb2 + (ks) * 3072), ldtr(wb2 + (ks) * 3072 + 768)); } while (0)
#define QMM(n, XF) do { D0 = MF16(wf[n][0], XF, D0); D1 = MF16(wf[n][1], XF, D1); D2 = MF16(wf[n][2], XF, D2); } while (0)
            QRD(0, 0);
#pragma unroll
            for (int s = 0; s < 9; ++s) {
                if (s + XPF < 9) { xa[s + XPF] = *(const u32x4*)(xp + 64 * (s + XPF)); xb[s + XPF] = *(const u32x4*)(xp + 64 * (s + XPF) + 8 * DM); }
                if (s == 8 && w == 7) fl0 = gload_sc1(flp);
                SBAR();
                QRD(1, 2 * s + 1);
                SBAR();
                if (s & 1) { QMM(0, g0); } else { QMM(0, f0); }
                SBAR();
                if (s < 8) {
                    ldsw(xw, xa[s + 1]); ldsw(xw2, xb[s + 1]);
                    if (s & 1) { f0 = ldsr<bf16x8>(xr0); } else { g0 = ldsr<bf16x8>(xr0); }
                    QRD(0, 2 * s + 2);
                }
                SBAR();
                if (s & 1) { QMM(1, g1); } else { QMM(1, f1); }
                SBAR();
                if (s < 8) { if (s & 1) { f1 = ldsr<bf16x8>(xr1); } else { g1 = ldsr<bf16x8>(xr1); } }
                SBAR();
            }
#undef QRD
#undef QMM
        }
        __builtin_amdgcn_s_setprio(0);
        STAMP(2);
        tag = (unsigned)__builtin_amdgcn_readfirstlane(epoch) + 1u;
        {
            lds_p qs = L + L_Q + w * 2048;
            ldsw(qs + c16 * 48 + 8 * kq, (u32x2){cvtpk(D0[0] * QSCALE, D0[1] * QSCALE), cvtpk(D0[2] * QSCALE, D0[3] * QSCALE)});
            const int a0 = kq == 0 ? c16 * 48 + 32 : (16 + c16) * 48 + 2 * (4 * kq - 2);
            const int a1 = (16 + c16) * 48 + 8 * kq;
            ldsw(qs + a0, cvtpk(D1[0] * QSCALE, D1[1] * QSCALE));
            ldsw(qs + a1, cvtpk(D1[2] * QSCALE, D1[3] * QSCALE));
            if (kq == 0) {
                ldsw(qs + (16 + c16) * 48 + 28, cvtpk(D2[0] * QSCALE, D2[1] * QSCALE));
                ldsw(qs + (16 + c16) * 48 + 32, cvtpk(D2[2] * QSCALE, D2[3] * QSCALE));
            }
        }
        arrive(L, C_Q, lane);
        if (w == 7) {
            if (!__all(fl0 == tag)) {
                for (unsigned spins = 0; spins < SPIN_GLB; ++spins) {
                    if (__all(gload_sc1(flp) == tag)) break;
                    __builtin_amdgcn_s_sleep(1);
                }
            }
            __builtin_amdgcn_fence(__ATOMIC_ACQUIRE, "wavefront");
            arrive(L, C_F1, lane);
            STAMP(8);
        }
        if (has_ssm) {
            u32x2 uu[4];
#pragma unroll
            for (int e = 0; e < 4; ++e) uu[e] = *(const u32x2*)(p.u + (size_t)(256 * w + 4 * lane + e) * DM + 72 * xcd + 4 * (idx - 14));
            const int uo = SCH_U + 512 + 2 * (256 * w + 4 * lane);
#pragma unroll
            for (int c = 0; c < 4; ++c) {
                const unsigned sel = (c & 1) ? 0x07060302u : 0x05040100u;
                ldsw(L + L_S + c * SCH + uo, (u32x2){__builtin_amdgcn_perm(uu[1][c >> 1], uu[0][c >> 1], sel), __builtin_amdgcn_perm(uu[3][c >> 1], uu[2][c >> 1], sel)});
            }
            arrive(L, C_U, lane);
        }
        waitc(L, C_Q, 8);
        {
            lds_p qa = L + L_Q + wq4 * 2048 + r32 * 48, qb = qa + 4 * 2048;
            qmA = ldsr<bf16x8>(qa + 16 * h); qmB = ldsr<bf16x8>(qb + 16 * h);
            qeA = ldsr<unsigned>(qa + 32); qeB = ldsr<unsigned>(qb + 32);
        }
        waitc(L, C_F1, 1);
        STAMP(3);
    }

    {
        lds_p ring = L + L_W + kh * (8 * KVT);
        const int dmo = WS_KV + (32 * kh + wq4) * KVT + lane * 16;
        const unsigned dmd = lds0 + L_W + kh * (8 * KVT) + wq4 * KVT;
#define KV_DMA_A(cc) do { dma16_sc1(rs_ws, dmo + (cc) * (4 * KVT), dmd + ((cc) & 1) * (4 * KVT)); dma16_sc1(rs_ws, dmo + (cc) * (4 * KVT) + 1024, dmd + ((cc) & 1) * (4 * KVT) + 1024); } while (0)
#define KV_DMA_B(cc) do { if (lane < 32) dma16_sc1(rs_ws, dmo + (cc) * (4 * KVT) + 2048, dmd + ((cc) & 1) * (4 * KVT) + 2048); } while (0)
        lds_p kb = ring + lane * 16, eb = ring + 2304 + r32 * 8, vb = ring + 1024 + (h * 20 + (r32 < 19 ? r32 : 19)) * 16;
        const f32x16 Z = {};
        unsigned mA = 0, mB = 0;
        f32x16 OA, OB;
        for (int pass = 0; pass < 2; ++pass) {
            KV_DMA_A(0); KV_DMA_B(0); KV_DMA_A(1); KV_DMA_B(1);
            asm volatile("s_waitcnt vmcnt(3)" ::: "memory");
            arrive(L, C_RW, lane); waitc(L, C_RW, 8 * (pass + 1));
            STAMP(9);
            if (pass == 0) {
                const bf16x8 km = ldsr<bf16x8>(L + L_W + lane * 16); const bf16x4 ke = ldsr<bf16x4>(L + L_W + 2304 + r32 * 8);
                const u32x2 ea = {h ? 0u : qeA, 0u}, ebq = {h ? 0u : qeB, 0u};
                f32x16 sa = MF32(km, qmA, Z), sb = MF32(km, qmB, Z);
                sa = MF32E(ke, __builtin_bit_cast(bf16x4, ea), sa); sb = MF32E(ke, __builtin_bit_cast(bf16x4, ebq), sb);
                float ma = sa[0], mb = sb[0];
#pragma unroll
                for (int i = 1; i < 16; ++i) { ma = fmaxf(ma, sa[i]); mb = fmaxf(mb, sb[i]); }
                auto ra = __builtin_amdgcn_permlane32_swap(__float_as_uint(ma), __float_as_uint(ma), false, false);
                auto rb = __builtin_amdgcn_permlane32_swap(__float_as_uint(mb), __float_as_uint(mb), false, false);
                ma = fmaxf(__uint_as_float(ra[0]), __uint_as_float(ra[1])); mb = fmaxf(__uint_as_float(rb[0]), __uint_as_float(rb[1]));
                const float ah = bflo(cvtpk(ma, 0.f)), bh = bflo(cvtpk(mb, 0.f));
                mA = cvtpk(-ah, ah - ma); mB = cvtpk(-bh, bh - mb);
            }
            const u32x2 ea = {h ? 0u : qeA, h ? 0u : mA}, ebq = {h ? 0u : qeB, h ? 0u : mB};
            const bf16x4 qxA = __builtin_bit_cast(bf16x4, ea), qxB = __builtin_bit_cast(bf16x4, ebq);
            f32x16 SA, SB = Z; u32x4 pa0, pa1, pb0 = {}, pb1 = {}; bf16x8 km, vx0, vx1, vy0, vy1; bf16x4 ke;
            OA = Z; OB = Z;
            km = ldsr<bf16x8>(kb); ke = ldsr<bf16x4>(eb);
            SA = MF32(km, qmA, Z); SA = MF32E(ke, qxA, SA);
            vx0 = ldsr<bf16x8>(vb); vx1 = ldsr<bf16x8>(vb + 640);
#define PH(SC, SN, QM, QX, ON, pp0, pp1, pn0, pn1, V0, V1, LD1, LD2, LD4) do { \
            ON = MF32(V0, BC8(pp0), ON); SBAR(); \
            LD1; \
            SC[0] = EXP2(SC[0]); SC[1] = EXP2(SC[1]); SC[2] = EXP2(SC[2]); SC[3] = EXP2(SC[3]); SBAR(); \
            SN = MF32(km, QM, Z); SBAR(); \
            LD2; \
            SC[4] = EXP2(SC[4]); SC[5] = EXP2(SC[5]); pn0[0] = cvtpk(SC[0], SC[1]); SC[6] = EXP2(SC[6]); SC[7] = EXP2(SC[7]); pn0[1] = cvtpk(SC[2], SC[3]); SBAR(); \
            ON = MF32(V1, BC8(pp1), ON); SBAR(); \
            SC[8] = EXP2(SC[8]); SC[9] = EXP2(SC[9]); pn0[2] = cvtpk(SC[4], SC[5]); SC[10] = EXP2(SC[10]); SC[11] = EXP2(SC[11]); pn0[3] = cvtpk(SC[6], SC[7]); PIN(pn0); SBAR(); \
            SN = MF32E(ke, QX, SN); SBAR(); \
            LD4; \
            SC[12] = EXP2(SC[12]); SC[13] = EXP2(SC[13]); pn1[0] = cvtpk(SC[8], SC[9]); SC[14] = EXP2(SC[14]); SC[15] = EXP2(SC[15]); pn1[1] = cvtpk(SC[10], SC[11]); \
            pn1[2] = cvtpk(SC[12], SC[13]); pn1[3] = cvtpk(SC[14], SC[15]); PIN(pn1); SBAR(); \
        } while (0)
#define VRD(t, N0, N1) (N0 = ldsr<bf16x8>(vb + (t) * KVT), N1 = ldsr<bf16x8>(vb + (t) * KVT + 640))
#define KMRD(t) (km = ldsr<bf16x8>(kb + (((t) + 1) & 7) * KVT))
#define KERD(t) (ke = ldsr<bf16x4>(eb + (((t) + 1) & 7) * KVT))
#define PHA(P0, P1, LD1, LD2, LD4) PH(SA, SB, qmB, qxB, OB, pb0, pb1, pa0, pa1, P0, P1, LD1, LD2, LD4);
#define PHB(N0, N1, LD1, LD2, LD4) PH(SB, SA, qmA, qxA, OA, pa0, pa1, pb0, pb1, N0, N1, LD1, LD2, LD4);
#define TA(t) PHA(vx0, vx1, VRD(t, vy0, vy1), KMRD(t), KERD(t))
#define TB() PHB(vy0, vy1, (void)0, (void)0, (void)0)
#define UA(t) PHA(vy0, vy1, VRD(t, vx0, vx1), KMRD(t), KERD(t))
#define UB() PHB(vx0, vx1, (void)0, (void)0, (void)0)
#define PRIO(x) __builtin_amdgcn_s_setprio(x);
#define LAST(t, b) \
            PHA(vy0, vy1, VRD(t, vx0, vx1), (void)0, (void)0) \
            asm volatile("s_waitcnt vmcnt(0)" ::: "memory"); \
            if ((b) < 7) { if (it == 0 && pass == 0) { arrive(L, C_B, lane); waitc(L, C_B, 8 * ((b) + 1)); } else __builtin_amdgcn_s_barrier(); } \
            CBAR(); PRIO(3) KMRD(t); KERD(t); SBAR(); \
            PHB(vx0, vx1, (void)0, if ((b) + 2 < 8) KV_DMA_A((b) + 2), if ((b) + 2 < 8) KV_DMA_B((b) + 2))
            PRIO(3)
            CSTAMP(10);
            for (int it = 0; it < 4; ++it) {
                TA(0) PRIO(2) TB() UA(1) PRIO(1) UB() TA(2) PRIO(0) TB()
                LAST(3, 2 * it)
                if (it == 1 && pass == 0) {
                    const __amdgpu_buffer_rsrc_t rs_wo = __builtin_amdgcn_make_buffer_rsrc((void*)p.wo, 0, DM * DM * 2, RSRC_W3);
                    int kp = 64 * w + lane; const int k9 = (kp * 7282) >> 16; int g = kp - 9 * k9; kp = k9;
                    for (int inst = w; inst < 81; inst += 8) {
                        dma16(rs_wo, swap23(kp) * 1152 + 144 * (pi >> 1) + 16 * g, lds0 + L_S + 1024 * inst);
                        g += 8; kp += 56; if (g >= 9) { g -= 9; kp += 1; }
                    }
                }
                TA(4) PRIO(2) TB() UA(5) PRIO(1) UB() TA(6) PRIO(0) TB()
                LAST(7, 2 * it + 1)
            }
            OB = MF32(vx0, BC8(pb0), OB); OB = MF32(vx1, BC8(pb1), OB);
#undef TA
#undef TB
#undef UA
#undef UB
#undef LAST
#undef PHA
#undef PHB
#undef VRD
#undef KMRD
#undef KERD
#undef PH
            PRIO(0)
            STAMP(4); CSTAMP(11);
            lds_p xo = (w < 4 ? L + L_X1 : L + L_Q) + wq4 * 3072, xi = (w < 4 ? L + L_Q : L + L_X1) + wq4 * 3072;
            {
                const f32x16& S = kh ? OA : OB;
#pragma unroll
                for (int g = 0; g < 3; ++g) ldsw(xo + g * 1024 + lane * 16, (f32x4){S[4 * g], S[4 * g + 1], S[4 * g + 2], S[4 * g + 3]});
            }
            BARRIER();
            STAMP(12);
            float o[12];
#pragma unroll
            for (int g = 0; g < 3; ++g) {
                const f32x4 t = ldsr<f32x4>(xi + g * 1024 + lane * 16);
#pragma unroll
                for (int i = 0; i < 4; ++i) o[4 * g + i] = (kh ? OB[4 * g + i] : OA[4 * g + i]) + t[i];
            }
            const float lsum = __uint_as_float(__builtin_amdgcn_permlane32_swap(__float_as_uint(o[10]), __float_as_uint(o[10]), false, false)[0]);
            const float rl = __builtin_amdgcn_rcpf(lsum);
            bool bad = !(lsum < 1.2676506e30f);
#pragma unroll
            for (int i = 0; i < 8; ++i) bad |= !(fabsf(o[i]) < 3.0e38f);
            if (h == 0) bad |= !(fabsf(o[8]) < 3.0e38f) || !(fabsf(o[9]) < 3.0e38f);
            {
                lds_p os = xi;
                lds_p ob = os + (r32 & 15) * 72 + (r32 >> 4) * 36 + 8 * h;
                ldsw(ob, cvtpk(o[0] * rl, o[1] * rl)); ldsw(ob + 4, cvtpk(o[2] * rl, o[3] * rl));
                ldsw(ob + 16, cvtpk(o[4] * rl, o[5] * rl)); ldsw(ob + 20, cvtpk(o[6] * rl, o[7] * rl));
                if (h == 0) ldsw(ob + 32, cvtpk(o[8] * rl, o[9] * rl));
#pragma unroll
                for (int i = 0; i < 3; ++i) {
                    const int e = lane + 64 * i;
                    if (e < 144) {
                        const int tk = e & 15, pc = e >> 4, k0 = 36 * pi + 4 * pc;
                        const u32x2 v = ldsr<u32x2>(os + tk * 72 + pc * 8);
                        __builtin_amdgcn_raw_buffer_store_b64(v, rs_ws, WS_O + (8 * tb + w) * 18432 + (k0 >> 3) * 256 + tk * 16 + (k0 & 4) * 2, 0, AUX_SC1);
                    }
                }
            }
            if (pass == 0 && __any(bad) && lane == 0) ((volatile LAS unsigned*)(L + L_C))[C_BAD] = 1u;
            STAMP(13);
            DRAIN();
            BARRIER();
            const bool redo = pass == 0 && __builtin_amdgcn_readfirstlane(((volatile LAS unsigned*)(L + L_C))[C_BAD]) != 0;
            if (!redo) break;
            {
                const u32x2 ea0 = {h ? 0u : qeA, 0u}, eb0 = {h ? 0u : qeB, 0u};
                float ma = -INFINITY, mb = -INFINITY;
                for (int t = 0; t < 64; ++t) {
                    const u32x4 kmv = __builtin_amdgcn_raw_buffer_load_b128(rs_ws, WS_KV + t * KVT + lane * 16, 0, AUX_SC1);
                    const u32x2 kev = __builtin_amdgcn_raw_buffer_load_b64(rs_ws, WS_KV + t * KVT + 2304 + r32 * 8, 0, AUX_SC1);
                    f32x16 sa = MF32(BC8(kmv), qmA, Z), sb = MF32(BC8(kmv), qmB, Z);
                    sa = MF32E(__builtin_bit_cast(bf16x4, kev), __builtin_bit_cast(bf16x4, ea0), sa);
                    sb = MF32E(__builtin_bit_cast(bf16x4, kev), __builtin_bit_cast(bf16x4, eb0), sb);
#pragma unroll
                    for (int i = 0; i < 16; ++i) { ma = fmaxf(ma, sa[i]); mb = fmaxf(mb, sb[i]); }
                }
                ma = fmaxf(ma, __shfl_xor(ma, 32)); mb = fmaxf(mb, __shfl_xor(mb, 32));
                const float ah = bflo(cvtpk(ma, 0.f)), bh = bflo(cvtpk(mb, 0.f));
                mA = cvtpk(-ah, ah - ma); mB = cvtpk(-bh, bh - mb);
            }
        }
#undef KV_DMA_A
#undef KV_DMA_B
#undef PRIO
        if (w == 0 && lane == 0) gstore_sc1((unsigned*)(p.ws + WS_FLAG2 + (tb * 16 + pi) * 128), tag);
        STAMP(5);
    }

    {
        constexpr int PF3 = 5;
        lds_p wb = L + L_S + (16 * (kq >> 1) + 4 * (kq & 1) + q4) * 144 + 8 * p4 + kh * (9 * 32 * 144);
        bf16x8 wf[PF3][5];
#define WORD3(i, T) cat8(ldtr(wb + (i) * 4608 + 32 * (T)), ldtr(wb + (i) * 4608 + 1152 + 32 * (T)))
#pragma unroll
        for (int i = 0; i < PF3; ++i)
#pragma unroll
            for (int T = 0; T < 5; ++T) wf[i][T] = WORD3(i, T);
        SBAR();
        if (w == 7) {
            const unsigned* f = (const unsigned*)(p.ws + WS_FLAG2 + tb * 2048) + (lane & 15) * 32;
            for (unsigned spins = 0; spins < SPIN_GLB; ++spins) {
                if (__all(gload_sc1(f) == tag)) break;
                __builtin_amdgcn_s_sleep(1);
            }
            __builtin_amdgcn_fence(__ATOMIC_ACQUIRE, "wavefront");
        }
        BARRIER();
        STAMP(6);
        const int Tt = 128 * tb + 64 * (pi & 1) + 16 * wq4, F0 = 72 * (pi >> 1);
        u32x4 of[9];
#pragma unroll
        for (int i = 0; i < 9; ++i) of[i] = __builtin_amdgcn_raw_buffer_load_b128(rs_ws, WS_O + (Tt >> 4) * 18432 + (9 * kh + i) * 1024 + lane * 16, 0, AUX_SC1);
        SBAR();
        f32x4 D[5] = {};
#pragma unroll
        for (int i = 0; i < 9; ++i) {
#pragma unroll
            for (int T = 0; T < 5; ++T) D[T] = MF16(wf[i % PF3][T], BC8(of[i]), D[T]);
            SBAR();
            if (i + PF3 < 9) {
#pragma unroll
                for (int T = 0; T < 5; ++T) wf[i % PF3][T] = WORD3(i + PF3, T);
            }
            SBAR();
        }
#undef WORD3
        lds_p xo = L + L_W + w * 3072, xi = L + L_W + (w ^ 4) * 3072;
        if (kh == 0) { ldsw(xo + lane * 16, D[3]); ldsw(xo + 1024 + lane * 16, D[4]); }
        else { ldsw(xo + lane * 16, D[0]); ldsw(xo + 1024 + lane * 16, D[1]); ldsw(xo + 2048 + lane * 16, D[2]); }
        BARRIER();
        unsigned short* op = p.out + (size_t)(Tt + c16) * (2 * DM) + F0 + 4 * kq;
        if (kh == 0) {
#pragma unroll
            for (int T = 0; T < 3; ++T) {
                const f32x4 v = D[T] + ldsr<f32x4>(xi + T * 1024 + lane * 16);
                *(u32x2*)(op + 16 * T) = (u32x2){cvtpk(v[0], v[1]), cvtpk(v[2], v[3])};
            }
        } else {
#pragma unroll
            for (int T = 3; T < 5; ++T) {
                const f32x4 v = D[T] + ldsr<f32x4>(xi + (T - 3) * 1024 + lane * 16);
                if (T == 3 || kq < 2) *(u32x2*)(op + 16 * T) = (u32x2){cvtpk(v[0], v[1]), cvtpk(v[2], v[3])};
            }
        }
        STAMP(7);
        STAMP_DUMP();
        if (bid == 0 && w == 7) {
            const unsigned* f = (const unsigned*)(p.ws + WS_FLAG2);
            for (unsigned spins = 0; spins < SPIN_GLB; ++spins) {
                bool ok = true;
#pragma unroll
                for (int i = 0; i < 4; ++i) { const int e = lane + 64 * i; ok &= gload_sc1(f + e * 32) == tag; }
                if (__all(ok)) break;
                __builtin_amdgcn_s_sleep(2);
            }
            if (lane == 0) gstore_sc1((unsigned*)(p.ws + WS_EPOCH), tag);
        }
    }
}

}

extern "C" void kernel_launch(void* const* d_in, const int* in_sizes, int n_in, void* d_out, int out_size, void* d_ws, size_t ws_size,
                              hipStream_t stream) {
    Params p;
    p.x = (const unsigned short*)d_in[0]; p.wq = (const unsigned short*)d_in[1]; p.wk = (const unsigned short*)d_in[2];
    p.wv = (const unsigned short*)d_in[3]; p.wo = (const unsigned short*)d_in[4]; p.u = (const unsigned short*)d_in[5];
    p.are = (const unsigned short*)d_in[6]; p.aim = (const unsigned short*)d_in[7]; p.bre = (const unsigned short*)d_in[8];
    p.bim = (const unsigned short*)d_in[9]; p.cre = (const unsigned short*)d_in[10]; p.cim = (const unsigned short*)d_in[11];
    p.ldt = (const unsigned short*)d_in[12]; p.dsk = (const unsigned short*)d_in[13];
    p.out = (unsigned short*)d_out; p.ws = (char*)d_ws;
    (void)hipFuncSetAttribute((const void*)fused_kernel, hipFuncAttributeMaxDynamicSharedMemorySize, L_END);
    fused_kernel<<<256, 768, L_END, stream>>>(p);
}
```

```cpp
#include <hip/hip_runtime.h>
#include <hip/hip_bf16.h>
#include <stdint.h>

#define S_PRIO0 0
#define U_BY_S 0
#define S_PRIOB 0
#define S_PRIOC 3
#define MK_PROBE 0
#define PRB(i) do {} while (0)
#define MK_NOSSM 0
#define MK_STAMPS 0

namespace {

typedef __attribute__((ext_vector_type(16))) float f32x16;
typedef __attribute__((ext_vector_type(4))) float f32x4;
typedef __attribute__((ext_vector_type(2))) float f32x2;
typedef __attribute__((ext_vector_type(8))) short bf16x8;
typedef __attribute__((ext_vector_type(4))) short bf16x4;
typedef __attribute__((ext_vector_type(2))) __bf16 bf16x2_t;
typedef __attribute__((ext_vector_type(4))) unsigned u32x4;
typedef __attribute__((ext_vector_type(2))) unsigned u32x2;
typedef short v4i16_t __attribute__((ext_vector_type(4)));
#define LAS __attribute__((address_space(3)))
#define GAS __attribute__((address_space(1)))
typedef LAS char* lds_p;
typedef const unsigned short* cbf;

constexpr int SEQ = 2048, DM = 576;
constexpr float QSCALE = 0.34005040627f;
constexpr float LOG2E = 1.4426950408889634f;
constexpr float INV2PI = 0.15915494309189535f;

constexpr int WS_EPOCH = 0;
constexpr int WS_FLAG1 = 4096;
constexpr int WS_FLAG2 = WS_FLAG1 + 32768;
constexpr int WS_KV = WS_FLAG2 + 32768;
constexpr int KVT = 2560;
constexpr int WS_O = WS_KV + 64 * KVT;
constexpr int WS_BYTES = WS_O + SEQ * DM * 2;
constexpr int WS_STAMP = 8 * 1024 * 1024;

constexpr int L_W = 0;
constexpr int L_X1 = 40960;
constexpr int L_S = 55296;
constexpr int L_Q = L_S + 87360;
constexpr int L_C = L_Q + 16384;
constexpr int L_END = L_C + 256;
constexpr int SCH = 21840;
constexpr int SCH_U = 16640;
enum { C_W = 0, C_P, C_Q, C_F1, C_RW, C_B, C_BAD, C_U, C_S2, C_D, C_KW, C_X, C_NUM };

constexpr int RSRC_W3 = 0x00020000;
constexpr int AUX_SC1 = 16;
constexpr unsigned SPIN_LDS = 1u << 20, SPIN_GLB = 1u << 16;

#define SBAR() __builtin_amdgcn_sched_barrier(0)
#define PIN(x) asm volatile("" : "+v"(x))
#define CBAR() asm volatile("" ::: "memory")
#define DRAIN() asm volatile("s_waitcnt vmcnt(0)" ::: "memory")
#define MF32(a, b, c) __builtin_amdgcn_mfma_f32_32x32x16_bf16(a, b, c, 0, 0, 0)
#define MF32E(a, b, c) __builtin_amdgcn_mfma_f32_32x32x8bf16_1k(a, b, c, 0, 0, 0)
#define MF16(a, b, c) __builtin_amdgcn_mfma_f32_16x16x32_bf16(a, b, c, 0, 0, 0)
#define EXP2(x) __builtin_amdgcn_exp2f(x)
#define BC8(x) __builtin_bit_cast(bf16x8, x)
#define DI __device__ __forceinline__

DI unsigned cvtpk(float lo, float hi) { f32x2 v = {lo, hi}; return __builtin_bit_cast(unsigned, __builtin_convertvector(v, bf16x2_t)); }
DI float bflo(unsigned x) { return __builtin_bit_cast(float, x << 16); }
DI float bfhi(unsigned x) { return __builtin_bit_cast(float, x & 0xffff0000u); }
DI float bf1(unsigned short x) { return __builtin_bit_cast(float, (unsigned)x << 16); }
template <class T> DI T ldsr(lds_p p) { return *(const LAS T*)p; }
template <class T> DI void ldsw(lds_p p, T v) { *(LAS T*)p = v; }
DI bf16x4 ldtr(lds_p p) { return __builtin_bit_cast(bf16x4, __builtin_amdgcn_ds_read_tr16_b64_v4i16((LAS v4i16_t*)p)); }
DI bf16x8 cat8(bf16x4 a, bf16x4 b) { return __builtin_shufflevector(a, b, 0, 1, 2, 3, 4, 5, 6, 7); }
DI int swap23(int k) { return (k & ~12) | ((k & 4) << 1) | ((k & 8) >> 1); }
DI void dma16(__amdgpu_buffer_rsrc_t rs, int voff, unsigned lds_base) {
    unsigned keep;
    asm volatile("s_mov_b32 %0, m0\n\ts_mov_b32 m0, %3\n\ts_nop 0\n\tbuffer_load_dwordx4 %1, %2, 0 offen lds\n\ts_mov_b32 m0, %0"
                 : "=&s"(keep) : "v"(voff), "s"(rs), "s"(lds_base) : "memory");
}
DI void dma16_sc1(__amdgpu_buffer_rsrc_t rs, int voff, unsigned lds_base) {
    unsigned keep;
    asm volatile("s_mov_b32 %0, m0\n\ts_mov_b32 m0, %3\n\ts_nop 0\n\tbuffer_load_dwordx4 %1, %2, 0 offen sc1 lds\n\ts_mov_b32 m0, %0"
                 : "=&s"(keep) : "v"(voff), "s"(rs), "s"(lds_base) : "memory");
}
#define BARRIER() asm volatile("s_waitcnt lgkmcnt(0)\n\ts_barrier" ::: "memory")
DI void arrive(lds_p L, int c, int lane) {
    (void)lane;
    unsigned long long keep;
    asm volatile("s_mov_b64 %0, exec\n\ts_mov_b64 exec, 1\n\tds_add_u32 %1, %2\n\ts_mov_b64 exec, %0" : "=&s"(keep) : "v"((unsigned)(uintptr_t)(L + L_C + 4 * c)), "v"(1u) : "memory");
}
DI void waitc(lds_p L, int c, unsigned target) {
    CBAR();
    volatile LAS unsigned* p = (volatile LAS unsigned*)(L + L_C) + c;
    for (unsigned spins = 0; spins < SPIN_LDS; ++spins) {
        if ((unsigned)__builtin_amdgcn_readfirstlane(*p) >= target) break;
        __builtin_amdgcn_s_sleep(1);
    }
    CBAR();
}
DI unsigned peek(lds_p L, int c) { return *((volatile LAS unsigned*)(L + L_C) + c); }
DI void waitp(lds_p L, int c, unsigned seen, unsigned target) { if ((unsigned)__builtin_amdgcn_readfirstlane(seen) < target) waitc(L, c, target); CBAR(); }
DI unsigned gload_sc1(const unsigned* p) { return __hip_atomic_load((GAS unsigned*)p, __ATOMIC_RELAXED, __HIP_MEMORY_SCOPE_AGENT); }
DI void gstore_sc1(unsigned* p, unsigned v) { __hip_atomic_store((GAS unsigned*)p, v, __ATOMIC_RELAXED, __HIP_MEMORY_SCOPE_AGENT); }

struct cplx2 { f32x2 re, im; };
DI cplx2 cmul(const cplx2& a, const cplx2& b) { cplx2 r; r.re = a.re * b.re - a.im * b.im; r.im = a.re * b.im + a.im * b.re; return r; }
DI cplx2 cexp(f32x2 ar2, f32x2 airev, float e) {
    f32x2 m = ar2 * e, t = airev * e; cplx2 r;
#pragma unroll
    for (int i = 0; i < 2; ++i) {
        const float mag = EXP2(m[i]), fr = __builtin_amdgcn_fractf(t[i]);
        r.re[i] = mag * __builtin_amdgcn_cosf(fr); r.im[i] = mag * __builtin_amdgcn_sinf(fr);
    }
    return r;
}

struct Params {
    const unsigned short *x, *wq, *wk, *wv, *wo, *u, *are, *aim, *bre, *bim, *cre, *cim, *ldt, *dsk;
    unsigned short* out;
    char* ws;
};

#define STAMP(i) do {} while (0)
#define CSTAMP(i) do {} while (0)
#define STAMP_DECL do {} while (0)
#define STAMP_DUMP() do {} while (0)
#define STAMP_ARG
#define STAMP_PASS

DI void dma16s(__amdgpu_buffer_rsrc_t rs, int voff, int soff, unsigned lds_base) {
    unsigned keep;
    asm volatile("s_mov_b32 %0, m0\n\ts_mov_b32 m0, %4\n\ts_nop 0\n\tbuffer_load_dwordx4 %1, %2, %3 offen lds\n\ts_mov_b32 m0, %0"
                 : "=&s"(keep) : "v"(voff), "s"(rs), "s"(soff), "s"(lds_base) : "memory");
}
DI void kv_wave(const Params& p, lds_p L, unsigned lds0, __amdgpu_buffer_rsrc_t rs_ws, int s, int lane, int xcd, int tt STAMP_ARG) {
    __builtin_amdgcn_s_setprio(3);
    const unsigned epoch = gload_sc1((const unsigned*)(p.ws + WS_EPOCH));
    const int T0 = 256 * xcd + 16 * tt;
    {
        const __amdgpu_buffer_rsrc_t rs_wk = __builtin_amdgcn_make_buffer_rsrc((void*)p.wk, 0, DM * 18 * 2, RSRC_W3);
        const __amdgpu_buffer_rsrc_t rs_wv = __builtin_amdgcn_make_buffer_rsrc((void*)p.wv, 0, DM * 18 * 2, RSRC_W3);
        int vo[3];
#pragma unroll
        for (int j = 0; j < 3; ++j) { const int sg = 64 * j + lane, kp = (sg * 171) >> 9; vo[j] = swap23(kp) * 36 + 16 * (sg - 3 * kp); }
        for (int b = s; b < 9; b += 4) {
#pragma unroll
            for (int j = 0; j < 3; ++j) {
                dma16s(rs_wk, vo[j], 2304 * b, lds0 + L_S + 3072 * b + 1024 * j);
                dma16s(rs_wv, vo[j], 2304 * b, lds0 + L_S + 27648 + 3072 * b + 1024 * j);
            }
        }
    }
    SBAR();
    u32x4 xa[9], xb[9];
    {
        const unsigned short* xp = p.x + (size_t)(T0 + (lane >> 3)) * DM + 8 * (lane & 7);
#pragma unroll
        for (int i = 0; i < 9; ++i) { xa[i] = *(const u32x4*)(xp + 64 * i); xb[i] = *(const u32x4*)(xp + 64 * i + 8 * DM); }
    }
    SBAR();
    BARRIER();
    arrive(L, C_D, lane);
    STAMP(3);
    asm volatile("s_waitcnt vmcnt(18)" ::: "memory");
    arrive(L, C_KW, lane);
    const int c16 = lane & 15, kq = lane >> 4, q4 = (lane >> 2) & 3, p4 = lane & 3;
    lds_p xs = L + L_S + 55296 + s * 2048;
    lds_p xw = xs + (lane >> 3) * 128 + 16 * ((lane & 7) ^ (lane >> 4)), xw2 = xs + 1024 + (lane >> 3) * 128 + 16 * ((lane & 7) ^ (4 + (lane >> 4)));
    lds_p xr0 = xs + c16 * 128 + 16 * (kq ^ ((c16 >> 1) & 7)), xr1 = xs + c16 * 128 + 16 * ((4 + kq) ^ ((c16 >> 1) & 7));
    const int rowb = (16 * (kq >> 1) + 4 * (kq & 1) + q4) * 48;
    lds_p wk = L + L_S + rowb + 8 * p4, wv = wk + 27648, wx = L + L_S + rowb + 32 + (p4 == 1 ? 27648 : 0);
    bf16x8 f0, f1, g0, g1;
    ldsw(xw, xa[0]); ldsw(xw2, xb[0]); f0 = ldsr<bf16x8>(xr0); f1 = ldsr<bf16x8>(xr1);
    waitc(L, C_KW, 4);
    STAMP(4);
    f32x4 DK = {}, DV = {}, DX = {}, DY = {};
    bf16x8 wa[2], wbv[2], wc[2];
#define KVRD(n, ks) do { wa[n] = cat8(ldtr(wk + (ks) * 1536), ldtr(wk + (ks) * 1536 + 384)); wbv[n] = cat8(ldtr(wv + (ks) * 1536), ldtr(wv + (ks) * 1536 + 384)); \
                         wc[n] = cat8(ldtr(wx + (ks) * 1536), ldtr(wx + (ks) * 1536 + 384)); } while (0)
#define KVMM(n, XF) do { DK = MF16(wa[n], XF, DK); DX = MF16(wc[n], XF, DX);        \
                         DV = MF16(XF, wbv[n], DV); DY = MF16(XF, wc[n], DY);       } while (0)
    KVRD(0, 0);
#pragma unroll
    for (int i = 0; i < 9; ++i) {
        KVRD(1, 2 * i + 1);
        SBAR();
        if (i & 1) { KVMM(0, g0); } else { KVMM(0, f0); }
        SBAR();
        if (i < 8) {
            ldsw(xw, xa[i + 1]); ldsw(xw2, xb[i + 1]);
            if (i & 1) { f0 = ldsr<bf16x8>(xr0); } else { g0 = ldsr<bf16x8>(xr0); }
            KVRD(0, 2 * i + 2);
        }
        SBAR();
        if (i & 1) { KVMM(1, g1); } else { KVMM(1, f1); }
        SBAR();
        if (i < 8) { if (i & 1) { f1 = ldsr<bf16x8>(xr1); } else { g1 = ldsr<bf16x8>(xr1); } }
        SBAR();
    }
#undef KVRD
#undef KVMM
    STAMP(5);
    const int tbase = WS_KV + (8 * xcd + (tt >> 1)) * KVT, rr = 16 * (tt & 1) + c16, s16 = tt & 1;
    __builtin_amdgcn_raw_buffer_store_b64((u32x2){cvtpk(DK[0], DK[1]), cvtpk(DK[2], DK[3])}, rs_ws, tbase + ((kq >> 1) * 32 + rr) * 16 + (kq & 1) * 8, 0, AUX_SC1);
    if (kq == 0) __builtin_amdgcn_raw_buffer_store_b64((u32x2){cvtpk(DX[0], DX[1]), 0x3F803F80u}, rs_ws, tbase + 2304 + rr * 8, 0, AUX_SC1);
    const int vo = tbase + 1024 + (s16 * 2 + (kq & 1)) * 320 + (kq >> 1) * 8;
    __builtin_amdgcn_raw_buffer_store_b64((u32x2){cvtpk(DV[0], DV[1]), cvtpk(DV[2], DV[3])}, rs_ws, vo + c16 * 16, 0, AUX_SC1);
    if (c16 >= 4 && c16 < 8) {
        u32x2 v = {cvtpk(DY[0], DY[1]), cvtpk(DY[2], DY[3])};
        if (c16 == 6) v = (u32x2){0x3F803F80u, 0x3F803F80u};
        if (c16 == 7) v = (u32x2){0u, 0u};
        __builtin_amdgcn_raw_buffer_store_b64(v, rs_ws, vo + (12 + c16) * 16, 0, AUX_SC1);
    }
    const unsigned tag = (unsigned)__builtin_amdgcn_readfirstlane(epoch) + 1u;
    DRAIN();
    arrive(L, C_P, lane);
    if (s == 0) {
        waitc(L, C_P, 4);
        if (lane < 8) gstore_sc1((unsigned*)(p.ws + WS_FLAG1 + lane * 4096 + (4 * xcd + (tt >> 2)) * 128), tag);
    }
    STAMP(6);
}

template <int G> DI void conv_group(lds_p L, lds_p R, f32x4 (&acc)[8], int pa0, int pb0) {
    if ((unsigned)__builtin_amdgcn_readfirstlane(peek(L, C_Q)) >= 8u) __builtin_amdgcn_s_setprio(S_PRIOC);
    lds_p a1 = R + pa0 - 64 * (8 * G + 8), b1 = R + pb0 - 64 * (8 * G + 8), a2 = R + pa0 - 64 * (63 - 8 * G), b2 = R + pb0 - 64 * (63 - 8 * G);
    PIN(a1); PIN(b1); PIN(a2); PIN(b2);
#define RDA(ptr) cat8(*(const volatile LAS bf16x4*)(ptr), *(const volatile LAS bf16x4*)((ptr) + 8))
    bf16x8 P1 = RDA(a1 + 512), P2 = RDA(a2), Q1, Q2, B1[8 - G], B2[G + 1];
#pragma unroll
    for (int n = 0; n < 8 - G; ++n) B1[n] = ldsr<bf16x8>(b1 + 512 + 512 * (G + n));
#pragma unroll
    for (int n = 0; n < G + 1; ++n) B2[n] = ldsr<bf16x8>(b2 + 512 * (7 - G + n));
#define CONV_STEP(i, C1, C2, N1, N2) do { \
        N1 = RDA(a1 + 64 * (7 - (i))); N2 = RDA(a2 + 64 * ((i) + 1)); SBAR(); \
        _Pragma("unroll") for (int n = 0; n < 8 - G; ++n) { acc[G + n] = MF16(C1, B1[n], acc[G + n]); SBAR(); B1[n] = ldsr<bf16x8>(b1 + 64 * (7 - (i)) + 512 * (G + n)); SBAR(); } \
        _Pragma("unroll") for (int n = 0; n < G + 1; ++n) { acc[7 - G + n] = MF16(C2, B2[n], acc[7 - G + n]); SBAR(); B2[n] = ldsr<bf16x8>(b2 + 64 * ((i) + 1) + 512 * (7 - G + n)); SBAR(); } \
    } while (0)
    CONV_STEP(0, P1, P2, Q1, Q2); CONV_STEP(1, Q1, Q2, P1, P2); CONV_STEP(2, P1, P2, Q1, Q2); CONV_STEP(3, Q1, Q2, P1, P2);
    CONV_STEP(4, P1, P2, Q1, Q2); CONV_STEP(5, Q1, Q2, P1, P2); CONV_STEP(6, P1, P2, Q1, Q2); CONV_STEP(7, Q1, Q2, P1, P2);
#undef CONV_STEP
#undef RDA
}

DI void ssm_wave(const Params& p, lds_p L, int s, int lane, int ch0 STAMP_ARG) {
    const int d = ch0 + s; const int w = 8 + s; (void)w;
    __builtin_amdgcn_s_setprio(S_PRIO0);
    lds_p R = L + L_S + s * SCH;
    const int pb = d * 128 + lane;
    unsigned short raw[13];
    raw[0] = p.are[pb]; raw[1] = p.are[pb + 64]; raw[2] = p.aim[pb]; raw[3] = p.aim[pb + 64]; raw[4] = p.bre[pb]; raw[5] = p.bre[pb + 64]; raw[6] = p.bim[pb]; raw[7] = p.bim[pb + 64];
    raw[8] = p.cre[pb]; raw[9] = p.cre[pb + 64]; raw[10] = p.cim[pb]; raw[11] = p.cim[pb + 64]; raw[12] = p.ldt[d];
    const unsigned short dsk = p.dsk[d];
    const int t0 = 512 * s + 8 * lane;
    SBAR();
    BARRIER();
    SBAR();
    const f32x2 are = {bf1(raw[0]), bf1(raw[1])}, aim = {bf1(raw[2]), bf1(raw[3])}, bre = {bf1(raw[4]), bf1(raw[5])}, bim = {bf1(raw[6]), bf1(raw[7])};
    const f32x2 cre = {bf1(raw[8]), bf1(raw[9])}, cim = {bf1(raw[10]), bf1(raw[11])};
    const float ldt = bf1(raw[12]);
    const float dt = bflo(cvtpk(expf(ldt), 0.f));
    const f32x2 ar2 = (are * dt) * LOG2E, airev = (aim * dt) * INV2PI;
    cplx2 dB; dB.re = bre * dt; dB.im = bim * dt;
    cplx2 C; C.re = cre; C.im = cim;
    const cplx2 CB = cmul(C, dB);
    const cplx2 w1 = cexp(ar2, airev, 1.f), w8 = cexp(ar2, airev, 8.f), w128 = cexp(ar2, airev, 128.f);

    STAMP(3);
    lds_p st0 = R + lane * 16, st1 = R + 4224 + lane * 16;
    const int kq = lane >> 4, q4 = (lane >> 2) & 3, p4 = lane & 3;
    lds_p trb = R + (p4 >> 1) * 4224 + (16 * (kq >> 1) + 4 * (kq & 1) + q4) * 16 + (p4 & 1) * 8;
#define PUT16(V, NEG) do { \
        u32x4 a0, a1, b0, b1, c0, c1, d0, d1; \
        _Pragma("unroll") for (int j = 0; j < 4; ++j) { \
            a0[j] = cvtpk(V[2 * j].re[0], V[2 * j + 1].re[0]); a1[j] = cvtpk(V[8 + 2 * j].re[0], V[9 + 2 * j].re[0]); \
            b0[j] = cvtpk(V[2 * j].re[1], V[2 * j + 1].re[1]); b1[j] = cvtpk(V[8 + 2 * j].re[1], V[9 + 2 * j].re[1]); \
            c0[j] = cvtpk(NEG V[2 * j].im[0], NEG V[2 * j + 1].im[0]); c1[j] = cvtpk(NEG V[8 + 2 * j].im[0], NEG V[9 + 2 * j].im[0]); \
            d0[j] = cvtpk(NEG V[2 * j].im[1], NEG V[2 * j + 1].im[1]); d1[j] = cvtpk(NEG V[8 + 2 * j].im[1], NEG V[9 + 2 * j].im[1]); } \
        ldsw(st0, a0); ldsw(st1, a1); ldsw(st0 + 1024, b0); ldsw(st1 + 1024, b1); \
        ldsw(st0 + 2048, c0); ldsw(st1 + 2048, c1); ldsw(st0 + 3072, d0); ldsw(st1 + 3072, d1); } while (0)
#define GETF(ks) cat8(ldtr(trb + (ks) * 512), ldtr(trb + (ks) * 512 + 128))
    bf16x8 yf[8];
    {
        cplx2 V[16];
        V[0].re = (f32x2){1.f, 1.f}; V[0].im = (f32x2){0.f, 0.f}; V[1].re = w8.re; V[1].im = -w8.im;
        V[2] = cmul(V[1], V[1]); V[3] = cmul(V[2], V[1]); V[4] = cmul(V[2], V[2]);
        V[5] = cmul(V[4], V[1]); V[6] = cmul(V[4], V[2]); V[7] = cmul(V[4], V[3]); V[8] = cmul(V[4], V[4]);
#pragma unroll
        for (int j = 9; j < 16; ++j) V[j] = cmul(V[8], V[j - 8]);
        PUT16(V, +);
#pragma unroll
        for (int ks = 0; ks < 8; ++ks) yf[ks] = GETF(ks);
    }
    STAMP(9);
    const cplx2 w2 = cmul(w1, w1), w3 = cmul(w2, w1), w4 = cmul(w2, w2), w5 = cmul(w4, w1), w6 = cmul(w4, w2), w7 = cmul(w4, w3);
    f32x4 D0 = {}, D1 = {}, D2 = {};
    cplx2 G = cmul(CB, w128);
    {
        cplx2 V[16];
        V[0] = CB; V[1] = cmul(CB, w1); V[2] = cmul(CB, w2); V[3] = cmul(CB, w3); V[4] = cmul(CB, w4); V[5] = cmul(CB, w5); V[6] = cmul(CB, w6); V[7] = cmul(CB, w7);
        V[8] = G; V[9] = cmul(G, w1); V[10] = cmul(G, w2); V[11] = cmul(G, w3); V[12] = cmul(G, w4); V[13] = cmul(G, w5); V[14] = cmul(G, w6); V[15] = cmul(G, w7);
        PUT16(V, +);
        bf16x8 xf[8];
#pragma unroll
        for (int ks = 0; ks < 8; ++ks) xf[ks] = GETF(ks);
        SBAR();
#pragma unroll
        for (int ks = 0; ks < 8; ++ks) D0 = MF16(xf[ks], yf[ks], D0);
    }
    PIN(D0); STAMP(10);
    {
        cplx2 V[16];
        G = cmul(G, w128); V[0] = G; V[1] = cmul(G, w2); V[2] = cmul(G, w4); V[3] = cmul(G, w6);
        G = cmul(G, w128); V[4] = G; V[5] = cmul(G, w2); V[6] = cmul(G, w4); V[7] = cmul(G, w6);
#pragma unroll
        for (int i = 0; i < 4; ++i) { G = cmul(G, w128); V[8 + 2 * i] = G; V[9 + 2 * i] = cmul(G, w4); }
        PUT16(V, +);
        bf16x8 xf[8];
#pragma unroll
        for (int ks = 0; ks < 8; ++ks) xf[ks] = GETF(ks);
        SBAR();
#pragma unroll
        for (int ks = 0; ks < 8; ++ks) D1 = MF16(xf[ks], yf[ks], D1);
    }
    {
        cplx2 V[16];
#pragma unroll
        for (int i = 0; i < 9; ++i) { G = cmul(G, w128); V[i] = G; }
#pragma unroll
        for (int i = 9; i < 16; ++i) V[i] = G;
        PUT16(V, +);
        bf16x8 xf[8];
#pragma unroll
        for (int ks = 0; ks < 8; ++ks) xf[ks] = GETF(ks);
        SBAR();
#pragma unroll
        for (int ks = 0; ks < 8; ++ks) D2 = MF16(yf[ks], xf[ks], D2);
    }
#undef PUT16
#undef GETF
    PIN(D2); STAMP(4);
    lds_p tab = R + 8448;
    {
        const int j = lane & 15, rq = lane >> 4;
        if (lane == 0) D0[0] += bf1(dsk);
        ldsw(tab + 2 * (8 * j + 4 * (rq & 1) + 128 * (rq >> 1)), (u32x2){cvtpk(D0[0], D0[1]), cvtpk(D0[2], D0[3])});
        const int e0 = rq < 2 ? 256 + 64 * rq + 4 * j : 384 + 64 * (rq - 2) + 2 * j, e1 = rq < 2 ? e0 + 2 : e0 + 32;
        ldsw(tab + 2 * e0, cvtpk(D1[0], D1[1])); ldsw(tab + 2 * e1, cvtpk(D1[2], D1[3]));
        if (j < 8 || lane == 8) ldsw(tab + 2 * (512 + 16 * j + 4 * rq), (u32x2){cvtpk(D2[0], D2[1]), cvtpk(D2[2], D2[3])});
    }
#define PK(hi_src, hi_half, lo_src, lo_half) __builtin_amdgcn_perm(hi_src, lo_src, ((hi_half) ? 0x07060000u : 0x05040000u) | ((lo_half) ? 0x0302u : 0x0100u))
    {
#pragma unroll
        for (int i = 0; i < 2; ++i) {
            const int g = lane + 64 * i, m = 255 - g;
            const unsigned lo = ldsr<unsigned short>(tab + 2 * (384 + m)), hi = ldsr<unsigned short>(tab + 2 * (385 + m));
            const unsigned mid = (m & 1) ? hi : lo, ll = PK(lo, 0, lo, 0);
            ldsw(R + 16 * g, (u32x4){PK(hi, 0, hi, 0), PK(mid, 0, hi, 0), ll, ll});
        }
        {
            const int g = 128 + lane, m = 255 - g;
            const unsigned ab = ldsr<unsigned>(tab + 2 * (256 + 2 * m)), c = ldsr<unsigned short>(tab + 2 * (258 + 2 * m));
            ldsw(R + 16 * g, (u32x4){PK(c, 0, c, 0), PK(ab, 1, ab, 1), PK(ab, 0, ab, 1), PK(ab, 0, ab, 0)});
        }
        {
            const int g = 192 + lane, m = 255 - g;
            u32x4 v;
            if (lane < 32) {
                const u32x2 q = ldsr<u32x2>(tab + 2 * (128 + 4 * m)); const unsigned e = ldsr<unsigned short>(tab + 2 * (132 + 4 * m));
                v = (u32x4){PK(q[1], 1, e, 0), PK(q[1], 0, q[1], 0), PK(q[0], 1, q[1], 0), PK(q[0], 0, q[0], 0)};
            } else {
                const u32x4 q = ldsr<u32x4>(tab + 16 * m);
                v = (u32x4){PK(q[3], 0, q[3], 1), PK(q[2], 0, q[2], 1), PK(q[1], 0, q[1], 1), PK(q[0], 0, q[0], 1)};
            }
            ldsw(R + 16 * g, v);
        }
        if (lane < 4) ldsw(R + 16 * (256 + lane), (u32x4){0u, 0u, 0u, 0u});
    }
#undef PK
    if (lane < 32) ldsw(R + SCH_U + 16 * lane, (u32x4){0u, 0u, 0u, 0u});
#pragma unroll
    for (int i = 0; i < 5; ++i) {
        const int g = lane + 64 * i;
        if (i < 4 || lane < 4) {
            const u32x4 a = ldsr<u32x4>(R + 16 * g); const u32x2 b = ldsr<u32x2>(R + 16 * g + 16);
            const unsigned a0 = __builtin_amdgcn_alignbit(a[1], a[0], 16), a1 = __builtin_amdgcn_alignbit(a[2], a[1], 16), a2 = __builtin_amdgcn_alignbit(a[3], a[2], 16),
                           a3 = __builtin_amdgcn_alignbit(b[0], a[3], 16), a4 = __builtin_amdgcn_alignbit(b[1], b[0], 16);
            ldsw(R + 4160 + 16 * g, (u32x4){a0, a1, a2, a3});
            ldsw(R + 8320 + 16 * g, (u32x4){a[1], a[2], a[3], b[0]});
            ldsw(R + 12480 + 16 * g, (u32x4){a1, a2, a3, a4});
        }
    }
    STAMP(5);
    waitc(L, C_U, 8);
    STAMP(6);
    f32x4 acc[8] = {};
    {
        const int b = lane & 15, kq = lane >> 4, c = (3 - b) & 3;
        const int pa0 = c * 4160 + 2 * (2031 - b + 8 * kq - c), pb0 = SCH_U + 480 + 32 * b + 16 * kq;
        __builtin_amdgcn_s_setprio(S_PRIOB);
        conv_group<0>(L, R, acc, pa0, pb0); conv_group<1>(L, R, acc, pa0, pb0); conv_group<2>(L, R, acc, pa0, pb0); conv_group<3>(L, R, acc, pa0, pb0);
        __builtin_amdgcn_s_setprio(3);
    }
    PIN(acc[0]); PIN(acc[7]); STAMP(7);
#pragma unroll
    for (int nu = 0; nu < 8; ++nu)
        ldsw(R + 512 * nu + 32 * (lane & 15) + 8 * (lane >> 4), (u32x2){cvtpk(acc[nu][0], acc[nu][1]), cvtpk(acc[nu][2], acc[nu][3])});
    arrive(L, C_S2, lane); waitc(L, C_S2, 4);
    {
        u32x4 res[4];
#pragma unroll
        for (int c = 0; c < 4; ++c) res[c] = ldsr<u32x4>(L + L_S + c * SCH + 2 * t0);
#pragma unroll
        for (int e = 0; e < 4; ++e) {
            unsigned short* op = p.out + (size_t)(t0 + 2 * e) * (2 * DM) + DM + ch0;
            *(u32x2*)op = (u32x2){__builtin_amdgcn_perm(res[1][e], res[0][e], 0x05040100u), __builtin_amdgcn_perm(res[3][e], res[2][e], 0x05040100u)};
            *(u32x2*)(op + 2 * DM) = (u32x2){__builtin_amdgcn_perm(res[1][e], res[0][e], 0x07060302u), __builtin_amdgcn_perm(res[3][e], res[2][e], 0x07060302u)};
        }
    }
    STAMP(8);

}

__global__ __launch_bounds__(768) void fused_kernel(Params p) {
    extern __shared__ __attribute__((aligned(16))) char smem[];
    lds_p L = (lds_p)smem;
    const unsigned lds0 = (unsigned)(uintptr_t)smem;
    const int tid = threadIdx.x, lane = tid & 63, w = __builtin_amdgcn_readfirstlane(tid >> 6);
    const int bid = blockIdx.x, xcd = bid & 7, idx = bid >> 3, tb = 2 * xcd + (idx & 1), pi = idx >> 1;
    if (tid < 64) ((LAS unsigned*)(L + L_C))[tid] = 0u;
    STAMP_DECL;
    STAMP(0);
    PRB(0);
    const __amdgpu_buffer_rsrc_t rs_ws = __builtin_amdgcn_make_buffer_rsrc((void*)p.ws, 0, WS_BYTES, RSRC_W3);

    if (w >= 8) {
        if (idx >= 14 && !MK_NOSSM) ssm_wave(p, L, w - 8, lane, 72 * xcd + 4 * (idx - 14) STAMP_PASS);
        else if (idx >= 10 && idx < 14) kv_wave(p, L, lds0, rs_ws, w - 8, lane, xcd, 4 * (idx - 10) + w - 8 STAMP_PASS);
        else BARRIER();
        STAMP(1);
        STAMP_DUMP();
        return;
    }

    const unsigned epoch = gload_sc1((const unsigned*)(p.ws + WS_EPOCH));
    SBAR();
    BARRIER();
    const int c16 = lane & 15, kq = lane >> 4, r32 = lane & 31, h = lane >> 5;
    const int q4 = (lane >> 2) & 3, p4 = lane & 3;
    const int kh = w >> 2, wq4 = w & 3;

    unsigned tag;
    bf16x8 qmA, qmB; unsigned qeA, qeB;
    {
        const int T0 = 128 * tb + 16 * w;
        const bool has_ssm = idx >= 14 && !MK_NOSSM;
        if (idx >= 10 && idx < 14) waitc(L, C_D, 4);
        {
            const __amdgpu_buffer_rsrc_t rs_wq = __builtin_amdgcn_make_buffer_rsrc((void*)p.wq, 0, DM * DM * 2, RSRC_W3);
            int kl = (lane * 43) >> 8, g = lane - 6 * kl;
            const unsigned dst = lds0 + L_W + w * 6912;
#pragma unroll
            for (int i = 0; i < 7; ++i) {
                const int vo = swap23(72 * w + kl) * 1152 + ((72 * pi) & ~15) + 16 * g;
                if (g < 5 && (i < 6 || lane < 48)) dma16(rs_wq, vo, dst + 1024 * i);
                g += 4; kl += 10; if (g >= 6) { g -= 6; kl += 1; }
            }
        }
        SBAR();
        arrive(L, C_X, lane); waitc(L, C_X, 8);
        constexpr int XPF = 4;
        u32x4 xa[9], xb[9];
        const unsigned short* xp = p.x + (size_t)(T0 + (lane >> 3)) * DM + 8 * (lane & 7);
#pragma unroll
        for (int s = 0; s < XPF; ++s) { xa[s] = *(const u32x4*)(xp + 64 * s); xb[s] = *(const u32x4*)(xp + 64 * s + 8 * DM); }
        SBAR();
        asm volatile("s_waitcnt vmcnt(8)" ::: "memory");
        SBAR();
        arrive(L, C_W, lane);
        f32x4 D0 = {}, D1 = {}, D2 = {};
        unsigned fl0 = 0;
        const unsigned* flp = (const unsigned*)(p.ws + WS_FLAG1 + xcd * 4096) + (lane & 31) * 32;
        {
            lds_p wb = L + L_W + (16 * (kq >> 1) + 4 * (kq & 1) + q4) * 96 + 8 * p4 + 8 * (pi & 1);
            lds_p xs = L + L_Q + w * 2048;
            lds_p xw = xs + (lane >> 3) * 128 + 16 * ((lane & 7) ^ (lane >> 4));
            lds_p xw2 = xs + 1024 + (lane >> 3) * 128 + 16 * ((lane & 7) ^ (4 + (lane >> 4)));
            lds_p xr0 = xs + c16 * 128 + 16 * (kq ^ ((c16 >> 1) & 7)), xr1 = xs + c16 * 128 + 16 * ((4 + kq) ^ ((c16 >> 1) & 7));
            lds_p wb2 = wb - 8 * p4 + 64;
            bf16x8 f0, f1, g0, g1;
            ldsw(xw, xa[0]); ldsw(xw2, xb[0]); f0 = ldsr<bf16x8>(xr0); f1 = ldsr<bf16x8>(xr1);
            waitc(L, C_W, 8);
            if (w >= 4) __builtin_amdgcn_s_setprio(1);
            STAMP(1);
            bf16x8 wf[2][3];
#define QRD(n, ks) do { wf[n][0] = cat8(ldtr(wb + (ks) * 3072), ldtr(wb + (ks) * 3072 + 768)); wf[n][1] = cat8(ldtr(wb + (ks) * 3072 + 32), ldtr(wb + (ks) * 3072 + 768 + 32)); \
                        wf[n][2] = cat8(ldtr(wb2 + (ks) * 3072), ldtr(wb2 + (ks) * 3072 + 768)); } while (0)
#define QMM(n, XF) do { D0 = MF16(wf[n][0], XF, D0); D1 = MF16(wf[n][1], XF, D1); D2 = MF16(wf[n][2], XF, D2); } while (0)
            QRD(0, 0);
#pragma unroll
            for (int s = 0; s < 9; ++s) {
                if (s + XPF < 9) { xa[s + XPF] = *(const u32x4*)(xp + 64 * (s + XPF)); xb[s + XPF] = *(const u32x4*)(xp + 64 * (s + XPF) + 8 * DM); }
                if (s == 8 && w == 7) fl0 = gload_sc1(flp);
                SBAR();
                QRD(1, 2 * s + 1);
                SBAR();
                if (s & 1) { QMM(0, g0); } else { QMM(0, f0); }
                SBAR();
                if (s < 8) {
                    ldsw(xw, xa[s + 1]); ldsw(xw2, xb[s + 1]);
                    if (s & 1) { f0 = ldsr<bf16x8>(xr0); } else { g0 = ldsr<bf16x8>(xr0); }
                    QRD(0, 2 * s + 2);
                }
                SBAR();
                if (s & 1) { QMM(1, g1); } else { QMM(1, f1); }
                SBAR();
                if (s < 8) { if (s & 1) { f1 = ldsr<bf16x8>(xr1); } else { g1 = ldsr<bf16x8>(xr1); } }
                SBAR();
            }
#undef QRD
#undef QMM
        }
        __builtin_amdgcn_s_setprio(0);
        STAMP(2);
        tag = (unsigned)__builtin_amdgcn_readfirstlane(epoch) + 1u;
        {
            lds_p qs = L + L_Q + w * 2048;
            ldsw(qs + c16 * 48 + 8 * kq, (u32x2){cvtpk(D0[0] * QSCALE, D0[1] * QSCALE), cvtpk(D0[2] * QSCALE, D0[3] * QSCALE)});
            const int a0 = kq == 0 ? c16 * 48 + 32 : (16 + c16) * 48 + 2 * (4 * kq - 2);
            const int a1 = (16 + c16) * 48 + 8 * kq;
            ldsw(qs + a0, cvtpk(D1[0] * QSCALE, D1[1] * QSCALE));
            ldsw(qs + a1, cvtpk(D1[2] * QSCALE, D1[3] * QSCALE));
            if (kq == 0) {
                ldsw(qs + (16 + c16) * 48 + 28, cvtpk(D2[0] * QSCALE, D2[1] * QSCALE));
                ldsw(qs + (16 + c16) * 48 + 32, cvtpk(D2[2] * QSCALE, D2[3] * QSCALE));
            }
        }
        arrive(L, C_Q, lane);
        if (w == 7) {
            if (!__all(fl0 == tag)) {
                for (unsigned spins = 0; spins < SPIN_GLB; ++spins) {
                    if (__all(gload_sc1(flp) == tag)) break;
                    __builtin_amdgcn_s_sleep(1);
                }
            }
            __builtin_amdgcn_fence(__ATOMIC_ACQUIRE, "wavefront");
            arrive(L, C_F1, lane);
            STAMP(8);
        }
        if (has_ssm) {
            u32x2 uu[4];
#pragma unroll
            for (int e = 0; e < 4; ++e) uu[e] = *(const u32x2*)(p.u + (size_t)(256 * w + 4 * lane + e) * DM + 72 * xcd + 4 * (idx - 14));
            const int uo = SCH_U + 512 + 2 * (256 * w + 4 * lane);
#pragma unroll
            for (int c = 0; c < 4; ++c) {
                const unsigned sel = (c & 1) ? 0x07060302u : 0x05040100u;
                ldsw(L + L_S + c * SCH + uo, (u32x2){__builtin_amdgcn_perm(uu[1][c >> 1], uu[0][c >> 1], sel), __builtin_amdgcn_perm(uu[3][c >> 1], uu[2][c >> 1], sel)});
            }
            arrive(L, C_U, lane);
        }
        waitc(L, C_Q, 8);
        {
            lds_p qa = L + L_Q + wq4 * 2048 + r32 * 48, qb = qa + 4 * 2048;
            qmA = ldsr<bf16x8>(qa + 16 * h); qmB = ldsr<bf16x8>(qb + 16 * h);
            qeA = ldsr<unsigned>(qa + 32); qeB = ldsr<unsigned>(qb + 32);
        }
        waitc(L, C_F1, 1);
        STAMP(3);
    }

    {
        lds_p ring = L + L_W + kh * (8 * KVT);
        const int dmo = WS_KV + (32 * kh + wq4) * KVT + lane * 16;
        const unsigned dmd = lds0 + L_W + kh * (8 * KVT) + wq4 * KVT;
#define KV_DMA_A(cc) do { dma16_sc1(rs_ws, dmo + (cc) * (4 * KVT), dmd + ((cc) & 1) * (4 * KVT)); dma16_sc1(rs_ws, dmo + (cc) * (4 * KVT) + 1024, dmd + ((cc) & 1) * (4 * KVT) + 1024); } while (0)
#define KV_DMA_B(cc) do { if (lane < 32) dma16_sc1(rs_ws, dmo + (cc) * (4 * KVT) + 2048, dmd + ((cc) & 1) * (4 * KVT) + 2048); } while (0)
        lds_p kb = ring + lane * 16, eb = ring + 2304 + r32 * 8, vb = ring + 1024 + (h * 20 + (r32 < 19 ? r32 : 19)) * 16;
        const f32x16 Z = {};
        unsigned mA = 0, mB = 0;
        f32x16 OA, OB;
        for (int pass = 0; pass < 2; ++pass) {
            KV_DMA_A(0); KV_DMA_B(0); KV_DMA_A(1); KV_DMA_B(1);
            asm volatile("s_waitcnt vmcnt(3)" ::: "memory");
            arrive(L, C_RW, lane); waitc(L, C_RW, 8 * (pass + 1));
            STAMP(9); if (pass == 0) PRB(1);
            if (pass == 0) {
                const bf16x8 km = ldsr<bf16x8>(L + L_W + lane * 16); const bf16x4 ke = ldsr<bf16x4>(L + L_W + 2304 + r32 * 8);
                const u32x2 ea = {h ? 0u : qeA, 0u}, ebq = {h ? 0u : qeB, 0u};
                f32x16 sa = MF32(km, qmA, Z), sb = MF32(km, qmB, Z);
                sa = MF32E(ke, __builtin_bit_cast(bf16x4, ea), sa); sb = MF32E(ke, __builtin_bit_cast(bf16x4, ebq), sb);
                float ma = sa[0], mb = sb[0];
#pragma unroll
                for (int i = 1; i < 16; ++i) { ma = fmaxf(ma, sa[i]); mb = fmaxf(mb, sb[i]); }
                auto ra = __builtin_amdgcn_permlane32_swap(__float_as_uint(ma), __float_as_uint(ma), false, false);
                auto rb = __builtin_amdgcn_permlane32_swap(__float_as_uint(mb), __float_as_uint(mb), false, false);
                ma = fmaxf(__uint_as_float(ra[0]), __uint_as_float(ra[1])); mb = fmaxf(__uint_as_float(rb[0]), __uint_as_float(rb[1]));
                const float ah = bflo(cvtpk(ma, 0.f)), bh = bflo(cvtpk(mb, 0.f));
                mA = cvtpk(-ah, ah - ma); mB = cvtpk(-bh, bh - mb);
            }
            const u32x2 ea = {h ? 0u : qeA, h ? 0u : mA}, ebq = {h ? 0u : qeB, h ? 0u : mB};
            const bf16x4 qxA = __builtin_bit_cast(bf16x4, ea), qxB = __builtin_bit_cast(bf16x4, ebq);
            f32x16 SA, SB = Z; u32x4 pa0, pa1, pb0 = {}, pb1 = {}; bf16x8 km, vx0, vx1, vy0, vy1; bf16x4 ke;
            OA = Z; OB = Z;
            km = ldsr<bf16x8>(kb); ke = ldsr<bf16x4>(eb);
            SA = MF32(km, qmA, Z); SA = MF32E(ke, qxA, SA);
            vx0 = ldsr<bf16x8>(vb); vx1 = ldsr<bf16x8>(vb + 640);
#define PH(SC, SN, QM, QX, ON, pp0, pp1, pn0, pn1, V0, V1, LD1, LD2, LD4) do { \
            ON = MF32(V0, BC8(pp0), ON); SBAR(); \
            LD1; \
            SC[0] = EXP2(SC[0]); SC[1] = EXP2(SC[1]); SC[2] = EXP2(SC[2]); SC[3] = EXP2(SC[3]); SBAR(); \
            SN = MF32(km, QM, Z); SBAR(); \
            LD2; \
            SC[4] = EXP2(SC[4]); SC[5] = EXP2(SC[5]); pn0[0] = cvtpk(SC[0], SC[1]); SC[6] = EXP2(SC[6]); SC[7] = EXP2(SC[7]); pn0[1] = cvtpk(SC[2], SC[3]); SBAR(); \
            ON = MF32(V1, BC8(pp1), ON); SBAR(); \
            SC[8] = EXP2(SC[8]); SC[9] = EXP2(SC[9]); pn0[2] = cvtpk(SC[4], SC[5]); SC[10] = EXP2(SC[10]); SC[11] = EXP2(SC[11]); pn0[3] = cvtpk(SC[6], SC[7]); PIN(pn0); SBAR(); \
            SN = MF32E(ke, QX, SN); SBAR(); \
            LD4; \
            SC[12] = EXP2(SC[12]); SC[13] = EXP2(SC[13]); pn1[0] = cvtpk(SC[8], SC[9]); SC[14] = EXP2(SC[14]); SC[15] = EXP2(SC[15]); pn1[1] = cvtpk(SC[10], SC[11]); \
            pn1[2] = cvtpk(SC[12], SC[13]); pn1[3] = cvtpk(SC[14], SC[15]); PIN(pn1); SBAR(); \
        } while (0)
#define VRD(t, N0, N1) (N0 = ldsr<bf16x8>(vb + (t) * KVT), N1 = ldsr<bf16x8>(vb + (t) * KVT + 640))
#define KMRD(t) (km = ldsr<bf16x8>(kb + (((t) + 1) & 7) * KVT))
#define KERD(t) (ke = ldsr<bf16x4>(eb + (((t) + 1) & 7) * KVT))
#define PHA(P0, P1, LD1, LD2, LD4) PH(SA, SB, qmB, qxB, OB, pb0, pb1, pa0, pa1, P0, P1, LD1, LD2, LD4);
#define PHB(N0, N1, LD1, LD2, LD4) PH(SB, SA, qmA, qxA, OA, pa0, pa1, pb0, pb1, N0, N1, LD1, LD2, LD4);
#define TA(t) PHA(vx0, vx1, VRD(t, vy0, vy1), KMRD(t), KERD(t))
#define TB() PHB(vy0, vy1, (void)0, (void)0, (void)0)
#define UA(t) PHA(vy0, vy1, VRD(t, vx0, vx1), KMRD(t), KERD(t))
#define UB() PHB(vx0, vx1, (void)0, (void)0, (void)0)
#define PRIO(x) __builtin_amdgcn_s_setprio(x);
#define LAST(t, b) \
            PHA(vy0, vy1, VRD(t, vx0, vx1), (void)0, (void)0) \
            asm volatile("s_waitcnt vmcnt(0)" ::: "memory"); \
            if ((b) < 7) { if (it == 0 && pass == 0) { arrive(L, C_B, lane); waitc(L, C_B, 8 * ((b) + 1)); } else __builtin_amdgcn_s_barrier(); } \
            CBAR(); PRIO(3) KMRD(t); KERD(t); SBAR(); \
            PHB(vx0, vx1, (void)0, if ((b) + 2 < 8) KV_DMA_A((b) + 2), if ((b) + 2 < 8) KV_DMA_B((b) + 2))
            PRIO(3)
            CSTAMP(10);
            for (int it = 0; it < 4; ++it) {
                TA(0) PRIO(2) TB() UA(1) PRIO(1) UB() TA(2) PRIO(0) TB()
                LAST(3, 2 * it)
                if (it == 1 && pass == 0) {
                    const __amdgpu_buffer_rsrc_t rs_wo = __builtin_amdgcn_make_buffer_rsrc((void*)p.wo, 0, DM * DM * 2, RSRC_W3);
                    int kp = 64 * w + lane; const int k9 = (kp * 7282) >> 16; int g = kp - 9 * k9; kp = k9;
                    for (int inst = w; inst < 81; inst += 8) {
                        dma16(rs_wo, swap23(kp) * 1152 + 144 * (pi >> 1) + 16 * g, lds0 + L_S + 1024 * inst);
                        g += 8; kp += 56; if (g >= 9) { g -= 9; kp += 1; }
                    }
                }
                TA(4) PRIO(2) TB() UA(5) PRIO(1) UB() TA(6) PRIO(0) TB()
                LAST(7, 2 * it + 1)
            }
            OB = MF32(vx0, BC8(pb0), OB); OB = MF32(vx1, BC8(pb1), OB);
#undef TA
#undef TB
#undef UA
#undef UB
#undef LAST
#undef PHA
#undef PHB
#undef VRD
#undef KMRD
#undef KERD
#undef PH
            PRIO(0)
            STAMP(4); CSTAMP(11); PRB(2);
            lds_p xo = (w < 4 ? L + L_X1 : L + L_Q) + wq4 * 3072, xi = (w < 4 ? L + L_Q : L + L_X1) + wq4 * 3072;
            {
                const f32x16& S = kh ? OA : OB;
#pragma unroll
                for (int g = 0; g < 3; ++g) ldsw(xo + g * 1024 + lane * 16, (f32x4){S[4 * g], S[4 * g + 1], S[4 * g + 2], S[4 * g + 3]});
            }
            BARRIER();
            STAMP(12);
            float o[12];
#pragma unroll
            for (int g = 0; g < 3; ++g) {
                const f32x4 t = ldsr<f32x4>(xi + g * 1024 + lane * 16);
#pragma unroll
                for (int i = 0; i < 4; ++i) o[4 * g + i] = (kh ? OB[4 * g + i] : OA[4 * g + i]) + t[i];
            }
            const float lsum = __uint_as_float(__builtin_amdgcn_permlane32_swap(__float_as_uint(o[10]), __float_as_uint(o[10]), false, false)[0]);
            const float rl = __builtin_amdgcn_rcpf(lsum);
            bool bad = !(lsum < 1.2676506e30f);
#pragma unroll
            for (int i = 0; i < 8; ++i) bad |= !(fabsf(o[i]) < 3.0e38f);
            if (h == 0) bad |= !(fabsf(o[8]) < 3.0e38f) || !(fabsf(o[9]) < 3.0e38f);
            {
                lds_p os = xi;
                lds_p ob = os + (r32 & 15) * 72 + (r32 >> 4) * 36 + 8 * h;
                ldsw(ob, cvtpk(o[0] * rl, o[1] * rl)); ldsw(ob + 4, cvtpk(o[2] * rl, o[3] * rl));
                ldsw(ob + 16, cvtpk(o[4] * rl, o[5] * rl)); ldsw(ob + 20, cvtpk(o[6] * rl, o[7] * rl));
                if (h == 0) ldsw(ob + 32, cvtpk(o[8] * rl, o[9] * rl));
#pragma unroll
                for (int i = 0; i < 3; ++i) {
                    const int e = lane + 64 * i;
                    if (e < 144) {
                        const int tk = e & 15, pc = e >> 4, k0 = 36 * pi + 4 * pc;
                        const u32x2 v = ldsr<u32x2>(os + tk * 72 + pc * 8);
                        __builtin_amdgcn_raw_buffer_store_b64(v, rs_ws, WS_O + (8 * tb + w) * 18432 + (k0 >> 3) * 256 + tk * 16 + (k0 & 4) * 2, 0, AUX_SC1);
                    }
                }
            }
            if (pass == 0 && __any(bad) && lane == 0) ((volatile LAS unsigned*)(L + L_C))[C_BAD] = 1u;
            STAMP(13);
            DRAIN();
            BARRIER();
            const bool redo = pass == 0 && __builtin_amdgcn_readfirstlane(((volatile LAS unsigned*)(L + L_C))[C_BAD]) != 0;
            if (!redo) break;
            {
                const u32x2 ea0 = {h ? 0u : qeA, 0u}, eb0 = {h ? 0u : qeB, 0u};
                float ma = -INFINITY, mb = -INFINITY;
                for (int t = 0; t < 64; ++t) {
                    const u32x4 kmv = __builtin_amdgcn_raw_buffer_load_b128(rs_ws, WS_KV + t * KVT + lane * 16, 0, AUX_SC1);
                    const u32x2 kev = __builtin_amdgcn_raw_buffer_load_b64(rs_ws, WS_KV + t * KVT + 2304 + r32 * 8, 0, AUX_SC1);
                    f32x16 sa = MF32(BC8(kmv), qmA, Z), sb = MF32(BC8(kmv), qmB, Z);
                    sa = MF32E(__builtin_bit_cast(bf16x4, kev), __builtin_bit_cast(bf16x4, ea0), sa);
                    sb = MF32E(__builtin_bit_cast(bf16x4, kev), __builtin_bit_cast(bf16x4, eb0), sb);
#pragma unroll
                    for (int i = 0; i < 16; ++i) { ma = fmaxf(ma, sa[i]); mb = fmaxf(mb, sb[i]); }
                }
                ma = fmaxf(ma, __shfl_xor(ma, 32)); mb = fmaxf(mb, __shfl_xor(mb, 32));
                const float ah = bflo(cvtpk(ma, 0.f)), bh = bflo(cvtpk(mb, 0.f));
                mA = cvtpk(-ah, ah - ma); mB = cvtpk(-bh, bh - mb);
            }
        }
#undef KV_DMA_A
#undef KV_DMA_B
#undef PRIO
        if (w == 0 && lane == 0) gstore_sc1((unsigned*)(p.ws + WS_FLAG2 + (tb * 16 + pi) * 128), tag);
        STAMP(5);
    }

    {
        constexpr int PF3 = 5;
        lds_p wb = L + L_S + (16 * (kq >> 1) + 4 * (kq & 1) + q4) * 144 + 8 * p4 + kh * (9 * 32 * 144);
        bf16x8 wf[PF3][5];
#define WORD3(i, T) cat8(ldtr(wb + (i) * 4608 + 32 * (T)), ldtr(wb + (i) * 4608 + 1152 + 32 * (T)))
#pragma unroll
        for (int i = 0; i < PF3; ++i)
#pragma unroll
            for (int T = 0; T < 5; ++T) wf[i][T] = WORD3(i, T);
        SBAR();
        if (w == 7) {
            const unsigned* f = (const unsigned*)(p.ws + WS_FLAG2 + tb * 2048) + (lane & 15) * 32;
            for (unsigned spins = 0; spins < SPIN_GLB; ++spins) {
                if (__all(gload_sc1(f) == tag)) break;
                __builtin_amdgcn_s_sleep(1);
            }
            __builtin_amdgcn_fence(__ATOMIC_ACQUIRE, "wavefront");
        }
        BARRIER();
        STAMP(6);
        const int Tt = 128 * tb + 64 * (pi & 1) + 16 * wq4, F0 = 72 * (pi >> 1);
        u32x4 of[9];
#pragma unroll
        for (int i = 0; i < 9; ++i) of[i] = __builtin_amdgcn_raw_buffer_load_b128(rs_ws, WS_O + (Tt >> 4) * 18432 + (9 * kh + i) * 1024 + lane * 16, 0, AUX_SC1);
        SBAR();
        f32x4 D[5] = {};
#pragma unroll
        for (int i = 0; i < 9; ++i) {
#pragma unroll
            for (int T = 0; T < 5; ++T) D[T] = MF16(wf[i % PF3][T], BC8(of[i]), D[T]);
            SBAR();
            if (i + PF3 < 9) {
#pragma unroll
                for (int T = 0; T < 5; ++T) wf[i % PF3][T] = WORD3(i + PF3, T);
            }
            SBAR();
        }
#undef WORD3
        lds_p xo = L + L_W + w * 3072, xi = L + L_W + (w ^ 4) * 3072;
        if (kh == 0) { ldsw(xo + lane * 16, D[3]); ldsw(xo + 1024 + lane * 16, D[4]); }
        else { ldsw(xo + lane * 16, D[0]); ldsw(xo + 1024 + lane * 16, D[1]); ldsw(xo + 2048 + lane * 16, D[2]); }
        BARRIER();
        unsigned short* op = p.out + (size_t)(Tt + c16) * (2 * DM) + F0 + 4 * kq;
        if (kh == 0) {
#pragma unroll
            for (int T = 0; T < 3; ++T) {
                const f32x4 v = D[T] + ldsr<f32x4>(xi + T * 1024 + lane * 16);
                *(u32x2*)(op + 16 * T) = (u32x2){cvtpk(v[0], v[1]), cvtpk(v[2], v[3])};
            }
        } else {
#pragma unroll
            for (int T = 3; T < 5; ++T) {
                const f32x4 v = D[T] + ldsr<f32x4>(xi + (T - 3) * 1024 + lane * 16);
                if (T == 3 || kq < 2) *(u32x2*)(op + 16 * T) = (u32x2){cvtpk(v[0], v[1]), cvtpk(v[2], v[3])};
            }
        }
        STAMP(7);
        STAMP_DUMP();
        if (bid == 0 && w == 7) {
            const unsigned* f = (const unsigned*)(p.ws + WS_FLAG2);
            for (unsigned spins = 0; spins < SPIN_GLB; ++spins) {
                bool ok = true;
#pragma unroll
                for (int i = 0; i < 4; ++i) { const int e = lane + 64 * i; ok &= gload_sc1(f + e * 32) == tag; }
                if (__all(ok)) break;
                __builtin_amdgcn_s_sleep(2);
            }
            if (lane == 0) gstore_sc1((unsigned*)(p.ws + WS_EPOCH), tag);
        }
    }
}

}

extern "C" void kernel_launch(void* const* d_in, const int* in_sizes, int n_in, void* d_out, int out_size, void* d_ws, size_t ws_size,
                              hipStream_t stream) {
    Params p;
    p.x = (const unsigned short*)d_in[0]; p.wq = (const unsigned short*)d_in[1]; p.wk = (const unsigned short*)d_in[2];
    p.wv = (const unsigned short*)d_in[3]; p.wo = (const unsigned short*)d_in[4]; p.u = (const unsigned short*)d_in[5];
    p.are = (const unsigned short*)d_in[6]; p.aim = (const unsigned short*)d_in[7]; p.bre = (const unsigned short*)d_in[8];
    p.bim = (const unsigned short*)d_in[9]; p.cre = (const unsigned short*)d_in[10]; p.cim = (const unsigned short*)d_in[11];
    p.ldt = (const unsigned short*)d_in[12]; p.dsk = (const unsigned short*)d_in[13];
    p.out = (unsigned short*)d_out; p.ws = (char*)d_ws;
    (void)hipFuncSetAttribute((const void*)fused_kernel, hipFuncAttributeMaxDynamicSharedMemorySize, L_END);
    fused_kernel<<<256, 768, L_END, stream>>>(p);
}
```

```cpp
#include <hip/hip_runtime.h>
#include <hip/hip_bf16.h>
#include <stdint.h>

#define S_PRIO0 0
#define U_BY_S 0
#define S_PRIOB 0
#define S_PRIOC 3
#define MK_PROBE 0
#define PRB(i) do {} while (0)
#define OUT_AUX_Y 0
#define OUT_AUX_A 0
#define MK_NOSSM 0
#define MK_STAMPS 0

namespace {

typedef __attribute__((ext_vector_type(16))) float f32x16;
typedef __attribute__((ext_vector_type(4))) float f32x4;
typedef __attribute__((ext_vector_type(2))) float f32x2;
typedef __attribute__((ext_vector_type(8))) short bf16x8;
typedef __attribute__((ext_vector_type(4))) short bf16x4;
typedef __attribute__((ext_vector_type(2))) __bf16 bf16x2_t;
typedef __attribute__((ext_vector_type(4))) unsigned u32x4;
typedef __attribute__((ext_vector_type(2))) unsigned u32x2;
typedef short v4i16_t __attribute__((ext_vector_type(4)));
#define LAS __attribute__((address_space(3)))
#define GAS __attribute__((address_space(1)))
typedef LAS char* lds_p;
typedef const unsigned short* cbf;

constexpr int SEQ = 2048, DM = 576;
constexpr float QSCALE = 0.34005040627f;
constexpr float LOG2E = 1.4426950408889634f;
constexpr float INV2PI = 0.15915494309189535f;

constexpr int WS_EPOCH = 0;
constexpr int WS_FLAG1 = 4096;
constexpr int WS_FLAG2 = WS_FLAG1 + 32768;
constexpr int WS_KV = WS_FLAG2 + 32768;
constexpr int KVT = 2560;
constexpr int WS_O = WS_KV + 64 * KVT;
constexpr int WS_BYTES = WS_O + SEQ * DM * 2;
constexpr int WS_STAMP = 8 * 1024 * 1024;

constexpr int L_W = 0;
constexpr int L_X1 = 40960;
constexpr int L_S = 55296;
constexpr int L_Q = L_S + 87360;
constexpr int L_C = L_Q + 16384;
constexpr int L_END = L_C + 256;
constexpr int SCH = 21840;
constexpr int SCH_U = 16640;
enum { C_W = 0, C_P, C_Q, C_F1, C_RW, C_B, C_BAD, C_U, C_S2, C_D, C_KW, C_X, C_NUM };

constexpr int RSRC_W3 = 0x00020000;
constexpr int AUX_SC1 = 16;
constexpr unsigned SPIN_LDS = 1u << 20, SPIN_GLB = 1u << 16;

#define SBAR() __builtin_amdgcn_sched_barrier(0)
#define PIN(x) asm volatile("" : "+v"(x))
#define CBAR() asm volatile("" ::: "memory")
#define DRAIN() asm volatile("s_waitcnt vmcnt(0)" ::: "memory")
#define MF32(a, b, c) __builtin_amdgcn_mfma_f32_32x32x16_bf16(a, b, c, 0, 0, 0)
#define MF32E(a, b, c) __builtin_amdgcn_mfma_f32_32x32x8bf16_1k(a, b, c, 0, 0, 0)
#define MF16(a, b, c) __builtin_amdgcn_mfma_f32_16x16x32_bf16(a, b, c, 0, 0, 0)
#define EXP2(x) __builtin_amdgcn_exp2f(x)
#define BC8(x) __builtin_bit_cast(bf16x8, x)
#define DI __device__ __forceinline__

DI unsigned cvtpk(float lo, float hi) { f32x2 v = {lo, hi}; return __builtin_bit_cast(unsigned, __builtin_convertvector(v, bf16x2_t)); }
DI float bflo(unsigned x) { return __builtin_bit_cast(float, x << 16); }
DI float bfhi(unsigned x) { return __builtin_bit_cast(float, x & 0xffff0000u); }
DI float bf1(unsigned short x) { return __builtin_bit_cast(float, (unsigned)x << 16); }
template <class T> DI T ldsr(lds_p p) { return *(const LAS T*)p; }
template <class T> DI void ldsw(lds_p p, T v) { *(LAS T*)p = v; }
DI bf16x4 ldtr(lds_p p) { return __builtin_bit_cast(bf16x4, __builtin_amdgcn_ds_read_tr16_b64_v4i16((LAS v4i16_t*)p)); }
DI bf16x8 cat8(bf16x4 a, bf16x4 b) { return __builtin_shufflevector(a, b, 0, 1, 2, 3, 4, 5, 6, 7); }
DI int swap23(int k) { return (k & ~12) | ((k & 4) << 1) | ((k & 8) >> 1); }
DI void dma16(__amdgpu_buffer_rsrc_t rs, int voff, unsigned lds_base) {
    unsigned keep;
    asm volatile("s_mov_b32 %0, m0\n\ts_mov_b32 m0, %3\n\ts_nop 0\n\tbuffer_load_dwordx4 %1, %2, 0 offen lds\n\ts_mov_b32 m0, %0"
                 : "=&s"(keep) : "v"(voff), "s"(rs), "s"(lds_base) : "memory");
}
DI void dma16_sc1(__amdgpu_buffer_rsrc_t rs, int voff, unsigned lds_base) {
    unsigned keep;
    asm volatile("s_mov_b32 %0, m0\n\ts_mov_b32 m0, %3\n\ts_nop 0\n\tbuffer_load_dwordx4 %1, %2, 0 offen sc1 lds\n\ts_mov_b32 m0, %0"
                 : "=&s"(keep) : "v"(voff), "s"(rs), "s"(lds_base) : "memory");
}
#define BARRIER() asm volatile("s_waitcnt lgkmcnt(0)\n\ts_barrier" ::: "memory")
DI void arrive(lds_p L, int c, int lane) {
    (void)lane;
    unsigned long long keep;
    asm volatile("s_mov_b64 %0, exec\n\ts_mov_b64 exec, 1\n\tds_add_u32 %1, %2\n\ts_mov_b64 exec, %0" : "=&s"(keep) : "v"((unsigned)(uintptr_t)(L + L_C + 4 * c)), "v"(1u) : "memory");
}
DI void waitc(lds_p L, int c, unsigned target) {
    CBAR();
    volatile LAS unsigned* p = (volatile LAS unsigned*)(L + L_C) + c;
    for (unsigned spins = 0; spins < SPIN_LDS; ++spins) {
        if ((unsigned)__builtin_amdgcn_readfirstlane(*p) >= target) break;
        __builtin_amdgcn_s_sleep(1);
    }
    CBAR();
}
DI unsigned peek(lds_p L, int c) { return *((volatile LAS unsigned*)(L + L_C) + c); }
DI void waitp(lds_p L, int c, unsigned seen, unsigned target) { if ((unsigned)__builtin_amdgcn_readfirstlane(seen) < target) waitc(L, c, target); CBAR(); }
DI unsigned gload_sc1(const unsigned* p) { return __hip_atomic_load((GAS unsigned*)p, __ATOMIC_RELAXED, __HIP_MEMORY_SCOPE_AGENT); }
DI void gstore_sc1(unsigned* p, unsigned v) { __hip_atomic_store((GAS unsigned*)p, v, __ATOMIC_RELAXED, __HIP_MEMORY_SCOPE_AGENT); }

struct cplx2 { f32x2 re, im; };
DI cplx2 cmul(const cplx2& a, const cplx2& b) { cplx2 r; r.re = a.re * b.re - a.im * b.im; r.im = a.re * b.im + a.im * b.re; return r; }
DI cplx2 cexp(f32x2 ar2, f32x2 airev, float e) {
    f32x2 m = ar2 * e, t = airev * e; cplx2 r;
#pragma unroll
    for (int i = 0; i < 2; ++i) {
        const float mag = EXP2(m[i]), fr = __builtin_amdgcn_fractf(t[i]);
        r.re[i] = mag * __builtin_amdgcn_cosf(fr); r.im[i] = mag * __builtin_amdgcn_sinf(fr);
    }
    return r;
}

struct Params {
    const unsigned short *x, *wq, *wk, *wv, *wo, *u, *are, *aim, *bre, *bim, *cre, *cim, *ldt, *dsk;
    unsigned short* out;
    char* ws;
};

#define STAMP(i) do {} while (0)
#define CSTAMP(i) do {} while (0)
#define STAMP_DECL do {} while (0)
#define STAMP_DUMP() do {} while (0)
#define STAMP_ARG
#define STAMP_PASS

DI void dma16s(__amdgpu_buffer_rsrc_t rs, int voff, int soff, unsigned lds_base) {
    unsigned keep;
    asm volatile("s_mov_b32 %0, m0\n\ts_mov_b32 m0, %4\n\ts_nop 0\n\tbuffer_load_dwordx4 %1, %2, %3 offen lds\n\ts_mov_b32 m0, %0"
                 : "=&s"(keep) : "v"(voff), "s"(rs), "s"(soff), "s"(lds_base) : "memory");
}
DI void kv_wave(const Params& p, lds_p L, unsigned lds0, __amdgpu_buffer_rsrc_t rs_ws, int s, int lane, int xcd, int tt STAMP_ARG) {
    __builtin_amdgcn_s_setprio(3);
    const unsigned epoch = gload_sc1((const unsigned*)(p.ws + WS_EPOCH));
    const int T0 = 256 * xcd + 16 * tt;
    {
        const __amdgpu_buffer_rsrc_t rs_wk = __builtin_amdgcn_make_buffer_rsrc((void*)p.wk, 0, DM * 18 * 2, RSRC_W3);
        const __amdgpu_buffer_rsrc_t rs_wv = __builtin_amdgcn_make_buffer_rsrc((void*)p.wv, 0, DM * 18 * 2, RSRC_W3);
        int vo[3];
#pragma unroll
        for (int j = 0; j < 3; ++j) { const int sg = 64 * j + lane, kp = (sg * 171) >> 9; vo[j] = swap23(kp) * 36 + 16 * (sg - 3 * kp); }
        for (int b = s; b < 9; b += 4) {
#pragma unroll
            for (int j = 0; j < 3; ++j) {
                dma16s(rs_wk, vo[j], 2304 * b, lds0 + L_S + 3072 * b + 1024 * j);
                dma16s(rs_wv, vo[j], 2304 * b, lds0 + L_S + 27648 + 3072 * b + 1024 * j);
            }
        }
    }
    SBAR();
    u32x4 xa[9], xb[9];
    {
        const unsigned short* xp = p.x + (size_t)(T0 + (lane >> 3)) * DM + 8 * (lane & 7);
#pragma unroll
        for (int i = 0; i < 9; ++i) { xa[i] = *(const u32x4*)(xp + 64 * i); xb[i] = *(const u32x4*)(xp + 64 * i + 8 * DM); }
    }
    SBAR();
    BARRIER();
    arrive(L, C_D, lane);
    STAMP(3);
    asm volatile("s_waitcnt vmcnt(18)" ::: "memory");
    arrive(L, C_KW, lane);
    const int c16 = lane & 15, kq = lane >> 4, q4 = (lane >> 2) & 3, p4 = lane & 3;
    lds_p xs = L + L_S + 55296 + s * 2048;
    lds_p xw = xs + (lane >> 3) * 128 + 16 * ((lane & 7) ^ (lane >> 4)), xw2 = xs + 1024 + (lane >> 3) * 128 + 16 * ((lane & 7) ^ (4 + (lane >> 4)));
    lds_p xr0 = xs + c16 * 128 + 16 * (kq ^ ((c16 >> 1) & 7)), xr1 = xs + c16 * 128 + 16 * ((4 + kq) ^ ((c16 >> 1) & 7));
    const int rowb = (16 * (kq >> 1) + 4 * (kq & 1) + q4) * 48;
    lds_p wk = L + L_S + rowb + 8 * p4, wv = wk + 27648, wx = L + L_S + rowb + 32 + (p4 == 1 ? 27648 : 0);
    bf16x8 f0, f1, g0, g1;
    ldsw(xw, xa[0]); ldsw(xw2, xb[0]); f0 = ldsr<bf16x8>(xr0); f1 = ldsr<bf16x8>(xr1);
    waitc(L, C_KW, 4);
    STAMP(4);
    f32x4 DK = {}, DV = {}, DX = {}, DY = {};
    bf16x8 wa[2], wbv[2], wc[2];
#define KVRD(n, ks) do { wa[n] = cat8(ldtr(wk + (ks) * 1536), ldtr(wk + (ks) * 1536 + 384)); wbv[n] = cat8(ldtr(wv + (ks) * 1536), ldtr(wv + (ks) * 1536 + 384)); \
                         wc[n] = cat8(ldtr(wx + (ks) * 1536), ldtr(wx + (ks) * 1536 + 384)); } while (0)
#define KVMM(n, XF) do { DK = MF16(wa[n], XF, DK); DX = MF16(wc[n], XF, DX);        \
                         DV = MF16(XF, wbv[n], DV); DY = MF16(XF, wc[n], DY);       } while (0)
    KVRD(0, 0);
#pragma unroll
    for (int i = 0; i < 9; ++i) {
        KVRD(1, 2 * i + 1);
        SBAR();
        if (i & 1) { KVMM(0, g0); } else { KVMM(0, f0); }
        SBAR();
        if (i < 8) {
            ldsw(xw, xa[i + 1]); ldsw(xw2, xb[i + 1]);
            if (i & 1) { f0 = ldsr<bf16x8>(xr0); } else { g0 = ldsr<bf16x8>(xr0); }
            KVRD(0, 2 * i + 2);
        }
        SBAR();
        if (i & 1) { KVMM(1, g1); } else { KVMM(1, f1); }
        SBAR();
        if (i < 8) { if (i & 1) { f1 = ldsr<bf16x8>(xr1); } else { g1 = ldsr<bf16x8>(xr1); } }
        SBAR();
    }
#undef KVRD
#undef KVMM
    STAMP(5);
    const int tbase = WS_KV + (8 * xcd + (tt >> 1)) * KVT, rr = 16 * (tt & 1) + c16, s16 = tt & 1;
    __builtin_amdgcn_raw_buffer_store_b64((u32x2){cvtpk(DK[0], DK[1]), cvtpk(DK[2], DK[3])}, rs_ws, tbase + ((kq >> 1) * 32 + rr) * 16 + (kq & 1) * 8, 0, AUX_SC1);
    if (kq == 0) __builtin_amdgcn_raw_buffer_store_b64((u32x2){cvtpk(DX[0], DX[1]), 0x3F803F80u}, rs_ws, tbase + 2304 + rr * 8, 0, AUX_SC1);
    const int vo = tbase + 1024 + (s16 * 2 + (kq & 1)) * 320 + (kq >> 1) * 8;
    __builtin_amdgcn_raw_buffer_store_b64((u32x2){cvtpk(DV[0], DV[1]), cvtpk(DV[2], DV[3])}, rs_ws, vo + c16 * 16, 0, AUX_SC1);
    if (c16 >= 4 && c16 < 8) {
        u32x2 v = {cvtpk(DY[0], DY[1]), cvtpk(DY[2], DY[3])};
        if (c16 == 6) v = (u32x2){0x3F803F80u, 0x3F803F80u};
        if (c16 == 7) v = (u32x2){0u, 0u};
        __builtin_amdgcn_raw_buffer_store_b64(v, rs_ws, vo + (12 + c16) * 16, 0, AUX_SC1);
    }
    const unsigned tag = (unsigned)__builtin_amdgcn_readfirstlane(epoch) + 1u;
    DRAIN();
    arrive(L, C_P, lane);
    if (s == 0) {
        waitc(L, C_P, 4);
        if (lane < 8) gstore_sc1((unsigned*)(p.ws + WS_FLAG1 + lane * 4096 + (4 * xcd + (tt >> 2)) * 128), tag);
    }
    STAMP(6);
}

template <int G> DI void conv_group(lds_p L, lds_p R, f32x4 (&acc)[8], int pa0, int pb0) {
    if ((unsigned)__builtin_amdgcn_readfirstlane(peek(L, C_Q)) >= 8u) __builtin_amdgcn_s_setprio(S_PRIOC);
    lds_p a1 = R + pa0 - 64 * (8 * G + 8), b1 = R + pb0 - 64 * (8 * G + 8), a2 = R + pa0 - 64 * (63 - 8 * G), b2 = R + pb0 - 64 * (63 - 8 * G);
    PIN(a1); PIN(b1); PIN(a2); PIN(b2);
#define RDA(ptr) cat8(*(const volatile LAS bf16x4*)(ptr), *(const volatile LAS bf16x4*)((ptr) + 8))
    bf16x8 P1 = RDA(a1 + 512), P2 = RDA(a2), Q1, Q2, B1[8 - G], B2[G + 1];
#pragma unroll
    for (int n = 0; n < 8 - G; ++n) B1[n] = ldsr<bf16x8>(b1 + 512 + 512 * (G + n));
#pragma unroll
    for (int n = 0; n < G + 1; ++n) B2[n] = ldsr<bf16x8>(b2 + 512 * (7 - G + n));
#define CONV_STEP(i, C1, C2, N1, N2) do { \
        N1 = RDA(a1 + 64 * (7 - (i))); N2 = RDA(a2 + 64 * ((i) + 1)); SBAR(); \
        _Pragma("unroll") for (int n = 0; n < 8 - G; ++n) { acc[G + n] = MF16(C1, B1[n], acc[G + n]); SBAR(); B1[n] = ldsr<bf16x8>(b1 + 64 * (7 - (i)) + 512 * (G + n)); SBAR(); } \
        _Pragma("unroll") for (int n = 0; n < G + 1; ++n) { acc[7 - G + n] = MF16(C2, B2[n], acc[7 - G + n]); SBAR(); B2[n] = ldsr<bf16x8>(b2 + 64 * ((i) + 1) + 512 * (7 - G + n)); SBAR(); } \
    } while (0)
    CONV_STEP(0, P1, P2, Q1, Q2); CONV_STEP(1, Q1, Q2, P1, P2); CONV_STEP(2, P1, P2, Q1, Q2); CONV_STEP(3, Q1, Q2, P1, P2);
    CONV_STEP(4, P1, P2, Q1, Q2); CONV_STEP(5, Q1, Q2, P1, P2); CONV_STEP(6, P1, P2, Q1, Q2); CONV_STEP(7, Q1, Q2, P1, P2);
#undef CONV_STEP
#undef RDA
}

DI void ssm_wave(const Params& p, lds_p L, int s, int lane, int ch0 STAMP_ARG) {
    const int d = ch0 + s; const int w = 8 + s; (void)w;
    __builtin_amdgcn_s_setprio(S_PRIO0);
    lds_p R = L + L_S + s * SCH;
    const int pb = d * 128 + lane;
    unsigned short raw[13];
    raw[0] = p.are[pb]; raw[1] = p.are[pb + 64]; raw[2] = p.aim[pb]; raw[3] = p.aim[pb + 64]; raw[4] = p.bre[pb]; raw[5] = p.bre[pb + 64]; raw[6] = p.bim[pb]; raw[7] = p.bim[pb + 64];
    raw[8] = p.cre[pb]; raw[9] = p.cre[pb + 64]; raw[10] = p.cim[pb]; raw[11] = p.cim[pb + 64]; raw[12] = p.ldt[d];
    const unsigned short dsk = p.dsk[d];
    const int t0 = 512 * s + 8 * lane;
    SBAR();
    BARRIER();
    SBAR();
    const f32x2 are = {bf1(raw[0]), bf1(raw[1])}, aim = {bf1(raw[2]), bf1(raw[3])}, bre = {bf1(raw[4]), bf1(raw[5])}, bim = {bf1(raw[6]), bf1(raw[7])};
    const f32x2 cre = {bf1(raw[8]), bf1(raw[9])}, cim = {bf1(raw[10]), bf1(raw[11])};
    const float ldt = bf1(raw[12]);
    const float dt = bflo(cvtpk(expf(ldt), 0.f));
    const f32x2 ar2 = (are * dt) * LOG2E, airev = (aim * dt) * INV2PI;
    cplx2 dB; dB.re = bre * dt; dB.im = bim * dt;
    cplx2 C; C.re = cre; C.im = cim;
    const cplx2 CB = cmul(C, dB);
    const cplx2 w1 = cexp(ar2, airev, 1.f), w8 = cexp(ar2, airev, 8.f), w128 = cexp(ar2, airev, 128.f);

    STAMP(3);
    lds_p st0 = R + lane * 16, st1 = R + 4224 + lane * 16;
    const int kq = lane >> 4, q4 = (lane >> 2) & 3, p4 = lane & 3;
    lds_p trb = R + (p4 >> 1) * 4224 + (16 * (kq >> 1) + 4 * (kq & 1) + q4) * 16 + (p4 & 1) * 8;
#define PUT16(V, NEG) do { \
        u32x4 a0, a1, b0, b1, c0, c1, d0, d1; \
        _Pragma("unroll") for (int j = 0; j < 4; ++j) { \
            a0[j] = cvtpk(V[2 * j].re[0], V[2 * j + 1].re[0]); a1[j] = cvtpk(V[8 + 2 * j].re[0], V[9 + 2 * j].re[0]); \
            b0[j] = cvtpk(V[2 * j].re[1], V[2 * j + 1].re[1]); b1[j] = cvtpk(V[8 + 2 * j].re[1], V[9 + 2 * j].re[1]); \
            c0[j] = cvtpk(NEG V[2 * j].im[0], NEG V[2 * j + 1].im[0]); c1[j] = cvtpk(NEG V[8 + 2 * j].im[0], NEG V[9 + 2 * j].im[0]); \
            d0[j] = cvtpk(NEG V[2 * j].im[1], NEG V[2 * j + 1].im[1]); d1[j] = cvtpk(NEG V[8 + 2 * j].im[1], NEG V[9 + 2 * j].im[1]); } \
        ldsw(st0, a0); ldsw(st1, a1); ldsw(st0 + 1024, b0); ldsw(st1 + 1024, b1); \
        ldsw(st0 + 2048, c0); ldsw(st1 + 2048, c1); ldsw(st0 + 3072, d0); ldsw(st1 + 3072, d1); } while (0)
#define GETF(ks) cat8(ldtr(trb + (ks) * 512), ldtr(trb + (ks) * 512 + 128))
    bf16x8 yf[8];
    {
        cplx2 V[16];
        V[0].re = (f32x2){1.f, 1.f}; V[0].im = (f32x2){0.f, 0.f}; V[1].re = w8.re; V[1].im = -w8.im;
        V[2] = cmul(V[1], V[1]); V[3] = cmul(V[2], V[1]); V[4] = cmul(V[2], V[2]);
        V[5] = cmul(V[4], V[1]); V[6] = cmul(V[4], V[2]); V[7] = cmul(V[4], V[3]); V[8] = cmul(V[4], V[4]);
#pragma unroll
        for (int j = 9; j < 16; ++j) V[j] = cmul(V[8], V[j - 8]);
        PUT16(V, +);
#pragma unroll
        for (int ks = 0; ks < 8; ++ks) yf[ks] = GETF(ks);
    }
    STAMP(9);
    const cplx2 w2 = cmul(w1, w1), w3 = cmul(w2, w1), w4 = cmul(w2, w2), w5 = cmul(w4, w1), w6 = cmul(w4, w2), w7 = cmul(w4, w3);
    f32x4 D0 = {}, D1 = {}, D2 = {};
    cplx2 G = cmul(CB, w128);
    {
        cplx2 V[16];
        V[0] = CB; V[1] = cmul(CB, w1); V[2] = cmul(CB, w2); V[3] = cmul(CB, w3); V[4] = cmul(CB, w4); V[5] = cmul(CB, w5); V[6] = cmul(CB, w6); V[7] = cmul(CB, w7);
        V[8] = G; V[9] = cmul(G, w1); V[10] = cmul(G, w2); V[11] = cmul(G, w3); V[12] = cmul(G, w4); V[13] = cmul(G, w5); V[14] = cmul(G, w6); V[15] = cmul(G, w7);
        PUT16(V, +);
        bf16x8 xf[8];
#pragma unroll
        for (int ks = 0; ks < 8; ++ks) xf[ks] = GETF(ks);
        SBAR();
#pragma unroll
        for (int ks = 0; ks < 8; ++ks) D0 = MF16(xf[ks], yf[ks], D0);
    }
    PIN(D0); STAMP(10);
    {
        cplx2 V[16];
        G = cmul(G, w128); V[0] = G; V[1] = cmul(G, w2); V[2] = cmul(G, w4); V[3] = cmul(G, w6);
        G = cmul(G, w128); V[4] = G; V[5] = cmul(G, w2); V[6] = cmul(G, w4); V[7] = cmul(G, w6);
#pragma unroll
        for (int i = 0; i < 4; ++i) { G = cmul(G, w128); V[8 + 2 * i] = G; V[9 + 2 * i] = cmul(G, w4); }
        PUT16(V, +);
        bf16x8 xf[8];
#pragma unroll
        for (int ks = 0; ks < 8; ++ks) xf[ks] = GETF(ks);
        SBAR();
#pragma unroll
        for (int ks = 0; ks < 8; ++ks) D1 = MF16(xf[ks], yf[ks], D1);
    }
    {
        cplx2 V[16];
#pragma unroll
        for (int i = 0; i < 9; ++i) { G = cmul(G, w128); V[i] = G; }
#pragma unroll
        for (int i = 9; i < 16; ++i) V[i] = G;
        PUT16(V, +);
        bf16x8 xf[8];
#pragma unroll
        for (int ks = 0; ks < 8; ++ks) xf[ks] = GETF(ks);
        SBAR();
#pragma unroll
        for (int ks = 0; ks < 8; ++ks) D2 = MF16(yf[ks], xf[ks], D2);
    }
#undef PUT16
#undef GETF
    PIN(D2); STAMP(4);
    lds_p tab = R + 8448;
    {
        const int j = lane & 15, rq = lane >> 4;
        if (lane == 0) D0[0] += bf1(dsk);
        ldsw(tab + 2 * (8 * j + 4 * (rq & 1) + 128 * (rq >> 1)), (u32x2){cvtpk(D0[0], D0[1]), cvtpk(D0[2], D0[3])});
        const int e0 = rq < 2 ? 256 + 64 * rq + 4 * j : 384 + 64 * (rq - 2) + 2 * j, e1 = rq < 2 ? e0 + 2 : e0 + 32;
        ldsw(tab + 2 * e0, cvtpk(D1[0], D1[1])); ldsw(tab + 2 * e1, cvtpk(D1[2], D1[3]));
        if (j < 8 || lane == 8) ldsw(tab + 2 * (512 + 16 * j + 4 * rq), (u32x2){cvtpk(D2[0], D2[1]), cvtpk(D2[2], D2[3])});
    }
#define PK(hi_src, hi_half, lo_src, lo_half) __builtin_amdgcn_perm(hi_src, lo_src, ((hi_half) ? 0x07060000u : 0x05040000u) | ((lo_half) ? 0x0302u : 0x0100u))
    {
#pragma unroll
        for (int i = 0; i < 2; ++i) {
            const int g = lane + 64 * i, m = 255 - g;
            const unsigned lo = ldsr<unsigned short>(tab + 2 * (384 + m)), hi = ldsr<unsigned short>(tab + 2 * (385 + m));
            const unsigned mid = (m & 1) ? hi : lo, ll = PK(lo, 0, lo, 0);
            ldsw(R + 16 * g, (u32x4){PK(hi, 0, hi, 0), PK(mid, 0, hi, 0), ll, ll});
        }
        {
            const int g = 128 + lane, m = 255 - g;
            const unsigned ab = ldsr<unsigned>(tab + 2 * (256 + 2 * m)), c = ldsr<unsigned short>(tab + 2 * (258 + 2 * m));
            ldsw(R + 16 * g, (u32x4){PK(c, 0, c, 0), PK(ab, 1, ab, 1), PK(ab, 0, ab, 1), PK(ab, 0, ab, 0)});
        }
        {
            const int g = 192 + lane, m = 255 - g;
            u32x4 v;
            if (lane < 32) {
                const u32x2 q = ldsr<u32x2>(tab + 2 * (128 + 4 * m)); const unsigned e = ldsr<unsigned short>(tab + 2 * (132 + 4 * m));
                v = (u32x4){PK(q[1], 1, e, 0), PK(q[1], 0, q[1], 0), PK(q[0], 1, q[1], 0), PK(q[0], 0, q[0], 0)};
            } else {
                const u32x4 q = ldsr<u32x4>(tab + 16 * m);
                v = (u32x4){PK(q[3], 0, q[3], 1), PK(q[2], 0, q[2], 1), PK(q[1], 0, q[1], 1), PK(q[0], 0, q[0], 1)};
            }
            ldsw(R + 16 * g, v);
        }
        if (lane < 4) ldsw(R + 16 * (256 + lane), (u32x4){0u, 0u, 0u, 0u});
    }
#undef PK
    if (lane < 32) ldsw(R + SCH_U + 16 * lane, (u32x4){0u, 0u, 0u, 0u});
#pragma unroll
    for (int i = 0; i < 5; ++i) {
        const int g = lane + 64 * i;
        if (i < 4 || lane < 4) {
            const u32x4 a = ldsr<u32x4>(R + 16 * g); const u32x2 b = ldsr<u32x2>(R + 16 * g + 16);
            const unsigned a0 = __builtin_amdgcn_alignbit(a[1], a[0], 16), a1 = __builtin_amdgcn_alignbit(a[2], a[1], 16), a2 = __builtin_amdgcn_alignbit(a[3], a[2], 16),
                           a3 = __builtin_amdgcn_alignbit(b[0], a[3], 16), a4 = __builtin_amdgcn_alignbit(b[1], b[0], 16);
            ldsw(R + 4160 + 16 * g, (u32x4){a0, a1, a2, a3});
            ldsw(R + 8320 + 16 * g, (u32x4){a[1], a[2], a[3], b[0]});
            ldsw(R + 12480 + 16 * g, (u32x4){a1, a2, a3, a4});
        }
    }
    STAMP(5);
    waitc(L, C_U, 8);
    STAMP(6);
    f32x4 acc[8] = {};
    {
        const int b = lane & 15, kq = lane >> 4, c = (3 - b) & 3;
        const int pa0 = c * 4160 + 2 * (2031 - b + 8 * kq - c), pb0 = SCH_U + 480 + 32 * b + 16 * kq;
        __builtin_amdgcn_s_setprio(S_PRIOB);
        conv_group<0>(L, R, acc, pa0, pb0); conv_group<1>(L, R, acc, pa0, pb0); conv_group<2>(L, R, acc, pa0, pb0); conv_group<3>(L, R, acc, pa0, pb0);
        __builtin_amdgcn_s_setprio(3);
    }
    PIN(acc[0]); PIN(acc[7]); STAMP(7);
#pragma unroll
    for (int nu = 0; nu < 8; ++nu)
        ldsw(R + 512 * nu + 32 * (lane & 15) + 8 * (lane >> 4), (u32x2){cvtpk(acc[nu][0], acc[nu][1]), cvtpk(acc[nu][2], acc[nu][3])});
    arrive(L, C_S2, lane); waitc(L, C_S2, 4);
    {
        u32x4 res[4];
#pragma unroll
        for (int c = 0; c < 4; ++c) res[c] = ldsr<u32x4>(L + L_S + c * SCH + 2 * t0);
        const __amdgpu_buffer_rsrc_t rs_out = __builtin_amdgcn_make_buffer_rsrc((void*)p.out, 0, SEQ * 2 * DM * 2, RSRC_W3);
#pragma unroll
        for (int e = 0; e < 4; ++e) {
            const int oo = ((t0 + 2 * e) * (2 * DM) + DM + ch0) * 2;
            __builtin_amdgcn_raw_buffer_store_b64((u32x2){__builtin_amdgcn_perm(res[1][e], res[0][e], 0x05040100u), __builtin_amdgcn_perm(res[3][e], res[2][e], 0x05040100u)}, rs_out, oo, 0, OUT_AUX_Y);
            __builtin_amdgcn_raw_buffer_store_b64((u32x2){__builtin_amdgcn_perm(res[1][e], res[0][e], 0x07060302u), __builtin_amdgcn_perm(res[3][e], res[2][e], 0x07060302u)}, rs_out, oo + 4 * DM, 0, OUT_AUX_Y);
        }
    }
    STAMP(8);

}

__global__ __launch_bounds__(768) void fused_kernel(Params p) {
    extern __shared__ __attribute__((aligned(16))) char smem[];
    lds_p L = (lds_p)smem;
    const unsigned lds0 = (unsigned)(uintptr_t)smem;
    const int tid = threadIdx.x, lane = tid & 63, w = __builtin_amdgcn_readfirstlane(tid >> 6);
    const int bid = blockIdx.x, xcd = bid & 7, idx = bid >> 3, tb = 2 * xcd + (idx & 1), pi = idx >> 1;
    if (tid < 64) ((LAS unsigned*)(L + L_C))[tid] = 0u;
    STAMP_DECL;
    STAMP(0);
    PRB(0);
    const __amdgpu_buffer_rsrc_t rs_ws = __builtin_amdgcn_make_buffer_rsrc((void*)p.ws, 0, WS_BYTES, RSRC_W3);

    if (w >= 8) {
        if (idx >= 14 && !MK_NOSSM) ssm_wave(p, L, w - 8, lane, 72 * xcd + 4 * (idx - 14) STAMP_PASS);
        else if (idx >= 10 && idx < 14) kv_wave(p, L, lds0, rs_ws, w - 8, lane, xcd, 4 * (idx - 10) + w - 8 STAMP_PASS);
        else BARRIER();
        STAMP(1);
        STAMP_DUMP();
        return;
    }

    const unsigned epoch = gload_sc1((const unsigned*)(p.ws + WS_EPOCH));
    SBAR();
    BARRIER();
    const int c16 = lane & 15, kq = lane >> 4, r32 = lane & 31, h = lane >> 5;
    const int q4 = (lane >> 2) & 3, p4 = lane & 3;
    const int kh = w >> 2, wq4 = w & 3;

    unsigned tag;
    bf16x8 qmA, qmB; unsigned qeA, qeB;
    {
        const int T0 = 128 * tb + 16 * w;
        const bool has_ssm = idx >= 14 && !MK_NOSSM;
        if (idx >= 10 && idx < 14) waitc(L, C_D, 4);
        {
            const __amdgpu_buffer_rsrc_t rs_wq = __builtin_amdgcn_make_buffer_rsrc((void*)p.wq, 0, DM * DM * 2, RSRC_W3);
            int kl = (lane * 43) >> 8, g = lane - 6 * kl;
            const unsigned dst = lds0 + L_W + w * 6912;
#pragma unroll
            for (int i = 0; i < 7; ++i) {
                const int vo = swap23(72 * w + kl) * 1152 + ((72 * pi) & ~15) + 16 * g;
                if (g < 5 && (i < 6 || lane < 48)) dma16(rs_wq, vo, dst + 1024 * i);
                g += 4; kl += 10; if (g >= 6) { g -= 6; kl += 1; }
            }
        }
        SBAR();
        arrive(L, C_X, lane); waitc(L, C_X, 8);
        constexpr int XPF = 4;
        u32x4 xa[9], xb[9];
        const unsigned short* xp = p.x + (size_t)(T0 + (lane >> 3)) * DM + 8 * (lane & 7);
#pragma unroll
        for (int s = 0; s < XPF; ++s) { xa[s] = *(const u32x4*)(xp + 64 * s); xb[s] = *(const u32x4*)(xp + 64 * s + 8 * DM); }
        SBAR();
        asm volatile("s_waitcnt vmcnt(8)" ::: "memory");
        SBAR();
        arrive(L, C_W, lane);
        f32x4 D0 = {}, D1 = {}, D2 = {};
        unsigned fl0 = 0;
        const unsigned* flp = (const unsigned*)(p.ws + WS_FLAG1 + xcd * 4096) + (lane & 31) * 32;
        {
            lds_p wb = L + L_W + (16 * (kq >> 1) + 4 * (kq & 1) + q4) * 96 + 8 * p4 + 8 * (pi & 1);
            lds_p xs = L + L_Q + w * 2048;
            lds_p xw = xs + (lane >> 3) * 128 + 16 * ((lane & 7) ^ (lane >> 4));
            lds_p xw2 = xs + 1024 + (lane >> 3) * 128 + 16 * ((lane & 7) ^ (4 + (lane >> 4)));
            lds_p xr0 = xs + c16 * 128 + 16 * (kq ^ ((c16 >> 1) & 7)), xr1 = xs + c16 * 128 + 16 * ((4 + kq) ^ ((c16 >> 1) & 7));
            lds_p wb2 = wb - 8 * p4 + 64;
            bf16x8 f0, f1, g0, g1;
            ldsw(xw, xa[0]); ldsw(xw2, xb[0]); f0 = ldsr<bf16x8>(xr0); f1 = ldsr<bf16x8>(xr1);
            waitc(L, C_W, 8);
            if (w >= 4) __builtin_amdgcn_s_setprio(1);
            STAMP(1);
            bf16x8 wf[2][3];
#define QRD(n, ks) do { wf[n][0] = cat8(ldtr(wb + (ks) * 3072), ldtr(wb + (ks) * 3072 + 768)); wf[n][1] = cat8(ldtr(wb + (ks) * 3072 + 32), ldtr(wb + (ks) * 3072 + 768 + 32)); \
                        wf[n][2] = cat8(ldtr(wb2 + (ks) * 3072), ldtr(wb2 + (ks) * 3072 + 768)); } while (0)
#define QMM(n, XF) do { D0 = MF16(wf[n][0], XF, D0); D1 = MF16(wf[n][1], XF, D1); D2 = MF16(wf[n][2], XF, D2); } while (0)
            QRD(0, 0);
#pragma unroll
            for (int s = 0; s < 9; ++s) {
                if (s + XPF < 9) { xa[s + XPF] = *(const u32x4*)(xp + 64 * (s + XPF)); xb[s + XPF] = *(const u32x4*)(xp + 64 * (s + XPF) + 8 * DM); }
                if (s == 8 && w == 7) fl0 = gload_sc1(flp);
                SBAR();
                QRD(1, 2 * s + 1);
                SBAR();
                if (s & 1) { QMM(0, g0); } else { QMM(0, f0); }
                SBAR();
                if (s < 8) {
                    ldsw(xw, xa[s + 1]); ldsw(xw2, xb[s + 1]);
                    if (s & 1) { f0 = ldsr<bf16x8>(xr0); } else { g0 = ldsr<bf16x8>(xr0); }
                    QRD(0, 2 * s + 2);
                }
                SBAR();
                if (s & 1) { QMM(1, g1); } else { QMM(1, f1); }
                SBAR();
                if (s < 8) { if (s & 1) { f1 = ldsr<bf16x8>(xr1); } else { g1 = ldsr<bf16x8>(xr1); } }
                SBAR();
            }
#undef QRD
#undef QMM
        }
        __builtin_amdgcn_s_setprio(0);
        STAMP(2);
        tag = (unsigned)__builtin_amdgcn_readfirstlane(epoch) + 1u;
        {
            lds_p qs = L + L_Q + w * 2048;
            ldsw(qs + c16 * 48 + 8 * kq, (u32x2){cvtpk(D0[0] * QSCALE, D0[1] * QSCALE), cvtpk(D0[2] * QSCALE, D0[3] * QSCALE)});
            const int a0 = kq == 0 ? c16 * 48 + 32 : (16 + c16) * 48 + 2 * (4 * kq - 2);
            const int a1 = (16 + c16) * 48 + 8 * kq;
            ldsw(qs + a0, cvtpk(D1[0] * QSCALE, D1[1] * QSCALE));
            ldsw(qs + a1, cvtpk(D1[2] * QSCALE, D1[3] * QSCALE));
            if (kq == 0) {
                ldsw(qs + (16 + c16) * 48 + 28, cvtpk(D2[0] * QSCALE, D2[1] * QSCALE));
                ldsw(qs + (16 + c16) * 48 + 32, cvtpk(D2[2] * QSCALE, D2[3] * QSCALE));
            }
        }
        arrive(L, C_Q, lane);
        if (w == 7) {
            if (!__all(fl0 == tag)) {
                for (unsigned spins = 0; spins < SPIN_GLB; ++spins) {
                    if (__all(gload_sc1(flp) == tag)) break;
                    __builtin_amdgcn_s_sleep(1);
                }
            }
            __builtin_amdgcn_fence(__ATOMIC_ACQUIRE, "wavefront");
            arrive(L, C_F1, lane);
            STAMP(8);
        }
        if (has_ssm) {
            u32x2 uu[4];
#pragma unroll
            for (int e = 0; e < 4; ++e) uu[e] = *(const u32x2*)(p.u + (size_t)(256 * w + 4 * lane + e) * DM + 72 * xcd + 4 * (idx - 14));
            const int uo = SCH_U + 512 + 2 * (256 * w + 4 * lane);
#pragma unroll
            for (int c = 0; c < 4; ++c) {
                const unsigned sel = (c & 1) ? 0x07060302u : 0x05040100u;
                ldsw(L + L_S + c * SCH + uo, (u32x2){__builtin_amdgcn_perm(uu[1][c >> 1], uu[0][c >> 1], sel), __builtin_amdgcn_perm(uu[3][c >> 1], uu[2][c >> 1], sel)});
            }
            arrive(L, C_U, lane);
        }
        waitc(L, C_Q, 8);
        {
            lds_p qa = L + L_Q + wq4 * 2048 + r32 * 48, qb = qa + 4 * 2048;
            qmA = ldsr<bf16x8>(qa + 16 * h); qmB = ldsr<bf16x8>(qb + 16 * h);
            qeA = ldsr<unsigned>(qa + 32); qeB = ldsr<unsigned>(qb + 32);
        }
        waitc(L, C_F1, 1);
        STAMP(3);
    }

    {
        lds_p ring = L + L_W + kh * (8 * KVT);
        const int dmo = WS_KV + (32 * kh + wq4) * KVT + lane * 16;
        const unsigned dmd = lds0 + L_W + kh * (8 * KVT) + wq4 * KVT;
#define KV_DMA_A(cc) do { dma16_sc1(rs_ws, dmo + (cc) * (4 * KVT), dmd + ((cc) & 1) * (4 * KVT)); dma16_sc1(rs_ws, dmo + (cc) * (4 * KVT) + 1024, dmd + ((cc) & 1) * (4 * KVT) + 1024); } while (0)
#define KV_DMA_B(cc) do { if (lane < 32) dma16_sc1(rs_ws, dmo + (cc) * (4 * KVT) + 2048, dmd + ((cc) & 1) * (4 * KVT) + 2048); } while (0)
        lds_p kb = ring + lane * 16, eb = ring + 2304 + r32 * 8, vb = ring + 1024 + (h * 20 + (r32 < 19 ? r32 : 19)) * 16;
        const f32x16 Z = {};
        unsigned mA = 0, mB = 0;
        f32x16 OA, OB;
        for (int pass = 0; pass < 2; ++pass) {
            KV_DMA_A(0); KV_DMA_B(0); KV_DMA_A(1); KV_DMA_B(1);
            asm volatile("s_waitcnt vmcnt(3)" ::: "memory");
            arrive(L, C_RW, lane); waitc(L, C_RW, 8 * (pass + 1));
            STAMP(9); if (pass == 0) PRB(1);
            if (pass == 0) {
                const bf16x8 km = ldsr<bf16x8>(L + L_W + lane * 16); const bf16x4 ke = ldsr<bf16x4>(L + L_W + 2304 + r32 * 8);
                const u32x2 ea = {h ? 0u : qeA, 0u}, ebq = {h ? 0u : qeB, 0u};
                f32x16 sa = MF32(km, qmA, Z), sb = MF32(km, qmB, Z);
                sa = MF32E(ke, __builtin_bit_cast(bf16x4, ea), sa); sb = MF32E(ke, __builtin_bit_cast(bf16x4, ebq), sb);
                float ma = sa[0], mb = sb[0];
#pragma unroll
                for (int i = 1; i < 16; ++i) { ma = fmaxf(ma, sa[i]); mb = fmaxf(mb, sb[i]); }
                auto ra = __builtin_amdgcn_permlane32_swap(__float_as_uint(ma), __float_as_uint(ma), false, false);
                auto rb = __builtin_amdgcn_permlane32_swap(__float_as_uint(mb), __float_as_uint(mb), false, false);
                ma = fmaxf(__uint_as_float(ra[0]), __uint_as_float(ra[1])); mb = fmaxf(__uint_as_float(rb[0]), __uint_as_float(rb[1]));
                const float ah = bflo(cvtpk(ma, 0.f)), bh = bflo(cvtpk(mb, 0.f));
                mA = cvtpk(-ah, ah - ma); mB = cvtpk(-bh, bh - mb);
            }
            const u32x2 ea = {h ? 0u : qeA, h ? 0u : mA}, ebq = {h ? 0u : qeB, h ? 0u : mB};
            const bf16x4 qxA = __builtin_bit_cast(bf16x4, ea), qxB = __builtin_bit_cast(bf16x4, ebq);
            f32x16 SA, SB = Z; u32x4 pa0, pa1, pb0 = {}, pb1 = {}; bf16x8 km, vx0, vx1, vy0, vy1; bf16x4 ke;
            OA = Z; OB = Z;
            km = ldsr<bf16x8>(kb); ke = ldsr<bf16x4>(eb);
            SA = MF32(km, qmA, Z); SA = MF32E(ke, qxA, SA);
            vx0 = ldsr<bf16x8>(vb); vx1 = ldsr<bf16x8>(vb + 640);
#define PH(SC, SN, QM, QX, ON, pp0, pp1, pn0, pn1, V0, V1, LD1, LD2, LD4) do { \
            ON = MF32(V0, BC8(pp0), ON); SBAR(); \
            LD1; \
            SC[0] = EXP2(SC[0]); SC[1] = EXP2(SC[1]); SC[2] = EXP2(SC[2]); SC[3] = EXP2(SC[3]); SBAR(); \
            SN = MF32(km, QM, Z); SBAR(); \
            LD2; \
            SC[4] = EXP2(SC[4]); SC[5] = EXP2(SC[5]); pn0[0] = cvtpk(SC[0], SC[1]); SC[6] = EXP2(SC[6]); SC[7] = EXP2(SC[7]); pn0[1] = cvtpk(SC[2], SC[3]); SBAR(); \
            ON = MF32(V1, BC8(pp1), ON); SBAR(); \
            SC[8] = EXP2(SC[8]); SC[9] = EXP2(SC[9]); pn0[2] = cvtpk(SC[4], SC[5]); SC[10] = EXP2(SC[10]); SC[11] = EXP2(SC[11]); pn0[3] = cvtpk(SC[6], SC[7]); PIN(pn0); SBAR(); \
            SN = MF32E(ke, QX, SN); SBAR(); \
            LD4; \
            SC[12] = EXP2(SC[12]); SC[13] = EXP2(SC[13]); pn1[0] = cvtpk(SC[8], SC[9]); SC[14] = EXP2(SC[14]); SC[15] = EXP2(SC[15]); pn1[1] = cvtpk(SC[10], SC[11]); \
            pn1[2] = cvtpk(SC[12], SC[13]); pn1[3] = cvtpk(SC[14], SC[15]); PIN(pn1); SBAR(); \
        } while (0)
#define VRD(t, N0, N1) (N0 = ldsr<bf16x8>(vb + (t) * KVT), N1 = ldsr<bf16x8>(vb + (t) * KVT + 640))
#define KMRD(t) (km = ldsr<bf16x8>(kb + (((t) + 1) & 7) * KVT))
#define KERD(t) (ke = ldsr<bf16x4>(eb + (((t) + 1) & 7) * KVT))
#define PHA(P0, P1, LD1, LD2, LD4) PH(SA, SB, qmB, qxB, OB, pb0, pb1, pa0, pa1, P0, P1, LD1, LD2, LD4);
#define PHB(N0, N1, LD1, LD2, LD4) PH(SB, SA, qmA, qxA, OA, pa0, pa1, pb0, pb1, N0, N1, LD1, LD2, LD4);
#define TA(t) PHA(vx0, vx1, VRD(t, vy0, vy1), KMRD(t), KERD(t))
#define TB() PHB(vy0, vy1, (void)0, (void)0, (void)0)
#define UA(t) PHA(vy0, vy1, VRD(t, vx0, vx1), KMRD(t), KERD(t))
#define UB() PHB(vx0, vx1, (void)0, (void)0, (void)0)
#define PRIO(x) __builtin_amdgcn_s_setprio(x);
#define LAST(t, b) \
            PHA(vy0, vy1, VRD(t, vx0, vx1), (void)0, (void)0) \
            asm volatile("s_waitcnt vmcnt(0)" ::: "memory"); \
            if ((b) < 7) { if (it == 0 && pass == 0) { arrive(L, C_B, lane); waitc(L, C_B, 8 * ((b) + 1)); } else __builtin_amdgcn_s_barrier(); } \
            CBAR(); PRIO(3) KMRD(t); KERD(t); SBAR(); \
            PHB(vx0, vx1, (void)0, if ((b) + 2 < 8) KV_DMA_A((b) + 2), if ((b) + 2 < 8) KV_DMA_B((b) + 2))
            PRIO(3)
            CSTAMP(10);
            for (int it = 0; it < 4; ++it) {
                TA(0) PRIO(2) TB() UA(1) PRIO(1) UB() TA(2) PRIO(0) TB()
                LAST(3, 2 * it)
                if (it == 1 && pass == 0) {
                    const __amdgpu_buffer_rsrc_t rs_wo = __builtin_amdgcn_make_buffer_rsrc((void*)p.wo, 0, DM * DM * 2, RSRC_W3);
                    int kp = 64 * w + lane; const int k9 = (kp * 7282) >> 16; int g = kp - 9 * k9; kp = k9;
                    for (int inst = w; inst < 81; inst += 8) {
                        dma16(rs_wo, swap23(kp) * 1152 + 144 * (pi >> 1) + 16 * g, lds0 + L_S + 1024 * inst);
                        g += 8; kp += 56; if (g >= 9) { g -= 9; kp += 1; }
                    }
                }
                TA(4) PRIO(2) TB() UA(5) PRIO(1) UB() TA(6) PRIO(0) TB()
                LAST(7, 2 * it + 1)
            }
            OB = MF32(vx0, BC8(pb0), OB); OB = MF32(vx1, BC8(pb1), OB);
#undef TA
#undef TB
#undef UA
#undef UB
#undef LAST
#undef PHA
#undef PHB
#undef VRD
#undef KMRD
#undef KERD
#undef PH
            PRIO(0)
            STAMP(4); CSTAMP(11); PRB(2);
            lds_p xo = (w < 4 ? L + L_X1 : L + L_Q) + wq4 * 3072, xi = (w < 4 ? L + L_Q : L + L_X1) + wq4 * 3072;
            {
                const f32x16& S = kh ? OA : OB;
#pragma unroll
                for (int g = 0; g < 3; ++g) ldsw(xo + g * 1024 + lane * 16, (f32x4){S[4 * g], S[4 * g + 1], S[4 * g + 2], S[4 * g + 3]});
            }
            BARRIER();
            STAMP(12);
            float o[12];
#pragma unroll
            for (int g = 0; g < 3; ++g) {
                const f32x4 t = ldsr<f32x4>(xi + g * 1024 + lane * 16);
#pragma unroll
                for (int i = 0; i < 4; ++i) o[4 * g + i] = (kh ? OB[4 * g + i] : OA[4 * g + i]) + t[i];
            }
            const float lsum = __uint_as_float(__builtin_amdgcn_permlane32_swap(__float_as_uint(o[10]), __float_as_uint(o[10]), false, false)[0]);
            const float rl = __builtin_amdgcn_rcpf(lsum);
            bool bad = !(lsum < 1.2676506e30f);
#pragma unroll
            for (int i = 0; i < 8; ++i) bad |= !(fabsf(o[i]) < 3.0e38f);
            if (h == 0) bad |= !(fabsf(o[8]) < 3.0e38f) || !(fabsf(o[9]) < 3.0e38f);
            {
                lds_p os = xi;
                lds_p ob = os + (r32 & 15) * 72 + (r32 >> 4) * 36 + 8 * h;
                ldsw(ob, cvtpk(o[0] * rl, o[1] * rl)); ldsw(ob + 4, cvtpk(o[2] * rl, o[3] * rl));
                ldsw(ob + 16, cvtpk(o[4] * rl, o[5] * rl)); ldsw(ob + 20, cvtpk(o[6] * rl, o[7] * rl));
                if (h == 0) ldsw(ob + 32, cvtpk(o[8] * rl, o[9] * rl));
#pragma unroll
                for (int i = 0; i < 3; ++i) {
                    const int e = lane + 64 * i;
                    if (e < 144) {
                        const int tk = e & 15, pc = e >> 4, k0 = 36 * pi + 4 * pc;
                        const u32x2 v = ldsr<u32x2>(os + tk * 72 + pc * 8);
                        __builtin_amdgcn_raw_buffer_store_b64(v, rs_ws, WS_O + (8 * tb + w) * 18432 + (k0 >> 3) * 256 + tk * 16 + (k0 & 4) * 2, 0, AUX_SC1);
                    }
                }
            }
            if (pass == 0 && __any(bad) && lane == 0) ((volatile LAS unsigned*)(L + L_C))[C_BAD] = 1u;
            STAMP(13);
            DRAIN();
            BARRIER();
            const bool redo = pass == 0 && __builtin_amdgcn_readfirstlane(((volatile LAS unsigned*)(L + L_C))[C_BAD]) != 0;
            if (!redo) break;
            {
                const u32x2 ea0 = {h ? 0u : qeA, 0u}, eb0 = {h ? 0u : qeB, 0u};
                float ma = -INFINITY, mb = -INFINITY;
                for (int t = 0; t < 64; ++t) {
                    const u32x4 kmv = __builtin_amdgcn_raw_buffer_load_b128(rs_ws, WS_KV + t * KVT + lane * 16, 0, AUX_SC1);
                    const u32x2 kev = __builtin_amdgcn_raw_buffer_load_b64(rs_ws, WS_KV + t * KVT + 2304 + r32 * 8, 0, AUX_SC1);
                    f32x16 sa = MF32(BC8(kmv), qmA, Z), sb = MF32(BC8(kmv), qmB, Z);
                    sa = MF32E(__builtin_bit_cast(bf16x4, kev), __builtin_bit_cast(bf16x4, ea0), sa);
                    sb = MF32E(__builtin_bit_cast(bf16x4, kev), __builtin_bit_cast(bf16x4, eb0), sb);
#pragma unroll
                    for (int i = 0; i < 16; ++i) { ma = fmaxf(ma, sa[i]); mb = fmaxf(mb, sb[i]); }
                }
                ma = fmaxf(ma, __shfl_xor(ma, 32)); mb = fmaxf(mb, __shfl_xor(mb, 32));
                const float ah = bflo(cvtpk(ma, 0.f)), bh = bflo(cvtpk(mb, 0.f));
                mA = cvtpk(-ah, ah - ma); mB = cvtpk(-bh, bh - mb);
            }
        }
#undef KV_DMA_A
#undef KV_DMA_B
#undef PRIO
        if (w == 0 && lane == 0) gstore_sc1((unsigned*)(p.ws + WS_FLAG2 + (tb * 16 + pi) * 128), tag);
        STAMP(5);
    }

    {
        constexpr int PF3 = 5;
        lds_p wb = L + L_S + (16 * (kq >> 1) + 4 * (kq & 1) + q4) * 144 + 8 * p4 + kh * (9 * 32 * 144);
        bf16x8 wf[PF3][5];
#define WORD3(i, T) cat8(ldtr(wb + (i) * 4608 + 32 * (T)), ldtr(wb + (i) * 4608 + 1152 + 32 * (T)))
#pragma unroll
        for (int i = 0; i < PF3; ++i)
#pragma unroll
            for (int T = 0; T < 5; ++T) wf[i][T] = WORD3(i, T);
        SBAR();
        if (w == 7) {
            const unsigned* f = (const unsigned*)(p.ws + WS_FLAG2 + tb * 2048) + (lane & 15) * 32;
            for (unsigned spins = 0; spins < SPIN_GLB; ++spins) {
                if (__all(gload_sc1(f) == tag)) break;
                __builtin_amdgcn_s_sleep(1);
            }
            __builtin_amdgcn_fence(__ATOMIC_ACQUIRE, "wavefront");
        }
        BARRIER();
        STAMP(6);
        const int Tt = 128 * tb + 64 * (pi & 1) + 16 * wq4, F0 = 72 * (pi >> 1);
        u32x4 of[9];
#pragma unroll
        for (int i = 0; i < 9; ++i) of[i] = __builtin_amdgcn_raw_buffer_load_b128(rs_ws, WS_O + (Tt >> 4) * 18432 + (9 * kh + i) * 1024 + lane * 16, 0, AUX_SC1);
        SBAR();
        f32x4 D[5] = {};
#pragma unroll
        for (int i = 0; i < 9; ++i) {
#pragma unroll
            for (int T = 0; T < 5; ++T) D[T] = MF16(wf[i % PF3][T], BC8(of[i]), D[T]);
            SBAR();
            if (i + PF3 < 9) {
#pragma unroll
                for (int T = 0; T < 5; ++T) wf[i % PF3][T] = WORD3(i + PF3, T);
            }
            SBAR();
        }
#undef WORD3
        lds_p xo = L + L_W + w * 3072, xi = L + L_W + (w ^ 4) * 3072;
        if (kh == 0) { ldsw(xo + lane * 16, D[3]); ldsw(xo + 1024 + lane * 16, D[4]); }
        else { ldsw(xo + lane * 16, D[0]); ldsw(xo + 1024 + lane * 16, D[1]); ldsw(xo + 2048 + lane * 16, D[2]); }
        BARRIER();
        const __amdgpu_buffer_rsrc_t rs_out = __builtin_amdgcn_make_buffer_rsrc((void*)p.out, 0, SEQ * 2 * DM * 2, RSRC_W3);
        const int oo = ((Tt + c16) * (2 * DM) + F0 + 4 * kq) * 2;
        if (kh == 0) {
#pragma unroll
            for (int T = 0; T < 3; ++T) {
                const f32x4 v = D[T] + ldsr<f32x4>(xi + T * 1024 + lane * 16);
                __builtin_amdgcn_raw_buffer_store_b64((u32x2){cvtpk(v[0], v[1]), cvtpk(v[2], v[3])}, rs_out, oo + 32 * T, 0, OUT_AUX_A);
            }
        } else {
#pragma unroll
            for (int T = 3; T < 5; ++T) {
                const f32x4 v = D[T] + ldsr<f32x4>(xi + (T - 3) * 1024 + lane * 16);
                if (T == 3 || kq < 2) __builtin_amdgcn_raw_buffer_store_b64((u32x2){cvtpk(v[0], v[1]), cvtpk(v[2], v[3])}, rs_out, oo + 32 * T, 0, OUT_AUX_A);
            }
        }
        STAMP(7);
        STAMP_DUMP();
        if (bid == 0 && w == 7) {
            const unsigned* f = (const unsigned*)(p.ws + WS_FLAG2);
            for (unsigned spins = 0; spins < SPIN_GLB; ++spins) {
                bool ok = true;
#pragma unroll
                for (int i = 0; i < 4; ++i) { const int e = lane + 64 * i; ok &= gload_sc1(f + e * 32) == tag; }
                if (__all(ok)) break;
                __builtin_amdgcn_s_sleep(2);
            }
            if (lane == 0) gstore_sc1((unsigned*)(p.ws + WS_EPOCH), tag);
        }
    }
}

}

extern "C" void kernel_launch(void* const* d_in, const int* in_sizes, int n_in, void* d_out, int out_size, void* d_ws, size_t ws_size,
                              hipStream_t stream) {
    Params p;
    p.x = (const unsigned short*)d_in[0]; p.wq = (const unsigned short*)d_in[1]; p.wk = (const unsigned short*)d_in[2];
    p.wv = (const unsigned short*)d_in[3]; p.wo = (const unsigned short*)d_in[4]; p.u = (const unsigned short*)d_in[5];
    p.are = (const unsigned short*)d_in[6]; p.aim = (const unsigned short*)d_in[7]; p.bre = (const unsigned short*)d_in[8];
    p.bim = (const unsigned short*)d_in[9]; p.cre = (const unsigned short*)d_in[10]; p.cim = (const unsigned short*)d_in[11];
    p.ldt = (const unsigned short*)d_in[12]; p.dsk = (const unsigned short*)d_in[13];
    p.out = (unsigned short*)d_out; p.ws = (char*)d_ws;
    (void)hipFuncSetAttribute((const void*)fused_kernel, hipFuncAttributeMaxDynamicSharedMemorySize, L_END);
    fused_kernel<<<256, 768, L_END, stream>>>(p);
}
```

```cpp
#include <hip/hip_runtime.h>
#include <hip/hip_bf16.h>
#include <stdint.h>

#define S_PRIO0 0
#define U_BY_S 0
#define S_PRIOB 0
#define S_PRIOC 3
#define MK_PROBE 4
#define PROBE_WG 0
#define PRB(i) do { prb[i] = __builtin_amdgcn_s_memrealtime(); } while (0)
#define MK_NOSSM 0
#define MK_STAMPS 0

namespace {

typedef __attribute__((ext_vector_type(16))) float f32x16;
typedef __attribute__((ext_vector_type(4))) float f32x4;
typedef __attribute__((ext_vector_type(2))) float f32x2;
typedef __attribute__((ext_vector_type(8))) short bf16x8;
typedef __attribute__((ext_vector_type(4))) short bf16x4;
typedef __attribute__((ext_vector_type(2))) __bf16 bf16x2_t;
typedef __attribute__((ext_vector_type(4))) unsigned u32x4;
typedef __attribute__((ext_vector_type(2))) unsigned u32x2;
typedef short v4i16_t __attribute__((ext_vector_type(4)));
#define LAS __attribute__((address_space(3)))
#define GAS __attribute__((address_space(1)))
typedef LAS char* lds_p;
typedef const unsigned short* cbf;

constexpr int SEQ = 2048, DM = 576;
constexpr float QSCALE = 0.34005040627f;
constexpr float LOG2E = 1.4426950408889634f;
constexpr float INV2PI = 0.15915494309189535f;

constexpr int WS_EPOCH = 0;
constexpr int WS_FLAG1 = 4096;
constexpr int WS_FLAG2 = WS_FLAG1 + 32768;
constexpr int WS_KV = WS_FLAG2 + 32768;
constexpr int KVT = 2560;
constexpr int WS_O = WS_KV + 64 * KVT;
constexpr int WS_BYTES = WS_O + SEQ * DM * 2;
constexpr int WS_STAMP = 8 * 1024 * 1024;

constexpr int L_W = 0;
constexpr int L_X1 = 40960;
constexpr int L_S = 55296;
constexpr int L_Q = L_S + 87360;
constexpr int L_C = L_Q + 16384;
constexpr int L_END = L_C + 256;
constexpr int SCH = 21840;
constexpr int SCH_U = 16640;
enum { C_W = 0, C_P, C_Q, C_F1, C_RW, C_B, C_BAD, C_U, C_S2, C_D, C_KW, C_X, C_NUM };

constexpr int RSRC_W3 = 0x00020000;
constexpr int AUX_SC1 = 16;
constexpr unsigned SPIN_LDS = 1u << 20, SPIN_GLB = 1u << 16;

#define SBAR() __builtin_amdgcn_sched_barrier(0)
#define PIN(x) asm volatile("" : "+v"(x))
#define CBAR() asm volatile("" ::: "memory")
#define DRAIN() asm volatile("s_waitcnt vmcnt(0)" ::: "memory")
#define MF32(a, b, c) __builtin_amdgcn_mfma_f32_32x32x16_bf16(a, b, c, 0, 0, 0)
#define MF32E(a, b, c) __builtin_amdgcn_mfma_f32_32x32x8bf16_1k(a, b, c, 0, 0, 0)
#define MF16(a, b, c) __builtin_amdgcn_mfma_f32_16x16x32_bf16(a, b, c, 0, 0, 0)
#define EXP2(x) __builtin_amdgcn_exp2f(x)
#define BC8(x) __builtin_bit_cast(bf16x8, x)
#define DI __device__ __forceinline__

DI unsigned cvtpk(float lo, float hi) { f32x2 v = {lo, hi}; return __builtin_bit_cast(unsigned, __builtin_convertvector(v, bf16x2_t)); }
DI float bflo(unsigned x) { return __builtin_bit_cast(float, x << 16); }
DI float bfhi(unsigned x) { return __builtin_bit_cast(float, x & 0xffff0000u); }
DI float bf1(unsigned short x) { return __builtin_bit_cast(float, (unsigned)x << 16); }
template <class T> DI T ldsr(lds_p p) { return *(const LAS T*)p; }
template <class T> DI void ldsw(lds_p p, T v) { *(LAS T*)p = v; }
DI bf16x4 ldtr(lds_p p) { return __builtin_bit_cast(bf16x4, __builtin_amdgcn_ds_read_tr16_b64_v4i16((LAS v4i16_t*)p)); }
DI bf16x8 cat8(bf16x4 a, bf16x4 b) { return __builtin_shufflevector(a, b, 0, 1, 2, 3, 4, 5, 6, 7); }
DI int swap23(int k) { return (k & ~12) | ((k & 4) << 1) | ((k & 8) >> 1); }
DI void dma16(__amdgpu_buffer_rsrc_t rs, int voff, unsigned lds_base) {
    unsigned keep;
    asm volatile("s_mov_b32 %0, m0\n\ts_mov_b32 m0, %3\n\ts_nop 0\n\tbuffer_load_dwordx4 %1, %2, 0 offen lds\n\ts_mov_b32 m0, %0"
                 : "=&s"(keep) : "v"(voff), "s"(rs), "s"(lds_base) : "memory");
}
DI void dma16_sc1(__amdgpu_buffer_rsrc_t rs, int voff, unsigned lds_base) {
    unsigned keep;
    asm volatile("s_mov_b32 %0, m0\n\ts_mov_b32 m0, %3\n\ts_nop 0\n\tbuffer_load_dwordx4 %1, %2, 0 offen sc1 lds\n\ts_mov_b32 m0, %0"
                 : "=&s"(keep) : "v"(voff), "s"(rs), "s"(lds_base) : "memory");
}
#define BARRIER() asm volatile("s_waitcnt lgkmcnt(0)\n\ts_barrier" ::: "memory")
DI void arrive(lds_p L, int c, int lane) {
    (void)lane;
    unsigned long long keep;
    asm volatile("s_mov_b64 %0, exec\n\ts_mov_b64 exec, 1\n\tds_add_u32 %1, %2\n\ts_mov_b64 exec, %0" : "=&s"(keep) : "v"((unsigned)(uintptr_t)(L + L_C + 4 * c)), "v"(1u) : "memory");
}
DI void waitc(lds_p L, int c, unsigned target) {
    CBAR();
    volatile LAS unsigned* p = (volatile LAS unsigned*)(L + L_C) + c;
    for (unsigned spins = 0; spins < SPIN_LDS; ++spins) {
        if ((unsigned)__builtin_amdgcn_readfirstlane(*p) >= target) break;
        __builtin_amdgcn_s_sleep(1);
    }
    CBAR();
}
DI unsigned peek(lds_p L, int c) { return *((volatile LAS unsigned*)(L + L_C) + c); }
DI void waitp(lds_p L, int c, unsigned seen, unsigned target) { if ((unsigned)__builtin_amdgcn_readfirstlane(seen) < target) waitc(L, c, target); CBAR(); }
DI unsigned gload_sc1(const unsigned* p) { return __hip_atomic_load((GAS unsigned*)p, __ATOMIC_RELAXED, __HIP_MEMORY_SCOPE_AGENT); }
DI void gstore_sc1(unsigned* p, unsigned v) { __hip_atomic_store((GAS unsigned*)p, v, __ATOMIC_RELAXED, __HIP_MEMORY_SCOPE_AGENT); }

struct cplx2 { f32x2 re, im; };
DI cplx2 cmul(const cplx2& a, const cplx2& b) { cplx2 r; r.re = a.re * b.re - a.im * b.im; r.im = a.re * b.im + a.im * b.re; return r; }
DI cplx2 cexp(f32x2 ar2, f32x2 airev, float e) {
    f32x2 m = ar2 * e, t = airev * e; cplx2 r;
#pragma unroll
    for (int i = 0; i < 2; ++i) {
        const float mag = EXP2(m[i]), fr = __builtin_amdgcn_fractf(t[i]);
        r.re[i] = mag * __builtin_amdgcn_cosf(fr); r.im[i] = mag * __builtin_amdgcn_sinf(fr);
    }
    return r;
}

struct Params {
    const unsigned short *x, *wq, *wk, *wv, *wo, *u, *are, *aim, *bre, *bim, *cre, *cim, *ldt, *dsk;
    unsigned short* out;
    char* ws;
};

#define STAMP(i) do {} while (0)
#define CSTAMP(i) do {} while (0)
#define STAMP_DECL do {} while (0)
#define STAMP_DUMP() do {} while (0)
#define STAMP_ARG
#define STAMP_PASS

DI void dma16s(__amdgpu_buffer_rsrc_t rs, int voff, int soff, unsigned lds_base) {
    unsigned keep;
    asm volatile("s_mov_b32 %0, m0\n\ts_mov_b32 m0, %4\n\ts_nop 0\n\tbuffer_load_dwordx4 %1, %2, %3 offen lds\n\ts_mov_b32 m0, %0"
                 : "=&s"(keep) : "v"(voff), "s"(rs), "s"(soff), "s"(lds_base) : "memory");
}
DI void kv_wave(const Params& p, lds_p L, unsigned lds0, __amdgpu_buffer_rsrc_t rs_ws, int s, int lane, int xcd, int tt STAMP_ARG) {
    __builtin_amdgcn_s_setprio(3);
    const unsigned epoch = gload_sc1((const unsigned*)(p.ws + WS_EPOCH));
    const int T0 = 256 * xcd + 16 * tt;
    {
        const __amdgpu_buffer_rsrc_t rs_wk = __builtin_amdgcn_make_buffer_rsrc((void*)p.wk, 0, DM * 18 * 2, RSRC_W3);
        const __amdgpu_buffer_rsrc_t rs_wv = __builtin_amdgcn_make_buffer_rsrc((void*)p.wv, 0, DM * 18 * 2, RSRC_W3);
        int vo[3];
#pragma unroll
        for (int j = 0; j < 3; ++j) { const int sg = 64 * j + lane, kp = (sg * 171) >> 9; vo[j] = swap23(kp) * 36 + 16 * (sg - 3 * kp); }
        for (int b = s; b < 9; b += 4) {
#pragma unroll
            for (int j = 0; j < 3; ++j) {
                dma16s(rs_wk, vo[j], 2304 * b, lds0 + L_S + 3072 * b + 1024 * j);
                dma16s(rs_wv, vo[j], 2304 * b, lds0 + L_S + 27648 + 3072 * b + 1024 * j);
            }
        }
    }
    SBAR();
    u32x4 xa[9], xb[9];
    {
        const unsigned short* xp = p.x + (size_t)(T0 + (lane >> 3)) * DM + 8 * (lane & 7);
#pragma unroll
        for (int i = 0; i < 9; ++i) { xa[i] = *(const u32x4*)(xp + 64 * i); xb[i] = *(const u32x4*)(xp + 64 * i + 8 * DM); }
    }
    SBAR();
    BARRIER();
    arrive(L, C_D, lane);
    STAMP(3);
    asm volatile("s_waitcnt vmcnt(18)" ::: "memory");
    arrive(L, C_KW, lane);
    const int c16 = lane & 15, kq = lane >> 4, q4 = (lane >> 2) & 3, p4 = lane & 3;
    lds_p xs = L + L_S + 55296 + s * 2048;
    lds_p xw = xs + (lane >> 3) * 128 + 16 * ((lane & 7) ^ (lane >> 4)), xw2 = xs + 1024 + (lane >> 3) * 128 + 16 * ((lane & 7) ^ (4 + (lane >> 4)));
    lds_p xr0 = xs + c16 * 128 + 16 * (kq ^ ((c16 >> 1) & 7)), xr1 = xs + c16 * 128 + 16 * ((4 + kq) ^ ((c16 >> 1) & 7));
    const int rowb = (16 * (kq >> 1) + 4 * (kq & 1) + q4) * 48;
    lds_p wk = L + L_S + rowb + 8 * p4, wv = wk + 27648, wx = L + L_S + rowb + 32 + (p4 == 1 ? 27648 : 0);
    bf16x8 f0, f1, g0, g1;
    ldsw(xw, xa[0]); ldsw(xw2, xb[0]); f0 = ldsr<bf16x8>(xr0); f1 = ldsr<bf16x8>(xr1);
    waitc(L, C_KW, 4);
    STAMP(4);
    f32x4 DK = {}, DV = {}, DX = {}, DY = {};
    bf16x8 wa[2], wbv[2], wc[2];
#define KVRD(n, ks) do { wa[n] = cat8(ldtr(wk + (ks) * 1536), ldtr(wk + (ks) * 1536 + 384)); wbv[n] = cat8(ldtr(wv + (ks) * 1536), ldtr(wv + (ks) * 1536 + 384)); \
                         wc[n] = cat8(ldtr(wx + (ks) * 1536), ldtr(wx + (ks) * 1536 + 384)); } while (0)
#define KVMM(n, XF) do { DK = MF16(wa[n], XF, DK); DX = MF16(wc[n], XF, DX);        \
                         DV = MF16(XF, wbv[n], DV); DY = MF16(XF, wc[n], DY);       } while (0)
    KVRD(0, 0);
#pragma unroll
    for (int i = 0; i < 9; ++i) {
        KVRD(1, 2 * i + 1);
        SBAR();
        if (i & 1) { KVMM(0, g0); } else { KVMM(0, f0); }
        SBAR();
        if (i < 8) {
            ldsw(xw, xa[i + 1]); ldsw(xw2, xb[i + 1]);
            if (i & 1) { f0 = ldsr<bf16x8>(xr0); } else { g0 = ldsr<bf16x8>(xr0); }
            KVRD(0, 2 * i + 2);
        }
        SBAR();
        if (i & 1) { KVMM(1, g1); } else { KVMM(1, f1); }
        SBAR();
        if (i < 8) { if (i & 1) { f1 = ldsr<bf16x8>(xr1); } else { g1 = ldsr<bf16x8>(xr1); } }
        SBAR();
    }
#undef KVRD
#undef KVMM
    STAMP(5);
    const int tbase = WS_KV + (8 * xcd + (tt >> 1)) * KVT, rr = 16 * (tt & 1) + c16, s16 = tt & 1;
    __builtin_amdgcn_raw_buffer_store_b64((u32x2){cvtpk(DK[0], DK[1]), cvtpk(DK[2], DK[3])}, rs_ws, tbase + ((kq >> 1) * 32 + rr) * 16 + (kq & 1) * 8, 0, AUX_SC1);
    if (kq == 0) __builtin_amdgcn_raw_buffer_store_b64((u32x2){cvtpk(DX[0], DX[1]), 0x3F803F80u}, rs_ws, tbase + 2304 + rr * 8, 0, AUX_SC1);
    const int vo = tbase + 1024 + (s16 * 2 + (kq & 1)) * 320 + (kq >> 1) * 8;
    __builtin_amdgcn_raw_buffer_store_b64((u32x2){cvtpk(DV[0], DV[1]), cvtpk(DV[2], DV[3])}, rs_ws, vo + c16 * 16, 0, AUX_SC1);
    if (c16 >= 4 && c16 < 8) {
        u32x2 v = {cvtpk(DY[0], DY[1]), cvtpk(DY[2], DY[3])};
        if (c16 == 6) v = (u32x2){0x3F803F80u, 0x3F803F80u};
        if (c16 == 7) v = (u32x2){0u, 0u};
        __builtin_amdgcn_raw_buffer_store_b64(v, rs_ws, vo + (12 + c16) * 16, 0, AUX_SC1);
    }
    const unsigned tag = (unsigned)__builtin_amdgcn_readfirstlane(epoch) + 1u;
    DRAIN();
    arrive(L, C_P, lane);
    if (s == 0) {
        waitc(L, C_P, 4);
        if (lane < 8) gstore_sc1((unsigned*)(p.ws + WS_FLAG1 + lane * 4096 + (4 * xcd + (tt >> 2)) * 128), tag);
    }
    STAMP(6);
}

template <int G> DI void conv_group(lds_p L, lds_p R, f32x4 (&acc)[8], int pa0, int pb0) {
    if ((unsigned)__builtin_amdgcn_readfirstlane(peek(L, C_Q)) >= 8u) __builtin_amdgcn_s_setprio(S_PRIOC);
    lds_p a1 = R + pa0 - 64 * (8 * G + 8), b1 = R + pb0 - 64 * (8 * G + 8), a2 = R + pa0 - 64 * (63 - 8 * G), b2 = R + pb0 - 64 * (63 - 8 * G);
    PIN(a1); PIN(b1); PIN(a2); PIN(b2);
#define RDA(ptr) cat8(*(const volatile LAS bf16x4*)(ptr), *(const volatile LAS bf16x4*)((ptr) + 8))
    bf16x8 P1 = RDA(a1 + 512), P2 = RDA(a2), Q1, Q2, B1[8 - G], B2[G + 1];
#pragma unroll
    for (int n = 0; n < 8 - G; ++n) B1[n] = ldsr<bf16x8>(b1 + 512 + 512 * (G + n));
#pragma unroll
    for (int n = 0; n < G + 1; ++n) B2[n] = ldsr<bf16x8>(b2 + 512 * (7 - G + n));
#define CONV_STEP(i, C1, C2, N1, N2) do { \
        N1 = RDA(a1 + 64 * (7 - (i))); N2 = RDA(a2 + 64 * ((i) + 1)); SBAR(); \
        _Pragma("unroll") for (int n = 0; n < 8 - G; ++n) { acc[G + n] = MF16(C1, B1[n], acc[G + n]); SBAR(); B1[n] = ldsr<bf16x8>(b1 + 64 * (7 - (i)) + 512 * (G + n)); SBAR(); } \
        _Pragma("unroll") for (int n = 0; n < G + 1; ++n) { acc[7 - G + n] = MF16(C2, B2[n], acc[7 - G + n]); SBAR(); B2[n] = ldsr<bf16x8>(b2 + 64 * ((i) + 1) + 512 * (7 - G + n)); SBAR(); } \
    } while (0)
    CONV_STEP(0, P1, P2, Q1, Q2); CONV_STEP(1, Q1, Q2, P1, P2); CONV_STEP(2, P1, P2, Q1, Q2); CONV_STEP(3, Q1, Q2, P1, P2);
    CONV_STEP(4, P1, P2, Q1, Q2); CONV_STEP(5, Q1, Q2, P1, P2); CONV_STEP(6, P1, P2, Q1, Q2); CONV_STEP(7, Q1, Q2, P1, P2);
#undef CONV_STEP
#undef RDA
}

DI void ssm_wave(const Params& p, lds_p L, int s, int lane, int ch0 STAMP_ARG) {
    const int d = ch0 + s; const int w = 8 + s; (void)w;
    __builtin_amdgcn_s_setprio(S_PRIO0);
    lds_p R = L + L_S + s * SCH;
    const int pb = d * 128 + lane;
    unsigned short raw[13];
    raw[0] = p.are[pb]; raw[1] = p.are[pb + 64]; raw[2] = p.aim[pb]; raw[3] = p.aim[pb + 64]; raw[4] = p.bre[pb]; raw[5] = p.bre[pb + 64]; raw[6] = p.bim[pb]; raw[7] = p.bim[pb + 64];
    raw[8] = p.cre[pb]; raw[9] = p.cre[pb + 64]; raw[10] = p.cim[pb]; raw[11] = p.cim[pb + 64]; raw[12] = p.ldt[d];
    const unsigned short dsk = p.dsk[d];
    const int t0 = 512 * s + 8 * lane;
    SBAR();
    BARRIER();
    SBAR();
    const f32x2 are = {bf1(raw[0]), bf1(raw[1])}, aim = {bf1(raw[2]), bf1(raw[3])}, bre = {bf1(raw[4]), bf1(raw[5])}, bim = {bf1(raw[6]), bf1(raw[7])};
    const f32x2 cre = {bf1(raw[8]), bf1(raw[9])}, cim = {bf1(raw[10]), bf1(raw[11])};
    const float ldt = bf1(raw[12]);
    const float dt = bflo(cvtpk(expf(ldt), 0.f));
    const f32x2 ar2 = (are * dt) * LOG2E, airev = (aim * dt) * INV2PI;
    cplx2 dB; dB.re = bre * dt; dB.im = bim * dt;
    cplx2 C; C.re = cre; C.im = cim;
    const cplx2 CB = cmul(C, dB);
    const cplx2 w1 = cexp(ar2, airev, 1.f), w8 = cexp(ar2, airev, 8.f), w128 = cexp(ar2, airev, 128.f);

    STAMP(3);
    lds_p st0 = R + lane * 16, st1 = R + 4224 + lane * 16;
    const int kq = lane >> 4, q4 = (lane >> 2) & 3, p4 = lane & 3;
    lds_p trb = R + (p4 >> 1) * 4224 + (16 * (kq >> 1) + 4 * (kq & 1) + q4) * 16 + (p4 & 1) * 8;
#define PUT16(V, NEG) do { \
        u32x4 a0, a1, b0, b1, c0, c1, d0, d1; \
        _Pragma("unroll") for (int j = 0; j < 4; ++j) { \
            a0[j] = cvtpk(V[2 * j].re[0], V[2 * j + 1].re[0]); a1[j] = cvtpk(V[8 + 2 * j].re[0], V[9 + 2 * j].re[0]); \
            b0[j] = cvtpk(V[2 * j].re[1], V[2 * j + 1].re[1]); b1[j] = cvtpk(V[8 + 2 * j].re[1], V[9 + 2 * j].re[1]); \
            c0[j] = cvtpk(NEG V[2 * j].im[0], NEG V[2 * j + 1].im[0]); c1[j] = cvtpk(NEG V[8 + 2 * j].im[0], NEG V[9 + 2 * j].im[0]); \
            d0[j] = cvtpk(NEG V[2 * j].im[1], NEG V[2 * j + 1].im[1]); d1[j] = cvtpk(NEG V[8 + 2 * j].im[1], NEG V[9 + 2 * j].im[1]); } \
        ldsw(st0, a0); ldsw(st1, a1); ldsw(st0 + 1024, b0); ldsw(st1 + 1024, b1); \
        ldsw(st0 + 2048, c0); ldsw(st1 + 2048, c1); ldsw(st0 + 3072, d0); ldsw(st1 + 3072, d1); } while (0)
#define GETF(ks) cat8(ldtr(trb + (ks) * 512), ldtr(trb + (ks) * 512 + 128))
    bf16x8 yf[8];
    {
        cplx2 V[16];
        V[0].re = (f32x2){1.f, 1.f}; V[0].im = (f32x2){0.f, 0.f}; V[1].re = w8.re; V[1].im = -w8.im;
        V[2] = cmul(V[1], V[1]); V[3] = cmul(V[2], V[1]); V[4] = cmul(V[2], V[2]);
        V[5] = cmul(V[4], V[1]); V[6] = cmul(V[4], V[2]); V[7] = cmul(V[4], V[3]); V[8] = cmul(V[4], V[4]);
#pragma unroll
        for (int j = 9; j < 16; ++j) V[j] = cmul(V[8], V[j - 8]);
        PUT16(V, +);
#pragma unroll
        for (int ks = 0; ks < 8; ++ks) yf[ks] = GETF(ks);
    }
    STAMP(9);
    const cplx2 w2 = cmul(w1, w1), w3 = cmul(w2, w1), w4 = cmul(w2, w2), w5 = cmul(w4, w1), w6 = cmul(w4, w2), w7 = cmul(w4, w3);
    f32x4 D0 = {}, D1 = {}, D2 = {};
    cplx2 G = cmul(CB, w128);
    {
        cplx2 V[16];
        V[0] = CB; V[1] = cmul(CB, w1); V[2] = cmul(CB, w2); V[3] = cmul(CB, w3); V[4] = cmul(CB, w4); V[5] = cmul(CB, w5); V[6] = cmul(CB, w6); V[7] = cmul(CB, w7);
        V[8] = G; V[9] = cmul(G, w1); V[10] = cmul(G, w2); V[11] = cmul(G, w3); V[12] = cmul(G, w4); V[13] = cmul(G, w5); V[14] = cmul(G, w6); V[15] = cmul(G, w7);
        PUT16(V, +);
        bf16x8 xf[8];
#pragma unroll
        for (int ks = 0; ks < 8; ++ks) xf[ks] = GETF(ks);
        SBAR();
#pragma unroll
        for (int ks = 0; ks < 8; ++ks) D0 = MF16(xf[ks], yf[ks], D0);
    }
    PIN(D0); STAMP(10);
    {
        cplx2 V[16];
        G = cmul(G, w128); V[0] = G; V[1] = cmul(G, w2); V[2] = cmul(G, w4); V[3] = cmul(G, w6);
        G = cmul(G, w128); V[4] = G; V[5] = cmul(G, w2); V[6] = cmul(G, w4); V[7] = cmul(G, w6);
#pragma unroll
        for (int i = 0; i < 4; ++i) { G = cmul(G, w128); V[8 + 2 * i] = G; V[9 + 2 * i] = cmul(G, w4); }
        PUT16(V, +);
        bf16x8 xf[8];
#pragma unroll
        for (int ks = 0; ks < 8; ++ks) xf[ks] = GETF(ks);
        SBAR();
#pragma unroll
        for (int ks = 0; ks < 8; ++ks) D1 = MF16(xf[ks], yf[ks], D1);
    }
    {
        cplx2 V[16];
#pragma unroll
        for (int i = 0; i < 9; ++i) { G = cmul(G, w128); V[i] = G; }
#pragma unroll
        for (int i = 9; i < 16; ++i) V[i] = G;
        PUT16(V, +);
        bf16x8 xf[8];
#pragma unroll
        for (int ks = 0; ks < 8; ++ks) xf[ks] = GETF(ks);
        SBAR();
#pragma unroll
        for (int ks = 0; ks < 8; ++ks) D2 = MF16(yf[ks], xf[ks], D2);
    }
#undef PUT16
#undef GETF
    PIN(D2); STAMP(4);
    lds_p tab = R + 8448;
    {
        const int j = lane & 15, rq = lane >> 4;
        if (lane == 0) D0[0] += bf1(dsk);
        ldsw(tab + 2 * (8 * j + 4 * (rq & 1) + 128 * (rq >> 1)), (u32x2){cvtpk(D0[0], D0[1]), cvtpk(D0[2], D0[3])});
        const int e0 = rq < 2 ? 256 + 64 * rq + 4 * j : 384 + 64 * (rq - 2) + 2 * j, e1 = rq < 2 ? e0 + 2 : e0 + 32;
        ldsw(tab + 2 * e0, cvtpk(D1[0], D1[1])); ldsw(tab + 2 * e1, cvtpk(D1[2], D1[3]));
        if (j < 8 || lane == 8) ldsw(tab + 2 * (512 + 16 * j + 4 * rq), (u32x2){cvtpk(D2[0], D2[1]), cvtpk(D2[2], D2[3])});
    }
#define PK(hi_src, hi_half, lo_src, lo_half) __builtin_amdgcn_perm(hi_src, lo_src, ((hi_half) ? 0x07060000u : 0x05040000u) | ((lo_half) ? 0x0302u : 0x0100u))
    {
#pragma unroll
        for (int i = 0; i < 2; ++i) {
            const int g = lane + 64 * i, m = 255 - g;
            const unsigned lo = ldsr<unsigned short>(tab + 2 * (384 + m)), hi = ldsr<unsigned short>(tab + 2 * (385 + m));
            const unsigned mid = (m & 1) ? hi : lo, ll = PK(lo, 0, lo, 0);
            ldsw(R + 16 * g, (u32x4){PK(hi, 0, hi, 0), PK(mid, 0, hi, 0), ll, ll});
        }
        {
            const int g = 128 + lane, m = 255 - g;
            const unsigned ab = ldsr<unsigned>(tab + 2 * (256 + 2 * m)), c = ldsr<unsigned short>(tab + 2 * (258 + 2 * m));
            ldsw(R + 16 * g, (u32x4){PK(c, 0, c, 0), PK(ab, 1, ab, 1), PK(ab, 0, ab, 1), PK(ab, 0, ab, 0)});
        }
        {
            const int g = 192 + lane, m = 255 - g;
            u32x4 v;
            if (lane < 32) {
                const u32x2 q = ldsr<u32x2>(tab + 2 * (128 + 4 * m)); const unsigned e = ldsr<unsigned short>(tab + 2 * (132 + 4 * m));
                v = (u32x4){PK(q[1], 1, e, 0), PK(q[1], 0, q[1], 0), PK(q[0], 1, q[1], 0), PK(q[0], 0, q[0], 0)};
            } else {
                const u32x4 q = ldsr<u32x4>(tab + 16 * m);
                v = (u32x4){PK(q[3], 0, q[3], 1), PK(q[2], 0, q[2], 1), PK(q[1], 0, q[1], 1), PK(q[0], 0, q[0], 1)};
            }
            ldsw(R + 16 * g, v);
        }
        if (lane < 4) ldsw(R + 16 * (256 + lane), (u32x4){0u, 0u, 0u, 0u});
    }
#undef PK
    if (lane < 32) ldsw(R + SCH_U + 16 * lane, (u32x4){0u, 0u, 0u, 0u});
#pragma unroll
    for (int i = 0; i < 5; ++i) {
        const int g = lane + 64 * i;
        if (i < 4 || lane < 4) {
            const u32x4 a = ldsr<u32x4>(R + 16 * g); const u32x2 b = ldsr<u32x2>(R + 16 * g + 16);
            const unsigned a0 = __builtin_amdgcn_alignbit(a[1], a[0], 16), a1 = __builtin_amdgcn_alignbit(a[2], a[1], 16), a2 = __builtin_amdgcn_alignbit(a[3], a[2], 16),
                           a3 = __builtin_amdgcn_alignbit(b[0], a[3], 16), a4 = __builtin_amdgcn_alignbit(b[1], b[0], 16);
            ldsw(R + 4160 + 16 * g, (u32x4){a0, a1, a2, a3});
            ldsw(R + 8320 + 16 * g, (u32x4){a[1], a[2], a[3], b[0]});
            ldsw(R + 12480 + 16 * g, (u32x4){a1, a2, a3, a4});
        }
    }
    STAMP(5);
    waitc(L, C_U, 8);
    STAMP(6);
    f32x4 acc[8] = {};
    {
        const int b = lane & 15, kq = lane >> 4, c = (3 - b) & 3;
        const int pa0 = c * 4160 + 2 * (2031 - b + 8 * kq - c), pb0 = SCH_U + 480 + 32 * b + 16 * kq;
        __builtin_amdgcn_s_setprio(S_PRIOB);
        conv_group<0>(L, R, acc, pa0, pb0); conv_group<1>(L, R, acc, pa0, pb0); conv_group<2>(L, R, acc, pa0, pb0); conv_group<3>(L, R, acc, pa0, pb0);
        __builtin_amdgcn_s_setprio(3);
    }
    PIN(acc[0]); PIN(acc[7]); STAMP(7);
#pragma unroll
    for (int nu = 0; nu < 8; ++nu)
        ldsw(R + 512 * nu + 32 * (lane & 15) + 8 * (lane >> 4), (u32x2){cvtpk(acc[nu][0], acc[nu][1]), cvtpk(acc[nu][2], acc[nu][3])});
    arrive(L, C_S2, lane); waitc(L, C_S2, 4);
    {
        u32x4 res[4];
#pragma unroll
        for (int c = 0; c < 4; ++c) res[c] = ldsr<u32x4>(L + L_S + c * SCH + 2 * t0);
#pragma unroll
        for (int e = 0; e < 4; ++e) {
            unsigned short* op = p.out + (size_t)(t0 + 2 * e) * (2 * DM) + DM + ch0;
            *(u32x2*)op = (u32x2){__builtin_amdgcn_perm(res[1][e], res[0][e], 0x05040100u), __builtin_amdgcn_perm(res[3][e], res[2][e], 0x05040100u)};
            *(u32x2*)(op + 2 * DM) = (u32x2){__builtin_amdgcn_perm(res[1][e], res[0][e], 0x07060302u), __builtin_amdgcn_perm(res[3][e], res[2][e], 0x07060302u)};
        }
    }
    STAMP(8);

}

__global__ __launch_bounds__(768) void fused_kernel(Params p) {
    extern __shared__ __attribute__((aligned(16))) char smem[];
    lds_p L = (lds_p)smem;
    const unsigned lds0 = (unsigned)(uintptr_t)smem;
    const int tid = threadIdx.x, lane = tid & 63, w = __builtin_amdgcn_readfirstlane(tid >> 6);
    const int bid = blockIdx.x, xcd = bid & 7, idx = bid >> 3, tb = 2 * xcd + (idx & 1), pi = idx >> 1;
    if (tid < 64) ((LAS unsigned*)(L + L_C))[tid] = 0u;
    STAMP_DECL;
    STAMP(0);
    unsigned long long prb[4];
    PRB(0);
    const __amdgpu_buffer_rsrc_t rs_ws = __builtin_amdgcn_make_buffer_rsrc((void*)p.ws, 0, WS_BYTES, RSRC_W3);

    if (w >= 8) {
        if (idx >= 14 && !MK_NOSSM) ssm_wave(p, L, w - 8, lane, 72 * xcd + 4 * (idx - 14) STAMP_PASS);
        else if (idx >= 10 && idx < 14) kv_wave(p, L, lds0, rs_ws, w - 8, lane, xcd, 4 * (idx - 10) + w - 8 STAMP_PASS);
        else BARRIER();
        STAMP(1);
        STAMP_DUMP();
        return;
    }

    const unsigned epoch = gload_sc1((const unsigned*)(p.ws + WS_EPOCH));
    SBAR();
    BARRIER();
    const int c16 = lane & 15, kq = lane >> 4, r32 = lane & 31, h = lane >> 5;
    const int q4 = (lane >> 2) & 3, p4 = lane & 3;
    const int kh = w >> 2, wq4 = w & 3;

    unsigned tag;
    bf16x8 qmA, qmB; unsigned qeA, qeB;
    {
        const int T0 = 128 * tb + 16 * w;
        const bool has_ssm = idx >= 14 && !MK_NOSSM;
        if (idx >= 10 && idx < 14) waitc(L, C_D, 4);
        {
            const __amdgpu_buffer_rsrc_t rs_wq = __builtin_amdgcn_make_buffer_rsrc((void*)p.wq, 0, DM * DM * 2, RSRC_W3);
            int kl = (lane * 43) >> 8, g = lane - 6 * kl;
            const unsigned dst = lds0 + L_W + w * 6912;
#pragma unroll
            for (int i = 0; i < 7; ++i) {
                const int vo = swap23(72 * w + kl) * 1152 + ((72 * pi) & ~15) + 16 * g;
                if (g < 5 && (i < 6 || lane < 48)) dma16(rs_wq, vo, dst + 1024 * i);
                g += 4; kl += 10; if (g >= 6) { g -= 6; kl += 1; }
            }
        }
        SBAR();
        arrive(L, C_X, lane); waitc(L, C_X, 8);
        constexpr int XPF = 4;
        u32x4 xa[9], xb[9];
        const unsigned short* xp = p.x + (size_t)(T0 + (lane >> 3)) * DM + 8 * (lane & 7);
#pragma unroll
        for (int s = 0; s < XPF; ++s) { xa[s] = *(const u32x4*)(xp + 64 * s); xb[s] = *(const u32x4*)(xp + 64 * s + 8 * DM); }
        SBAR();
        asm volatile("s_waitcnt vmcnt(8)" ::: "memory");
        SBAR();
        arrive(L, C_W, lane);
        f32x4 D0 = {}, D1 = {}, D2 = {};
        unsigned fl0 = 0;
        const unsigned* flp = (const unsigned*)(p.ws + WS_FLAG1 + xcd * 4096) + (lane & 31) * 32;
        {
            lds_p wb = L + L_W + (16 * (kq >> 1) + 4 * (kq & 1) + q4) * 96 + 8 * p4 + 8 * (pi & 1);
            lds_p xs = L + L_Q + w * 2048;
            lds_p xw = xs + (lane >> 3) * 128 + 16 * ((lane & 7) ^ (lane >> 4));
            lds_p xw2 = xs + 1024 + (lane >> 3) * 128 + 16 * ((lane & 7) ^ (4 + (lane >> 4)));
            lds_p xr0 = xs + c16 * 128 + 16 * (kq ^ ((c16 >> 1) & 7)), xr1 = xs + c16 * 128 + 16 * ((4 + kq) ^ ((c16 >> 1) & 7));
            lds_p wb2 = wb - 8 * p4 + 64;
            bf16x8 f0, f1, g0, g1;
            ldsw(xw, xa[0]); ldsw(xw2, xb[0]); f0 = ldsr<bf16x8>(xr0); f1 = ldsr<bf16x8>(xr1);
            waitc(L, C_W, 8);
            if (w >= 4) __builtin_amdgcn_s_setprio(1);
            STAMP(1);
            bf16x8 wf[2][3];
#define QRD(n, ks) do { wf[n][0] = cat8(ldtr(wb + (ks) * 3072), ldtr(wb + (ks) * 3072 + 768)); wf[n][1] = cat8(ldtr(wb + (ks) * 3072 + 32), ldtr(wb + (ks) * 3072 + 768 + 32)); \
                        wf[n][2] = cat8(ldtr(wb2 + (ks) * 3072), ldtr(wb2 + (ks) * 3072 + 768)); } while (0)
#define QMM(n, XF) do { D0 = MF16(wf[n][0], XF, D0); D1 = MF16(wf[n][1], XF, D1); D2 = MF16(wf[n][2], XF, D2); } while (0)
            QRD(0, 0);
#pragma unroll
            for (int s = 0; s < 9; ++s) {
                if (s + XPF < 9) { xa[s + XPF] = *(const u32x4*)(xp + 64 * (s + XPF)); xb[s + XPF] = *(const u32x4*)(xp + 64 * (s + XPF) + 8 * DM); }
                if (s == 8 && w == 7) fl0 = gload_sc1(flp);
                SBAR();
                QRD(1, 2 * s + 1);
                SBAR();
                if (s & 1) { QMM(0, g0); } else { QMM(0, f0); }
                SBAR();
                if (s < 8) {
                    ldsw(xw, xa[s + 1]); ldsw(xw2, xb[s + 1]);
                    if (s & 1) { f0 = ldsr<bf16x8>(xr0); } else { g0 = ldsr<bf16x8>(xr0); }
                    QRD(0, 2 * s + 2);
                }
                SBAR();
                if (s & 1) { QMM(1, g1); } else { QMM(1, f1); }
                SBAR();
                if (s < 8) { if (s & 1) { f1 = ldsr<bf16x8>(xr1); } else { g1 = ldsr<bf16x8>(xr1); } }
                SBAR();
            }
#undef QRD
#undef QMM
        }
        __builtin_amdgcn_s_setprio(0);
        STAMP(2);
        tag = (unsigned)__builtin_amdgcn_readfirstlane(epoch) + 1u;
        {
            lds_p qs = L + L_Q + w * 2048;
            ldsw(qs + c16 * 48 + 8 * kq, (u32x2){cvtpk(D0[0] * QSCALE, D0[1] * QSCALE), cvtpk(D0[2] * QSCALE, D0[3] * QSCALE)});
            const int a0 = kq == 0 ? c16 * 48 + 32 : (16 + c16) * 48 + 2 * (4 * kq - 2);
            const int a1 = (16 + c16) * 48 + 8 * kq;
            ldsw(qs + a0, cvtpk(D1[0] * QSCALE, D1[1] * QSCALE));
            ldsw(qs + a1, cvtpk(D1[2] * QSCALE, D1[3] * QSCALE));
            if (kq == 0) {
                ldsw(qs + (16 + c16) * 48 + 28, cvtpk(D2[0] * QSCALE, D2[1] * QSCALE));
                ldsw(qs + (16 + c16) * 48 + 32, cvtpk(D2[2] * QSCALE, D2[3] * QSCALE));
            }
        }
        arrive(L, C_Q, lane);
        if (w == 7) {
            if (!__all(fl0 == tag)) {
                for (unsigned spins = 0; spins < SPIN_GLB; ++spins) {
                    if (__all(gload_sc1(flp) == tag)) break;
                    __builtin_amdgcn_s_sleep(1);
                }
            }
            __builtin_amdgcn_fence(__ATOMIC_ACQUIRE, "wavefront");
            arrive(L, C_F1, lane);
            STAMP(8);
        }
        if (has_ssm) {
            u32x2 uu[4];
#pragma unroll
            for (int e = 0; e < 4; ++e) uu[e] = *(const u32x2*)(p.u + (size_t)(256 * w + 4 * lane + e) * DM + 72 * xcd + 4 * (idx - 14));
            const int uo = SCH_U + 512 + 2 * (256 * w + 4 * lane);
#pragma unroll
            for (int c = 0; c < 4; ++c) {
                const unsigned sel = (c & 1) ? 0x07060302u : 0x05040100u;
                ldsw(L + L_S + c * SCH + uo, (u32x2){__builtin_amdgcn_perm(uu[1][c >> 1], uu[0][c >> 1], sel), __builtin_amdgcn_perm(uu[3][c >> 1], uu[2][c >> 1], sel)});
            }
            arrive(L, C_U, lane);
        }
        waitc(L, C_Q, 8);
        {
            lds_p qa = L + L_Q + wq4 * 2048 + r32 * 48, qb = qa + 4 * 2048;
            qmA = ldsr<bf16x8>(qa + 16 * h); qmB = ldsr<bf16x8>(qb + 16 * h);
            qeA = ldsr<unsigned>(qa + 32); qeB = ldsr<unsigned>(qb + 32);
        }
        waitc(L, C_F1, 1);
        STAMP(3);
    }

    {
        lds_p ring = L + L_W + kh * (8 * KVT);
        const int dmo = WS_KV + (32 * kh + wq4) * KVT + lane * 16;
        const unsigned dmd = lds0 + L_W + kh * (8 * KVT) + wq4 * KVT;
#define KV_DMA_A(cc) do { dma16_sc1(rs_ws, dmo + (cc) * (4 * KVT), dmd + ((cc) & 1) * (4 * KVT)); dma16_sc1(rs_ws, dmo + (cc) * (4 * KVT) + 1024, dmd + ((cc) & 1) * (4 * KVT) + 1024); } while (0)
#define KV_DMA_B(cc) do { if (lane < 32) dma16_sc1(rs_ws, dmo + (cc) * (4 * KVT) + 2048, dmd + ((cc) & 1) * (4 * KVT) + 2048); } while (0)
        lds_p kb = ring + lane * 16, eb = ring + 2304 + r32 * 8, vb = ring + 1024 + (h * 20 + (r32 < 19 ? r32 : 19)) * 16;
        const f32x16 Z = {};
        unsigned mA = 0, mB = 0;
        f32x16 OA, OB;
        for (int pass = 0; pass < 2; ++pass) {
            KV_DMA_A(0); KV_DMA_B(0); KV_DMA_A(1); KV_DMA_B(1);
            asm volatile("s_waitcnt vmcnt(3)" ::: "memory");
            arrive(L, C_RW, lane); waitc(L, C_RW, 8 * (pass + 1));
            STAMP(9); if (pass == 0) PRB(1);
            if (pass == 0) {
                const bf16x8 km = ldsr<bf16x8>(L + L_W + lane * 16); const bf16x4 ke = ldsr<bf16x4>(L + L_W + 2304 + r32 * 8);
                const u32x2 ea = {h ? 0u : qeA, 0u}, ebq = {h ? 0u : qeB, 0u};
                f32x16 sa = MF32(km, qmA, Z), sb = MF32(km, qmB, Z);
                sa = MF32E(ke, __builtin_bit_cast(bf16x4, ea), sa); sb = MF32E(ke, __builtin_bit_cast(bf16x4, ebq), sb);
                float ma = sa[0], mb = sb[0];
#pragma unroll
                for (int i = 1; i < 16; ++i) { ma = fmaxf(ma, sa[i]); mb = fmaxf(mb, sb[i]); }
                auto ra = __builtin_amdgcn_permlane32_swap(__float_as_uint(ma), __float_as_uint(ma), false, false);
                auto rb = __builtin_amdgcn_permlane32_swap(__float_as_uint(mb), __float_as_uint(mb), false, false);
                ma = fmaxf(__uint_as_float(ra[0]), __uint_as_float(ra[1])); mb = fmaxf(__uint_as_float(rb[0]), __uint_as_float(rb[1]));
                const float ah = bflo(cvtpk(ma, 0.f)), bh = bflo(cvtpk(mb, 0.f));
                mA = cvtpk(-ah, ah - ma); mB = cvtpk(-bh, bh - mb);
            }
            const u32x2 ea = {h ? 0u : qeA, h ? 0u : mA}, ebq = {h ? 0u : qeB, h ? 0u : mB};
            const bf16x4 qxA = __builtin_bit_cast(bf16x4, ea), qxB = __builtin_bit_cast(bf16x4, ebq);
            f32x16 SA, SB = Z; u32x4 pa0, pa1, pb0 = {}, pb1 = {}; bf16x8 km, vx0, vx1, vy0, vy1; bf16x4 ke;
            OA = Z; OB = Z;
            km = ldsr<bf16x8>(kb); ke = ldsr<bf16x4>(eb);
            SA = MF32(km, qmA, Z); SA = MF32E(ke, qxA, SA);
            vx0 = ldsr<bf16x8>(vb); vx1 = ldsr<bf16x8>(vb + 640);
#define PH(SC, SN, QM, QX, ON, pp0, pp1, pn0, pn1, V0, V1, LD1, LD2, LD4) do { \
            ON = MF32(V0, BC8(pp0), ON); SBAR(); \
            LD1; \
            SC[0] = EXP2(SC[0]); SC[1] = EXP2(SC[1]); SC[2] = EXP2(SC[2]); SC[3] = EXP2(SC[3]); SBAR(); \
            SN = MF32(km, QM, Z); SBAR(); \
            LD2; \
            SC[4] = EXP2(SC[4]); SC[5] = EXP2(SC[5]); pn0[0] = cvtpk(SC[0], SC[1]); SC[6] = EXP2(SC[6]); SC[7] = EXP2(SC[7]); pn0[1] = cvtpk(SC[2], SC[3]); SBAR(); \
            ON = MF32(V1, BC8(pp1), ON); SBAR(); \
            SC[8] = EXP2(SC[8]); SC[9] = EXP2(SC[9]); pn0[2] = cvtpk(SC[4], SC[5]); SC[10] = EXP2(SC[10]); SC[11] = EXP2(SC[11]); pn0[3] = cvtpk(SC[6], SC[7]); PIN(pn0); SBAR(); \
            SN = MF32E(ke, QX, SN); SBAR(); \
            LD4; \
            SC[12] = EXP2(SC[12]); SC[13] = EXP2(SC[13]); pn1[0] = cvtpk(SC[8], SC[9]); SC[14] = EXP2(SC[14]); SC[15] = EXP2(SC[15]); pn1[1] = cvtpk(SC[10], SC[11]); \
            pn1[2] = cvtpk(SC[12], SC[13]); pn1[3] = cvtpk(SC[14], SC[15]); PIN(pn1); SBAR(); \
        } while (0)
#define VRD(t, N0, N1) (N0 = ldsr<bf16x8>(vb + (t) * KVT), N1 = ldsr<bf16x8>(vb + (t) * KVT + 640))
#define KMRD(t) (km = ldsr<bf16x8>(kb + (((t) + 1) & 7) * KVT))
#define KERD(t) (ke = ldsr<bf16x4>(eb + (((t) + 1) & 7) * KVT))
#define PHA(P0, P1, LD1, LD2, LD4) PH(SA, SB, qmB, qxB, OB, pb0, pb1, pa0, pa1, P0, P1, LD1, LD2, LD4);
#define PHB(N0, N1, LD1, LD2, LD4) PH(SB, SA, qmA, qxA, OA, pa0, pa1, pb0, pb1, N0, N1, LD1, LD2, LD4);
#define TA(t) PHA(vx0, vx1, VRD(t, vy0, vy1), KMRD(t), KERD(t))
#define TB() PHB(vy0, vy1, (void)0, (void)0, (void)0)
#define UA(t) PHA(vy0, vy1, VRD(t, vx0, vx1), KMRD(t), KERD(t))
#define UB() PHB(vx0, vx1, (void)0, (void)0, (void)0)
#define PRIO(x) __builtin_amdgcn_s_setprio(x);
#define LAST(t, b) \
            PHA(vy0, vy1, VRD(t, vx0, vx1), (void)0, (void)0) \
            asm volatile("s_waitcnt vmcnt(0)" ::: "memory"); \
            if ((b) < 7) { if (it == 0 && pass == 0) { arrive(L, C_B, lane); waitc(L, C_B, 8 * ((b) + 1)); } else __builtin_amdgcn_s_barrier(); } \
            CBAR(); PRIO(3) KMRD(t); KERD(t); SBAR(); \
            PHB(vx0, vx1, (void)0, if ((b) + 2 < 8) KV_DMA_A((b) + 2), if ((b) + 2 < 8) KV_DMA_B((b) + 2))
            PRIO(3)
            CSTAMP(10);
            for (int it = 0; it < 4; ++it) {
                TA(0) PRIO(2) TB() UA(1) PRIO(1) UB() TA(2) PRIO(0) TB()
                LAST(3, 2 * it)
                if (it == 1 && pass == 0) {
                    const __amdgpu_buffer_rsrc_t rs_wo = __builtin_amdgcn_make_buffer_rsrc((void*)p.wo, 0, DM * DM * 2, RSRC_W3);
                    int kp = 64 * w + lane; const int k9 = (kp * 7282) >> 16; int g = kp - 9 * k9; kp = k9;
                    for (int inst = w; inst < 81; inst += 8) {
                        dma16(rs_wo, swap23(kp) * 1152 + 144 * (pi >> 1) + 16 * g, lds0 + L_S + 1024 * inst);
                        g += 8; kp += 56; if (g >= 9) { g -= 9; kp += 1; }
                    }
                }
                TA(4) PRIO(2) TB() UA(5) PRIO(1) UB() TA(6) PRIO(0) TB()
                LAST(7, 2 * it + 1)
            }
            OB = MF32(vx0, BC8(pb0), OB); OB = MF32(vx1, BC8(pb1), OB);
#undef TA
#undef TB
#undef UA
#undef UB
#undef LAST
#undef PHA
#undef PHB
#undef VRD
#undef KMRD
#undef KERD
#undef PH
            PRIO(0)
            STAMP(4); CSTAMP(11); PRB(2);
            lds_p xo = (w < 4 ? L + L_X1 : L + L_Q) + wq4 * 3072, xi = (w < 4 ? L + L_Q : L + L_X1) + wq4 * 3072;
            {
                const f32x16& S = kh ? OA : OB;
#pragma unroll
                for (int g = 0; g < 3; ++g) ldsw(xo + g * 1024 + lane * 16, (f32x4){S[4 * g], S[4 * g + 1], S[4 * g + 2], S[4 * g + 3]});
            }
            BARRIER();
            STAMP(12);
            float o[12];
#pragma unroll
            for (int g = 0; g < 3; ++g) {
                const f32x4 t = ldsr<f32x4>(xi + g * 1024 + lane * 16);
#pragma unroll
                for (int i = 0; i < 4; ++i) o[4 * g + i] = (kh ? OB[4 * g + i] : OA[4 * g + i]) + t[i];
            }
            const float lsum = __uint_as_float(__builtin_amdgcn_permlane32_swap(__float_as_uint(o[10]), __float_as_uint(o[10]), false, false)[0]);
            const float rl = __builtin_amdgcn_rcpf(lsum);
            bool bad = !(lsum < 1.2676506e30f);
#pragma unroll
            for (int i = 0; i < 8; ++i) bad |= !(fabsf(o[i]) < 3.0e38f);
            if (h == 0) bad |= !(fabsf(o[8]) < 3.0e38f) || !(fabsf(o[9]) < 3.0e38f);
            {
                lds_p os = xi;
                lds_p ob = os + (r32 & 15) * 72 + (r32 >> 4) * 36 + 8 * h;
                ldsw(ob, cvtpk(o[0] * rl, o[1] * rl)); ldsw(ob + 4, cvtpk(o[2] * rl, o[3] * rl));
                ldsw(ob + 16, cvtpk(o[4] * rl, o[5] * rl)); ldsw(ob + 20, cvtpk(o[6] * rl, o[7] * rl));
                if (h == 0) ldsw(ob + 32, cvtpk(o[8] * rl, o[9] * rl));
#pragma unroll
                for (int i = 0; i < 3; ++i) {
                    const int e = lane + 64 * i;
                    if (e < 144) {
                        const int tk = e & 15, pc = e >> 4, k0 = 36 * pi + 4 * pc;
                        const u32x2 v = ldsr<u32x2>(os + tk * 72 + pc * 8);
                        __builtin_amdgcn_raw_buffer_store_b64(v, rs_ws, WS_O + (8 * tb + w) * 18432 + (k0 >> 3) * 256 + tk * 16 + (k0 & 4) * 2, 0, AUX_SC1);
                    }
                }
            }
            if (pass == 0 && __any(bad) && lane == 0) ((volatile LAS unsigned*)(L + L_C))[C_BAD] = 1u;
            STAMP(13);
            DRAIN();
            BARRIER();
            const bool redo = pass == 0 && __builtin_amdgcn_readfirstlane(((volatile LAS unsigned*)(L + L_C))[C_BAD]) != 0;
            if (!redo) break;
            {
                const u32x2 ea0 = {h ? 0u : qeA, 0u}, eb0 = {h ? 0u : qeB, 0u};
                float ma = -INFINITY, mb = -INFINITY;
                for (int t = 0; t < 64; ++t) {
                    const u32x4 kmv = __builtin_amdgcn_raw_buffer_load_b128(rs_ws, WS_KV + t * KVT + lane * 16, 0, AUX_SC1);
                    const u32x2 kev = __builtin_amdgcn_raw_buffer_load_b64(rs_ws, WS_KV + t * KVT + 2304 + r32 * 8, 0, AUX_SC1);
                    f32x16 sa = MF32(BC8(kmv), qmA, Z), sb = MF32(BC8(kmv), qmB, Z);
                    sa = MF32E(__builtin_bit_cast(bf16x4, kev), __builtin_bit_cast(bf16x4, ea0), sa);
                    sb = MF32E(__builtin_bit_cast(bf16x4, kev), __builtin_bit_cast(bf16x4, eb0), sb);
#pragma unroll
                    for (int i = 0; i < 16; ++i) { ma = fmaxf(ma, sa[i]); mb = fmaxf(mb, sb[i]); }
                }
                ma = fmaxf(ma, __shfl_xor(ma, 32)); mb = fmaxf(mb, __shfl_xor(mb, 32));
                const float ah = bflo(cvtpk(ma, 0.f)), bh = bflo(cvtpk(mb, 0.f));
                mA = cvtpk(-ah, ah - ma); mB = cvtpk(-bh, bh - mb);
            }
        }
#undef KV_DMA_A
#undef KV_DMA_B
#undef PRIO
        if (w == 0 && lane == 0) gstore_sc1((unsigned*)(p.ws + WS_FLAG2 + (tb * 16 + pi) * 128), tag);
        STAMP(5);
    }

    {
        constexpr int PF3 = 5;
        lds_p wb = L + L_S + (16 * (kq >> 1) + 4 * (kq & 1) + q4) * 144 + 8 * p4 + kh * (9 * 32 * 144);
        bf16x8 wf[PF3][5];
#define WORD3(i, T) cat8(ldtr(wb + (i) * 4608 + 32 * (T)), ldtr(wb + (i) * 4608 + 1152 + 32 * (T)))
#pragma unroll
        for (int i = 0; i < PF3; ++i)
#pragma unroll
            for (int T = 0; T < 5; ++T) wf[i][T] = WORD3(i, T);
        SBAR();
        if (w == 7) {
            const unsigned* f = (const unsigned*)(p.ws + WS_FLAG2 + tb * 2048) + (lane & 15) * 32;
            for (unsigned spins = 0; spins < SPIN_GLB; ++spins) {
                if (__all(gload_sc1(f) == tag)) break;
                __builtin_amdgcn_s_sleep(1);
            }
            __builtin_amdgcn_fence(__ATOMIC_ACQUIRE, "wavefront");
        }
        BARRIER();
        STAMP(6);
        const int Tt = 128 * tb + 64 * (pi & 1) + 16 * wq4, F0 = 72 * (pi >> 1);
        u32x4 of[9];
#pragma unroll
        for (int i = 0; i < 9; ++i) of[i] = __builtin_amdgcn_raw_buffer_load_b128(rs_ws, WS_O + (Tt >> 4) * 18432 + (9 * kh + i) * 1024 + lane * 16, 0, AUX_SC1);
        SBAR();
        f32x4 D[5] = {};
#pragma unroll
        for (int i = 0; i < 9; ++i) {
#pragma unroll
            for (int T = 0; T < 5; ++T) D[T] = MF16(wf[i % PF3][T], BC8(of[i]), D[T]);
            SBAR();
            if (i + PF3 < 9) {
#pragma unroll
                for (int T = 0; T < 5; ++T) wf[i % PF3][T] = WORD3(i + PF3, T);
            }
            SBAR();
        }
#undef WORD3
        lds_p xo = L + L_W + w * 3072, xi = L + L_W + (w ^ 4) * 3072;
        if (kh == 0) { ldsw(xo + lane * 16, D[3]); ldsw(xo + 1024 + lane * 16, D[4]); }
        else { ldsw(xo + lane * 16, D[0]); ldsw(xo + 1024 + lane * 16, D[1]); ldsw(xo + 2048 + lane * 16, D[2]); }
        BARRIER();
        unsigned short* op = p.out + (size_t)(Tt + c16) * (2 * DM) + F0 + 4 * kq;
        if (kh == 0) {
#pragma unroll
            for (int T = 0; T < 3; ++T) {
                const f32x4 v = D[T] + ldsr<f32x4>(xi + T * 1024 + lane * 16);
                *(u32x2*)(op + 16 * T) = (u32x2){cvtpk(v[0], v[1]), cvtpk(v[2], v[3])};
            }
        } else {
#pragma unroll
            for (int T = 3; T < 5; ++T) {
                const f32x4 v = D[T] + ldsr<f32x4>(xi + (T - 3) * 1024 + lane * 16);
                if (T == 3 || kq < 2) *(u32x2*)(op + 16 * T) = (u32x2){cvtpk(v[0], v[1]), cvtpk(v[2], v[3])};
            }
        }
        STAMP(7);
        STAMP_DUMP();
        PRB(3);
        if (bid == PROBE_WG && w == 0) { const unsigned long long d_ = MK_PROBE == 1 ? prb[1] - prb[0] : MK_PROBE == 2 ? prb[2] - prb[1] : MK_PROBE == 3 ? prb[3] - prb[2] : 500;
            while (__builtin_amdgcn_s_memrealtime() < prb[3] + 2 * d_) __builtin_amdgcn_s_sleep(1); }
        if (bid == 0 && w == 7) {
            const unsigned* f = (const unsigned*)(p.ws + WS_FLAG2);
            for (unsigned spins = 0; spins < SPIN_GLB; ++spins) {
                bool ok = true;
#pragma unroll
                for (int i = 0; i < 4; ++i) { const int e = lane + 64 * i; ok &= gload_sc1(f + e * 32) == tag; }
                if (__all(ok)) break;
                __builtin_amdgcn_s_sleep(2);
            }
            if (lane == 0) gstore_sc1((unsigned*)(p.ws + WS_EPOCH), tag);
        }
    }
}

}

extern "C" void kernel_launch(void* const* d_in, const int* in_sizes, int n_in, void* d_out, int out_size, void* d_ws, size_t ws_size,
                              hipStream_t stream) {
    Params p;
    p.x = (const unsigned short*)d_in[0]; p.wq = (const unsigned short*)d_in[1]; p.wk = (const unsigned short*)d_in[2];
    p.wv = (const unsigned short*)d_in[3]; p.wo = (const unsigned short*)d_in[4]; p.u = (const unsigned short*)d_in[5];
    p.are = (const unsigned short*)d_in[6]; p.aim = (const unsigned short*)d_in[7]; p.bre = (const unsigned short*)d_in[8];
    p.bim = (const unsigned short*)d_in[9]; p.cre = (const unsigned short*)d_in[10]; p.cim = (const unsigned short*)d_in[11];
    p.ldt = (const unsigned short*)d_in[12]; p.dsk = (const unsigned short*)d_in[13];
    p.out = (unsigned short*)d_out; p.ws = (char*)d_ws;
    (void)hipFuncSetAttribute((const void*)fused_kernel, hipFuncAttributeMaxDynamicSharedMemorySize, L_END);
    fused_kernel<<<256, 768, L_END, stream>>>(p);
}
```

```cpp
#include <hip/hip_runtime.h>
#include <hip/hip_bf16.h>
#include <stdint.h>

namespace {

typedef __attribute__((ext_vector_type(16))) float f32x16;
typedef __attribute__((ext_vector_type(4))) float f32x4;
typedef __attribute__((ext_vector_type(2))) float f32x2;
typedef __attribute__((ext_vector_type(8))) short bf16x8;
typedef __attribute__((ext_vector_type(4))) short bf16x4;
typedef __attribute__((ext_vector_type(2))) __bf16 bf16x2_t;
typedef __attribute__((ext_vector_type(4))) unsigned u32x4;
typedef __attribute__((ext_vector_type(2))) unsigned u32x2;
typedef short v4i16_t __attribute__((ext_vector_type(4)));
#define LAS __attribute__((address_space(3)))
#define GAS __attribute__((address_space(1)))
typedef LAS char* lds_p;
typedef const unsigned short* cbf;

constexpr int SEQ = 2048, DM = 576;
constexpr float QSCALE = 0.34005040627f;
constexpr float LOG2E = 1.4426950408889634f;
constexpr float INV2PI = 0.15915494309189535f;

constexpr int WS_EPOCH = 0;
constexpr int WS_FLAG1 = 4096;
constexpr int WS_FLAG2 = WS_FLAG1 + 32768;
constexpr int WS_FLAG3 = WS_FLAG2 + 32768;
constexpr int WS_KV = WS_FLAG3 + 8192;
constexpr int KVT = 2560;
constexpr int WS_O = WS_KV + 64 * KVT;
constexpr int WS_UT = WS_O + SEQ * DM * 2;
constexpr int UT_PITCH = 4096 + 384;
constexpr int WS_BYTES = WS_UT + DM * UT_PITCH;

constexpr int L_W = 0;
constexpr int L_X1 = 40960;
constexpr int L_S = 55296;
constexpr int L_Q = L_S + 87360;
constexpr int L_C = L_Q + 16384;
constexpr int L_END = L_C + 256;
constexpr int SCH = 21840;
constexpr int SCH_U = 16640;
enum { C_W = 0, C_P, C_Q, C_F1, C_RW, C_B, C_BAD, C_U, C_S2, C_D, C_KW, C_X, C_NUM };

constexpr int RSRC_W3 = 0x00020000;
constexpr int AUX_SC1 = 16;
constexpr unsigned SPIN_LDS = 1u << 20, SPIN_GLB = 1u << 16;

#define SBAR() __builtin_amdgcn_sched_barrier(0)
#define PIN(x) asm volatile("" : "+v"(x))
#define CBAR() asm volatile("" ::: "memory")
#define DRAIN() asm volatile("s_waitcnt vmcnt(0)" ::: "memory")
#define MF32(a, b, c) __builtin_amdgcn_mfma_f32_32x32x16_bf16(a, b, c, 0, 0, 0)
#define MF32E(a, b, c) __builtin_amdgcn_mfma_f32_32x32x8bf16_1k(a, b, c, 0, 0, 0)
#define MF16(a, b, c) __builtin_amdgcn_mfma_f32_16x16x32_bf16(a, b, c, 0, 0, 0)
#define EXP2(x) __builtin_amdgcn_exp2f(x)
#define BC8(x) __builtin_bit_cast(bf16x8, x)
#define DI __device__ __forceinline__

DI unsigned cvtpk(float lo, float hi) { f32x2 v = {lo, hi}; return __builtin_bit_cast(unsigned, __builtin_convertvector(v, bf16x2_t)); }
DI float bflo(unsigned x) { return __builtin_bit_cast(float, x << 16); }
DI float bfhi(unsigned x) { return __builtin_bit_cast(float, x & 0xffff0000u); }
DI float bf1(unsigned short x) { return __builtin_bit_cast(float, (unsigned)x << 16); }
template <class T> DI T ldsr(lds_p p) { return *(const LAS T*)p; }
template <class T> DI void ldsw(lds_p p, T v) { *(LAS T*)p = v; }
DI bf16x4 ldtr(lds_p p) { return __builtin_bit_cast(bf16x4, __builtin_amdgcn_ds_read_tr16_b64_v4i16((LAS v4i16_t*)p)); }
DI bf16x8 cat8(bf16x4 a, bf16x4 b) { return __builtin_shufflevector(a, b, 0, 1, 2, 3, 4, 5, 6, 7); }
DI int swap23(int k) { return (k & ~12) | ((k & 4) << 1) | ((k & 8) >> 1); }
DI void dma16(__amdgpu_buffer_rsrc_t rs, int voff, unsigned lds_base) {
    unsigned keep;
    asm volatile("s_mov_b32 %0, m0\n\ts_mov_b32 m0, %3\n\ts_nop 0\n\tbuffer_load_dwordx4 %1, %2, 0 offen lds\n\ts_mov_b32 m0, %0"
                 : "=&s"(keep) : "v"(voff), "s"(rs), "s"(lds_base) : "memory");
}
DI void dma16_sc1(__amdgpu_buffer_rsrc_t rs, int voff, unsigned lds_base) {
    unsigned keep;
    asm volatile("s_mov_b32 %0, m0\n\ts_mov_b32 m0, %3\n\ts_nop 0\n\tbuffer_load_dwordx4 %1, %2, 0 offen sc1 lds\n\ts_mov_b32 m0, %0"
                 : "=&s"(keep) : "v"(voff), "s"(rs), "s"(lds_base) : "memory");
}
#define BARRIER() asm volatile("s_waitcnt lgkmcnt(0)\n\ts_barrier" ::: "memory")
DI void arrive(lds_p L, int c, int lane) {
    (void)lane;
    unsigned long long keep;
    asm volatile("s_mov_b64 %0, exec\n\ts_mov_b64 exec, 1\n\tds_add_u32 %1, %2\n\ts_mov_b64 exec, %0" : "=&s"(keep) : "v"((unsigned)(uintptr_t)(L + L_C + 4 * c)), "v"(1u) : "memory");
}
template <int SLEEP = 1> DI void waitc(lds_p L, int c, unsigned target) {
    CBAR();
    volatile LAS unsigned* p = (volatile LAS unsigned*)(L + L_C) + c;
    for (unsigned spins = 0; spins < SPIN_LDS; ++spins) {
        if ((unsigned)__builtin_amdgcn_readfirstlane(*p) >= target) break;
        __builtin_amdgcn_s_sleep(SLEEP);
    }
    CBAR();
}
DI unsigned peek(lds_p L, int c) { return *((volatile LAS unsigned*)(L + L_C) + c); }
DI void waitp(lds_p L, int c, unsigned seen, unsigned target) { if ((unsigned)__builtin_amdgcn_readfirstlane(seen) < target) waitc(L, c, target); CBAR(); }
DI unsigned gload_sc1(const unsigned* p) { return __hip_atomic_load((GAS unsigned*)p, __ATOMIC_RELAXED, __HIP_MEMORY_SCOPE_AGENT); }
DI void gstore_sc1(unsigned* p, unsigned v) { __hip_atomic_store((GAS unsigned*)p, v, __ATOMIC_RELAXED, __HIP_MEMORY_SCOPE_AGENT); }

struct cplx2 { f32x2 re, im; };
DI cplx2 cmul(const cplx2& a, const cplx2& b) { cplx2 r; r.re = a.re * b.re - a.im * b.im; r.im = a.re * b.im + a.im * b.re; return r; }
DI cplx2 cexp(f32x2 ar2, f32x2 airev, float e) {
    f32x2 m = ar2 * e, t = airev * e; cplx2 r;
#pragma unroll
    for (int i = 0; i < 2; ++i) {
        const float mag = EXP2(m[i]), fr = __builtin_amdgcn_fractf(t[i]);
        r.re[i] = mag * __builtin_amdgcn_cosf(fr); r.im[i] = mag * __builtin_amdgcn_sinf(fr);
    }
    return r;
}

struct Params {
    const unsigned short *x, *wq, *wk, *wv, *wo, *u, *are, *aim, *bre, *bim, *cre, *cim, *ldt, *dsk;
    unsigned short* out;
    char* ws;
};

DI void ut_wave(const Params& p, lds_p L, __amdgpu_buffer_rsrc_t rs_ws, int s, int lane, int xcd, int wg) {
    __builtin_amdgcn_s_setprio(3);
    const unsigned epoch = gload_sc1((const unsigned*)(p.ws + WS_EPOCH));
    const int tile = 4 * wg + s;
    u32x4 v[9];
    {
        int row = (lane * 57) >> 9, g = lane - 9 * row;
#pragma unroll
        for (int i = 0; i < 9; ++i) {
            v[i] = *(const u32x4*)(p.u + (size_t)(64 * tile + row) * DM + 72 * xcd + 8 * g);
            g += 1; row += 7; if (g >= 9) { g -= 9; row += 1; }
        }
    }
    SBAR();
    BARRIER();
    const int i16 = lane & 15, gq = lane >> 4;
    lds_p T = L + L_S + s * 10240;
#pragma unroll
    for (int i = 0; i < 9; ++i) ldsw(T + 1024 * i + lane * 16, v[i]);
    lds_p rb = T + (8 * gq + (i16 >> 2)) * 144 + 8 * (i16 & 3);
    const int so = WS_UT + (72 * xcd + (i16 & 7)) * UT_PITCH + (64 * tile + 8 * gq + 32 * (i16 >> 3)) * 2;
#pragma unroll
    for (int cg = 0; cg < 5; ++cg) {
        const u32x4 A = __builtin_bit_cast(u32x4, cat8(ldtr(rb + 32 * cg), ldtr(rb + 4 * 144 + 32 * cg)));
        const u32x4 B = __builtin_bit_cast(u32x4, cat8(ldtr(rb + 32 * 144 + 32 * cg), ldtr(rb + 36 * 144 + 32 * cg)));
        u32x4 X1, X2;
#pragma unroll
        for (int e = 0; e < 4; ++e) {
            const unsigned ar = __builtin_amdgcn_update_dpp(0u, A[e], 0x128, 0xf, 0xf, false), br = __builtin_amdgcn_update_dpp(0u, B[e], 0x128, 0xf, 0xf, false);
            X1[e] = i16 < 8 ? A[e] : br; X2[e] = i16 < 8 ? ar : B[e];
        }
        __builtin_amdgcn_raw_buffer_store_b128(X1, rs_ws, so + 16 * cg * UT_PITCH, 0, AUX_SC1);
        if (cg < 4) __builtin_amdgcn_raw_buffer_store_b128(X2, rs_ws, so + (16 * cg + 8) * UT_PITCH, 0, AUX_SC1);
    }
    const unsigned tag = (unsigned)__builtin_amdgcn_readfirstlane(epoch) + 1u;
    DRAIN();
    arrive(L, C_P, lane);
    if (s == 0) {
        waitc(L, C_P, 4);
        if (lane == 0) gstore_sc1((unsigned*)(p.ws + WS_FLAG3 + (8 * xcd + wg) * 128), tag);
    }
}

DI void dma16s(__amdgpu_buffer_rsrc_t rs, int voff, int soff, unsigned lds_base) {
    unsigned keep;
    asm volatile("s_mov_b32 %0, m0\n\ts_mov_b32 m0, %4\n\ts_nop 0\n\tbuffer_load_dwordx4 %1, %2, %3 offen lds\n\ts_mov_b32 m0, %0"
                 : "=&s"(keep) : "v"(voff), "s"(rs), "s"(soff), "s"(lds_base) : "memory");
}
DI void kv_wave(const Params& p, lds_p L, unsigned lds0, __amdgpu_buffer_rsrc_t rs_ws, int s, int lane, int xcd, int tt) {
    __builtin_amdgcn_s_setprio(3);
    const unsigned epoch = gload_sc1((const unsigned*)(p.ws + WS_EPOCH));
    const int T0 = 256 * xcd + 16 * tt;
    {
        const __amdgpu_buffer_rsrc_t rs_wk = __builtin_amdgcn_make_buffer_rsrc((void*)p.wk, 0, DM * 18 * 2, RSRC_W3);
        const __amdgpu_buffer_rsrc_t rs_wv = __builtin_amdgcn_make_buffer_rsrc((void*)p.wv, 0, DM * 18 * 2, RSRC_W3);
        int vo[3];
#pragma unroll
        for (int j = 0; j < 3; ++j) { const int sg = 64 * j + lane, kp = (sg * 171) >> 9; vo[j] = swap23(kp) * 36 + 16 * (sg - 3 * kp); }
        for (int b = s; b < 9; b += 4) {
#pragma unroll
            for (int j = 0; j < 3; ++j) {
                dma16s(rs_wk, vo[j], 2304 * b, lds0 + L_S + 3072 * b + 1024 * j);
                dma16s(rs_wv, vo[j], 2304 * b, lds0 + L_S + 27648 + 3072 * b + 1024 * j);
            }
        }
    }
    SBAR();
    u32x4 xa[9], xb[9];
    {
        const unsigned short* xp = p.x + (size_t)(T0 + (lane >> 3)) * DM + 8 * (lane & 7);
#pragma unroll
        for (int i = 0; i < 9; ++i) { xa[i] = *(const u32x4*)(xp + 64 * i); xb[i] = *(const u32x4*)(xp + 64 * i + 8 * DM); }
    }
    SBAR();
    BARRIER();
    arrive(L, C_D, lane);
    asm volatile("s_waitcnt vmcnt(18)" ::: "memory");
    arrive(L, C_KW, lane);
    const int c16 = lane & 15, kq = lane >> 4, q4 = (lane >> 2) & 3, p4 = lane & 3;
    lds_p xs = L + L_S + 55296 + s * 2048;
    lds_p xw = xs + (lane >> 3) * 128 + 16 * ((lane & 7) ^ (lane >> 4)), xw2 = xs + 1024 + (lane >> 3) * 128 + 16 * ((lane & 7) ^ (4 + (lane >> 4)));
    lds_p xr0 = xs + c16 * 128 + 16 * (kq ^ ((c16 >> 1) & 7)), xr1 = xs + c16 * 128 + 16 * ((4 + kq) ^ ((c16 >> 1) & 7));
    const int rowb = (16 * (kq >> 1) + 4 * (kq & 1) + q4) * 48;
    lds_p wk = L + L_S + rowb + 8 * p4, wv = wk + 27648, wx = L + L_S + rowb + 32 + (p4 == 1 ? 27648 : 0);
    bf16x8 f0, f1, g0, g1;
    ldsw(xw, xa[0]); ldsw(xw2, xb[0]); f0 = ldsr<bf16x8>(xr0); f1 = ldsr<bf16x8>(xr1);
    waitc(L, C_KW, 4);
    f32x4 DK = {}, DV = {}, DX = {}, DY = {};
    bf16x8 wa[2], wbv[2], wc[2];
#define KVRD(n, ks) do { wa[n] = cat8(ldtr(wk + (ks) * 1536), ldtr(wk + (ks) * 1536 + 384)); wbv[n] = cat8(ldtr(wv + (ks) * 1536), ldtr(wv + (ks) * 1536 + 384)); \
                         wc[n] = cat8(ldtr(wx + (ks) * 1536), ldtr(wx + (ks) * 1536 + 384)); } while (0)
#define KVMM(n, XF) do { DK = MF16(wa[n], XF, DK); DX = MF16(wc[n], XF, DX);        \
                         DV = MF16(XF, wbv[n], DV); DY = MF16(XF, wc[n], DY);       } while (0)
    KVRD(0, 0);
#pragma unroll
    for (int i = 0; i < 9; ++i) {
        KVRD(1, 2 * i + 1);
        SBAR();
        if (i & 1) { KVMM(0, g0); } else { KVMM(0, f0); }
        SBAR();
        if (i < 8) {
            ldsw(xw, xa[i + 1]); ldsw(xw2, xb[i + 1]);
            if (i & 1) { f0 = ldsr<bf16x8>(xr0); } else { g0 = ldsr<bf16x8>(xr0); }
            KVRD(0, 2 * i + 2);
        }
        SBAR();
        if (i & 1) { KVMM(1, g1); } else { KVMM(1, f1); }
        SBAR();
        if (i < 8) { if (i & 1) { f1 = ldsr<bf16x8>(xr1); } else { g1 = ldsr<bf16x8>(xr1); } }
        SBAR();
    }
#undef KVRD
#undef KVMM
    const int tbase = WS_KV + (8 * xcd + (tt >> 1)) * KVT, rr = 16 * (tt & 1) + c16, s16 = tt & 1;
    __builtin_amdgcn_raw_buffer_store_b64((u32x2){cvtpk(DK[0], DK[1]), cvtpk(DK[2], DK[3])}, rs_ws, tbase + ((kq >> 1) * 32 + rr) * 16 + (kq & 1) * 8, 0, AUX_SC1);
    if (kq == 0) __builtin_amdgcn_raw_buffer_store_b64((u32x2){cvtpk(DX[0], DX[1]), 0x3F803F80u}, rs_ws, tbase + 2304 + rr * 8, 0, AUX_SC1);
    const int vo = tbase + 1024 + (s16 * 2 + (kq & 1)) * 320 + (kq >> 1) * 8;
    __builtin_amdgcn_raw_buffer_store_b64((u32x2){cvtpk(DV[0], DV[1]), cvtpk(DV[2], DV[3])}, rs_ws, vo + c16 * 16, 0, AUX_SC1);
    if (c16 >= 4 && c16 < 8) {
        u32x2 v = {cvtpk(DY[0], DY[1]), cvtpk(DY[2], DY[3])};
        if (c16 == 6) v = (u32x2){0x3F803F80u, 0x3F803F80u};
        if (c16 == 7) v = (u32x2){0u, 0u};
        __builtin_amdgcn_raw_buffer_store_b64(v, rs_ws, vo + (12 + c16) * 16, 0, AUX_SC1);
    }
    const unsigned tag = (unsigned)__builtin_amdgcn_readfirstlane(epoch) + 1u;
    DRAIN();
    arrive(L, C_P, lane);
    if (s == 0) {
        waitc(L, C_P, 4);
        if (lane < 8) gstore_sc1((unsigned*)(p.ws + WS_FLAG1 + lane * 4096 + (4 * xcd + (tt >> 2)) * 128), tag);
    }
}

#define CONV_ASM_0 \
    "ds_read_b128 v[116:119], %9 offset:512\n\t" \
    "ds_read_b64 v[100:101], %8 offset:512\n\t" \
    "ds_read_b128 v[120:123], %9 offset:1024\n\t" \
    "ds_read_b64 v[102:103], %8 offset:520\n\t" \
    "ds_read_b128 v[124:127], %9 offset:1536\n\t" \
    "ds_read_b64 v[104:105], %10 offset:0\n\t" \
    "ds_read_b128 v[128:131], %9 offset:2048\n\t" \
    "ds_read_b64 v[106:107], %10 offset:8\n\t" \
    "ds_read_b128 v[132:135], %9 offset:2560\n\t" \
    "ds_read_b128 v[136:139], %9 offset:3072\n\t" \
    "ds_read_b128 v[140:143], %9 offset:3584\n\t" \
    "ds_read_b128 v[144:147], %9 offset:4096\n\t" \
    "ds_read_b128 v[148:151], %11 offset:3584\n\t" \
    "s_waitcnt lgkmcnt(5)\n\t" \
    "v_mfma_f32_16x16x32_bf16 %0, v[100:103], v[116:119], %0\n\t" \
    "ds_read_b128 v[116:119], %9 offset:448\n\t" \
    "ds_read_b64 v[108:109], %8 offset:448\n\t" \
    "v_mfma_f32_16x16x32_bf16 %1, v[100:103], v[120:123], %1\n\t" \
    "ds_read_b128 v[120:123], %9 offset:960\n\t" \
    "ds_read_b64 v[110:111], %8 offset:456\n\t" \
    "v_mfma_f32_16x16x32_bf16 %2, v[100:103], v[124:127], %2\n\t" \
    "ds_read_b128 v[124:127], %9 offset:1472\n\t" \
    "ds_read_b64 v[112:113], %10 offset:64\n\t" \
    "v_mfma_f32_16x16x32_bf16 %3, v[100:103], v[128:131], %3\n\t" \
    "ds_read_b128 v[128:131], %9 offset:1984\n\t" \
    "ds_read_b64 v[114:115], %10 offset:72\n\t" \
    "s_waitcnt lgkmcnt(10)\n\t" \
    "v_mfma_f32_16x16x32_bf16 %4, v[100:103], v[132:135], %4\n\t" \
    "ds_read_b128 v[132:135], %9 offset:2496\n\t" \
    "v_mfma_f32_16x16x32_bf16 %5, v[100:103], v[136:139], %5\n\t" \
    "ds_read_b128 v[136:139], %9 offset:3008\n\t" \
    "v_mfma_f32_16x16x32_bf16 %6, v[100:103], v[140:143], %6\n\t" \
    "ds_read_b128 v[140:143], %9 offset:3520\n\t" \
    "s_waitcnt lgkmcnt(11)\n\t" \
    "v_mfma_f32_16x16x32_bf16 %7, v[100:103], v[144:147], %7\n\t" \
    "ds_read_b128 v[144:147], %9 offset:4032\n\t" \
    "v_mfma_f32_16x16x32_bf16 %7, v[104:107], v[148:151], %7\n\t" \
    "ds_read_b128 v[148:151], %11 offset:3648\n\t" \
    "s_waitcnt lgkmcnt(5)\n\t" \
    "v_mfma_f32_16x16x32_bf16 %0, v[108:111], v[116:119], %0\n\t" \
    "ds_read_b128 v[116:119], %9 offset:384\n\t" \
    "ds_read_b64 v[100:101], %8 offset:384\n\t" \
    "v_mfma_f32_16x16x32_bf16 %1, v[108:111], v[120:123], %1\n\t" \
    "ds_read_b128 v[120:123], %9 offset:896\n\t" \
    "ds_read_b64 v[102:103], %8 offset:392\n\t" \
    "v_mfma_f32_16x16x32_bf16 %2, v[108:111], v[124:127], %2\n\t" \
    "ds_read_b128 v[124:127], %9 offset:1408\n\t" \
    "ds_read_b64 v[104:105], %10 offset:128\n\t" \
    "v_mfma_f32_16x16x32_bf16 %3, v[108:111], v[128:131], %3\n\t" \
    "ds_read_b128 v[128:131], %9 offset:1920\n\t" \
    "ds_read_b64 v[106:107], %10 offset:136\n\t" \
    "s_waitcnt lgkmcnt(10)\n\t" \
    "v_mfma_f32_16x16x32_bf16 %4, v[108:111], v[132:135], %4\n\t" \
    "ds_read_b128 v[132:135], %9 offset:2432\n\t" \
    "v_mfma_f32_16x16x32_bf16 %5, v[108:111], v[136:139], %5\n\t" \
    "ds_read_b128 v[136:139], %9 offset:2944\n\t" \
    "v_mfma_f32_16x16x32_bf16 %6, v[108:111], v[140:143], %6\n\t" \
    "ds_read_b128 v[140:143], %9 offset:3456\n\t" \
    "s_waitcnt lgkmcnt(11)\n\t" \
    "v_mfma_f32_16x16x32_bf16 %7, v[108:111], v[144:147], %7\n\t" \
    "ds_read_b128 v[144:147], %9 offset:3968\n\t" \
    "v_mfma_f32_16x16x32_bf16 %7, v[112:115], v[148:151], %7\n\t" \
    "ds_read_b128 v[148:151], %11 offset:3712\n\t" \
    "s_waitcnt lgkmcnt(5)\n\t" \
    "v_mfma_f32_16x16x32_bf16 %0, v[100:103], v[116:119], %0\n\t" \
    "ds_read_b128 v[116:119], %9 offset:320\n\t" \
    "ds_read_b64 v[108:109], %8 offset:320\n\t" \
    "v_mfma_f32_16x16x32_bf16 %1, v[100:103], v[120:123], %1\n\t" \
    "ds_read_b128 v[120:123], %9 offset:832\n\t" \
    "ds_read_b64 v[110:111], %8 offset:328\n\t" \
    "v_mfma_f32_16x16x32_bf16 %2, v[100:103], v[124:127], %2\n\t" \
    "ds_read_b128 v[124:127], %9 offset:1344\n\t" \
    "ds_read_b64 v[112:113], %10 offset:192\n\t" \
    "v_mfma_f32_16x16x32_bf16 %3, v[100:103], v[128:131], %3\n\t" \
    "ds_read_b128 v[128:131], %9 offset:1856\n\t" \
    "ds_read_b64 v[114:115], %10 offset:200\n\t" \
    "s_waitcnt lgkmcnt(10)\n\t" \
    "v_mfma_f32_16x16x32_bf16 %4, v[100:103], v[132:135], %4\n\t" \
    "ds_read_b128 v[132:135], %9 offset:2368\n\t" \
    "v_mfma_f32_16x16x32_bf16 %5, v[100:103], v[136:139], %5\n\t" \
    "ds_read_b128 v[136:139], %9 offset:2880\n\t" \
    "v_mfma_f32_16x16x32_bf16 %6, v[100:103], v[140:143], %6\n\t" \
    "ds_read_b128 v[140:143], %9 offset:3392\n\t" \
    "s_waitcnt lgkmcnt(11)\n\t" \
    "v_mfma_f32_16x16x32_bf16 %7, v[100:103], v[144:147], %7\n\t" \
    "ds_read_b128 v[144:147], %9 offset:3904\n\t" \
    "v_mfma_f32_16x16x32_bf16 %7, v[104:107], v[148:151], %7\n\t" \
    "ds_read_b128 v[148:151], %11 offset:3776\n\t" \
    "s_waitcnt lgkmcnt(5)\n\t" \
    "v_mfma_f32_16x16x32_bf16 %0, v[108:111], v[116:119], %0\n\t" \
    "ds_read_b128 v[116:119], %9 offset:256\n\t" \
    "ds_read_b64 v[100:101], %8 offset:256\n\t" \
    "v_mfma_f32_16x16x32_bf16 %1, v[108:111], v[120:123], %1\n\t" \
    "ds_read_b128 v[120:123], %9 offset:768\n\t" \
    "ds_read_b64 v[102:103], %8 offset:264\n\t" \
    "v_mfma_f32_16x16x32_bf16 %2, v[108:111], v[124:127], %2\n\t" \
    "ds_read_b128 v[124:127], %9 offset:1280\n\t" \
    "ds_read_b64 v[104:105], %10 offset:256\n\t" \
    "v_mfma_f32_16x16x32_bf16 %3, v[108:111], v[128:131], %3\n\t" \
    "ds_read_b128 v[128:131], %9 offset:1792\n\t" \
    "ds_read_b64 v[106:107], %10 offset:264\n\t" \
    "s_waitcnt lgkmcnt(10)\n\t" \
    "v_mfma_f32_16x16x32_bf16 %4, v[108:111], v[132:135], %4\n\t" \
    "ds_read_b128 v[132:135], %9 offset:2304\n\t" \
    "v_mfma_f32_16x16x32_bf16 %5, v[108:111], v[136:139], %5\n\t" \
    "ds_read_b128 v[136:139], %9 offset:2816\n\t" \
    "v_mfma_f32_16x16x32_bf16 %6, v[108:111], v[140:143], %6\n\t" \
    "ds_read_b128 v[140:143], %9 offset:3328\n\t" \
    "s_waitcnt lgkmcnt(11)\n\t" \
    "v_mfma_f32_16x16x32_bf16 %7, v[108:111], v[144:147], %7\n\t" \
    "ds_read_b128 v[144:147], %9 offset:3840\n\t" \
    "v_mfma_f32_16x16x32_bf16 %7, v[112:115], v[148:151], %7\n\t" \
    "ds_read_b128 v[148:151], %11 offset:3840\n\t" \
    "s_waitcnt lgkmcnt(5)\n\t" \
    "v_mfma_f32_16x16x32_bf16 %0, v[100:103], v[116:119], %0\n\t" \
    "ds_read_b128 v[116:119], %9 offset:192\n\t" \
    "ds_read_b64 v[108:109], %8 offset:192\n\t" \
    "v_mfma_f32_16x16x32_bf16 %1, v[100:103], v[120:123], %1\n\t" \
    "ds_read_b128 v[120:123], %9 offset:704\n\t" \
    "ds_read_b64 v[110:111], %8 offset:200\n\t" \
    "v_mfma_f32_16x16x32_bf16 %2, v[100:103], v[124:127], %2\n\t" \
    "ds_read_b128 v[124:127], %9 offset:1216\n\t" \
    "ds_read_b64 v[112:113], %10 offset:320\n\t" \
    "v_mfma_f32_16x16x32_bf16 %3, v[100:103], v[128:131], %3\n\t" \
    "ds_read_b128 v[128:131], %9 offset:1728\n\t" \
    "ds_read_b64 v[114:115], %10 offset:328\n\t" \
    "s_waitcnt lgkmcnt(10)\n\t" \
    "v_mfma_f32_16x16x32_bf16 %4, v[100:103], v[132:135], %4\n\t" \
    "ds_read_b128 v[132:135], %9 offset:2240\n\t" \
    "v_mfma_f32_16x16x32_bf16 %5, v[100:103], v[136:139], %5\n\t" \
    "ds_read_b128 v[136:139], %9 offset:2752\n\t" \
    "v_mfma_f32_16x16x32_bf16 %6, v[100:103], v[140:143], %6\n\t" \
    "ds_read_b128 v[140:143], %9 offset:3264\n\t" \
    "s_waitcnt lgkmcnt(11)\n\t" \
    "v_mfma_f32_16x16x32_bf16 %7, v[100:103], v[144:147], %7\n\t" \
    "ds_read_b128 v[144:147], %9 offset:3776\n\t" \
    "v_mfma_f32_16x16x32_bf16 %7, v[104:107], v[148:151], %7\n\t" \
    "ds_read_b128 v[148:151], %11 offset:3904\n\t" \
    "s_waitcnt lgkmcnt(5)\n\t" \
    "v_mfma_f32_16x16x32_bf16 %0, v[108:111], v[116:119], %0\n\t" \
    "ds_read_b128 v[116:119], %9 offset:128\n\t" \
    "ds_read_b64 v[100:101], %8 offset:128\n\t" \
    "v_mfma_f32_16x16x32_bf16 %1, v[108:111], v[120:123], %1\n\t" \
    "ds_read_b128 v[120:123], %9 offset:640\n\t" \
    "ds_read_b64 v[102:103], %8 offset:136\n\t" \
    "v_mfma_f32_16x16x32_bf16 %2, v[108:111], v[124:127], %2\n\t" \
    "ds_read_b128 v[124:127], %9 offset:1152\n\t" \
    "ds_read_b64 v[104:105], %10 offset:384\n\t" \
    "v_mfma_f32_16x16x32_bf16 %3, v[108:111], v[128:131], %3\n\t" \
    "ds_read_b128 v[128:131], %9 offset:1664\n\t" \
    "ds_read_b64 v[106:107], %10 offset:392\n\t" \
    "s_waitcnt lgkmcnt(10)\n\t" \
    "v_mfma_f32_16x16x32_bf16 %4, v[108:111], v[132:135], %4\n\t" \
    "ds_read_b128 v[132:135], %9 offset:2176\n\t" \
    "v_mfma_f32_16x16x32_bf16 %5, v[108:111], v[136:139], %5\n\t" \
    "ds_read_b128 v[136:139], %9 offset:2688\n\t" \
    "v_mfma_f32_16x16x32_bf16 %6, v[108:111], v[140:143], %6\n\t" \
    "ds_read_b128 v[140:143], %9 offset:3200\n\t" \
    "s_waitcnt lgkmcnt(11)\n\t" \
    "v_mfma_f32_16x16x32_bf16 %7, v[108:111], v[144:147], %7\n\t" \
    "ds_read_b128 v[144:147], %9 offset:3712\n\t" \
    "v_mfma_f32_16x16x32_bf16 %7, v[112:115], v[148:151], %7\n\t" \
    "ds_read_b128 v[148:151], %11 offset:3968\n\t" \
    "s_waitcnt lgkmcnt(5)\n\t" \
    "v_mfma_f32_16x16x32_bf16 %0, v[100:103], v[116:119], %0\n\t" \
    "ds_read_b128 v[116:119], %9 offset:64\n\t" \
    "ds_read_b64 v[108:109], %8 offset:64\n\t" \
    "v_mfma_f32_16x16x32_bf16 %1, v[100:103], v[120:123], %1\n\t" \
    "ds_read_b128 v[120:123], %9 offset:576\n\t" \
    "ds_read_b64 v[110:111], %8 offset:72\n\t" \
    "v_mfma_f32_16x16x32_bf16 %2, v[100:103], v[124:127], %2\n\t" \
    "ds_read_b128 v[124:127], %9 offset:1088\n\t" \
    "ds_read_b64 v[112:113], %10 offset:448\n\t" \
    "v_mfma_f32_16x16x32_bf16 %3, v[100:103], v[128:131], %3\n\t" \
    "ds_read_b128 v[128:131], %9 offset:1600\n\t" \
    "ds_read_b64 v[114:115], %10 offset:456\n\t" \
    "s_waitcnt lgkmcnt(10)\n\t" \
    "v_mfma_f32_16x16x32_bf16 %4, v[100:103], v[132:135], %4\n\t" \
    "ds_read_b128 v[132:135], %9 offset:2112\n\t" \
    "v_mfma_f32_16x16x32_bf16 %5, v[100:103], v[136:139], %5\n\t" \
    "ds_read_b128 v[136:139], %9 offset:2624\n\t" \
    "v_mfma_f32_16x16x32_bf16 %6, v[100:103], v[140:143], %6\n\t" \
    "ds_read_b128 v[140:143], %9 offset:3136\n\t" \
    "s_waitcnt lgkmcnt(11)\n\t" \
    "v_mfma_f32_16x16x32_bf16 %7, v[100:103], v[144:147], %7\n\t" \
    "ds_read_b128 v[144:147], %9 offset:3648\n\t" \
    "v_mfma_f32_16x16x32_bf16 %7, v[104:107], v[148:151], %7\n\t" \
    "ds_read_b128 v[148:151], %11 offset:4032\n\t" \
    "s_waitcnt lgkmcnt(5)\n\t" \
    "v_mfma_f32_16x16x32_bf16 %0, v[108:111], v[116:119], %0\n\t" \
    "v_mfma_f32_16x16x32_bf16 %1, v[108:111], v[120:123], %1\n\t" \
    "v_mfma_f32_16x16x32_bf16 %2, v[108:111], v[124:127], %2\n\t" \
    "v_mfma_f32_16x16x32_bf16 %3, v[108:111], v[128:131], %3\n\t" \
    "s_waitcnt lgkmcnt(2)\n\t" \
    "v_mfma_f32_16x16x32_bf16 %4, v[108:111], v[132:135], %4\n\t" \
    "v_mfma_f32_16x16x32_bf16 %5, v[108:111], v[136:139], %5\n\t" \
    "v_mfma_f32_16x16x32_bf16 %6, v[108:111], v[140:143], %6\n\t" \
    "s_waitcnt lgkmcnt(0)\n\t" \
    "v_mfma_f32_16x16x32_bf16 %7, v[108:111], v[144:147], %7\n\t" \
    "v_mfma_f32_16x16x32_bf16 %7, v[112:115], v[148:151], %7\n\t" \
    "s_nop 7\n\t"
#define CONV_ASM_1 \
    "ds_read_b128 v[116:119], %9 offset:1024\n\t" \
    "ds_read_b64 v[100:101], %8 offset:512\n\t" \
    "ds_read_b128 v[120:123], %9 offset:1536\n\t" \
    "ds_read_b64 v[102:103], %8 offset:520\n\t" \
    "ds_read_b128 v[124:127], %9 offset:2048\n\t" \
    "ds_read_b64 v[104:105], %10 offset:0\n\t" \
    "ds_read_b128 v[128:131], %9 offset:2560\n\t" \
    "ds_read_b64 v[106:107], %10 offset:8\n\t" \
    "ds_read_b128 v[132:135], %9 offset:3072\n\t" \
    "ds_read_b128 v[136:139], %9 offset:3584\n\t" \
    "ds_read_b128 v[140:143], %9 offset:4096\n\t" \
    "ds_read_b128 v[144:147], %11 offset:3072\n\t" \
    "ds_read_b128 v[148:151], %11 offset:3584\n\t" \
    "s_waitcnt lgkmcnt(5)\n\t" \
    "v_mfma_f32_16x16x32_bf16 %1, v[100:103], v[116:119], %1\n\t" \
    "ds_read_b128 v[116:119], %9 offset:960\n\t" \
    "ds_read_b64 v[108:109], %8 offset:448\n\t" \
    "v_mfma_f32_16x16x32_bf16 %2, v[100:103], v[120:123], %2\n\t" \
    "ds_read_b128 v[120:123], %9 offset:1472\n\t" \
    "ds_read_b64 v[110:111], %8 offset:456\n\t" \
    "v_mfma_f32_16x16x32_bf16 %3, v[100:103], v[124:127], %3\n\t" \
    "ds_read_b128 v[124:127], %9 offset:1984\n\t" \
    "ds_read_b64 v[112:113], %10 offset:64\n\t" \
    "v_mfma_f32_16x16x32_bf16 %4, v[100:103], v[128:131], %4\n\t" \
    "ds_read_b128 v[128:131], %9 offset:2496\n\t" \
    "ds_read_b64 v[114:115], %10 offset:72\n\t" \
    "s_waitcnt lgkmcnt(10)\n\t" \
    "v_mfma_f32_16x16x32_bf16 %5, v[100:103], v[132:135], %5\n\t" \
    "ds_read_b128 v[132:135], %9 offset:3008\n\t" \
    "v_mfma_f32_16x16x32_bf16 %6, v[100:103], v[136:139], %6\n\t" \
    "ds_read_b128 v[136:139], %9 offset:3520\n\t" \
    "v_mfma_f32_16x16x32_bf16 %7, v[100:103], v[140:143], %7\n\t" \
    "ds_read_b128 v[140:143], %9 offset:4032\n\t" \
    "s_waitcnt lgkmcnt(11)\n\t" \
    "v_mfma_f32_16x16x32_bf16 %6, v[104:107], v[144:147], %6\n\t" \
    "ds_read_b128 v[144:147], %11 offset:3136\n\t" \
    "v_mfma_f32_16x16x32_bf16 %7, v[104:107], v[148:151], %7\n\t" \
    "ds_read_b128 v[148:151], %11 offset:3648\n\t" \
    "s_waitcnt lgkmcnt(5)\n\t" \
    "v_mfma_f32_16x16x32_bf16 %1, v[108:111], v[116:119], %1\n\t" \
    "ds_read_b128 v[116:119], %9 offset:896\n\t" \
    "ds_read_b64 v[100:101], %8 offset:384\n\t" \
    "v_mfma_f32_16x16x32_bf16 %2, v[108:111], v[120:123], %2\n\t" \
    "ds_read_b128 v[120:123], %9 offset:1408\n\t" \
    "ds_read_b64 v[102:103], %8 offset:392\n\t" \
    "v_mfma_f32_16x16x32_bf16 %3, v[108:111], v[124:127], %3\n\t" \
    "ds_read_b128 v[124:127], %9 offset:1920\n\t" \
    "ds_read_b64 v[104:105], %10 offset:128\n\t" \
    "v_mfma_f32_16x16x32_bf16 %4, v[108:111], v[128:131], %4\n\t" \
    "ds_read_b128 v[128:131], %9 offset:2432\n\t" \
    "ds_read_b64 v[106:107], %10 offset:136\n\t" \
    "s_waitcnt lgkmcnt(10)\n\t" \
    "v_mfma_f32_16x16x32_bf16 %5, v[108:111], v[132:135], %5\n\t" \
    "ds_read_b128 v[132:135], %9 offset:2944\n\t" \
    "v_mfma_f32_16x16x32_bf16 %6, v[108:111], v[136:139], %6\n\t" \
    "ds_read_b128 v[136:139], %9 offset:3456\n\t" \
    "v_mfma_f32_16x16x32_bf16 %7, v[108:111], v[140:143], %7\n\t" \
    "ds_read_b128 v[140:143], %9 offset:3968\n\t" \
    "s_waitcnt lgkmcnt(11)\n\t" \
    "v_mfma_f32_16x16x32_bf16 %6, v[112:115], v[144:147], %6\n\t" \
    "ds_read_b128 v[144:147], %11 offset:3200\n\t" \
    "v_mfma_f32_16x16x32_bf16 %7, v[112:115], v[148:151], %7\n\t" \
    "ds_read_b128 v[148:151], %11 offset:3712\n\t" \
    "s_waitcnt lgkmcnt(5)\n\t" \
    "v_mfma_f32_16x16x32_bf16 %1, v[100:103], v[116:119], %1\n\t" \
    "ds_read_b128 v[116:119], %9 offset:832\n\t" \
    "ds_read_b64 v[108:109], %8 offset:320\n\t" \
    "v_mfma_f32_16x16x32_bf16 %2, v[100:103], v[120:123], %2\n\t" \
    "ds_read_b128 v[120:123], %9 offset:1344\n\t" \
    "ds_read_b64 v[110:111], %8 offset:328\n\t" \
    "v_mfma_f32_16x16x32_bf16 %3, v[100:103], v[124:127], %3\n\t" \
    "ds_read_b128 v[124:127], %9 offset:1856\n\t" \
    "ds_read_b64 v[112:113], %10 offset:192\n\t" \
    "v_mfma_f32_16x16x32_bf16 %4, v[100:103], v[128:131], %4\n\t" \
    "ds_read_b128 v[128:131], %9 offset:2368\n\t" \
    "ds_read_b64 v[114:115], %10 offset:200\n\t" \
    "s_waitcnt lgkmcnt(10)\n\t" \
    "v_mfma_f32_16x16x32_bf16 %5, v[100:103], v[132:135], %5\n\t" \
    "ds_read_b128 v[132:135], %9 offset:2880\n\t" \
    "v_mfma_f32_16x16x32_bf16 %6, v[100:103], v[136:139], %6\n\t" \
    "ds_read_b128 v[136:139], %9 offset:3392\n\t" \
    "v_mfma_f32_16x16x32_bf16 %7, v[100:103], v[140:143], %7\n\t" \
    "ds_read_b128 v[140:143], %9 offset:3904\n\t" \
    "s_waitcnt lgkmcnt(11)\n\t" \
    "v_mfma_f32_16x16x32_bf16 %6, v[104:107], v[144:147], %6\n\t" \
    "ds_read_b128 v[144:147], %11 offset:3264\n\t" \
    "v_mfma_f32_16x16x32_bf16 %7, v[104:107], v[148:151], %7\n\t" \
    "ds_read_b128 v[148:151], %11 offset:3776\n\t" \
    "s_waitcnt lgkmcnt(5)\n\t" \
    "v_mfma_f32_16x16x32_bf16 %1, v[108:111], v[116:119], %1\n\t" \
    "ds_read_b128 v[116:119], %9 offset:768\n\t" \
    "ds_read_b64 v[100:101], %8 offset:256\n\t" \
    "v_mfma_f32_16x16x32_bf16 %2, v[108:111], v[120:123], %2\n\t" \
    "ds_read_b128 v[120:123], %9 offset:1280\n\t" \
    "ds_read_b64 v[102:103], %8 offset:264\n\t" \
    "v_mfma_f32_16x16x32_bf16 %3, v[108:111], v[124:127], %3\n\t" \
    "ds_read_b128 v[124:127], %9 offset:1792\n\t" \
    "ds_read_b64 v[104:105], %10 offset:256\n\t" \
    "v_mfma_f32_16x16x32_bf16 %4, v[108:111], v[128:131], %4\n\t" \
    "ds_read_b128 v[128:131], %9 offset:2304\n\t" \
    "ds_read_b64 v[106:107], %10 offset:264\n\t" \
    "s_waitcnt lgkmcnt(10)\n\t" \
    "v_mfma_f32_16x16x32_bf16 %5, v[108:111], v[132:135], %5\n\t" \
    "ds_read_b128 v[132:135], %9 offset:2816\n\t" \
    "v_mfma_f32_16x16x32_bf16 %6, v[108:111], v[136:139], %6\n\t" \
    "ds_read_b128 v[136:139], %9 offset:3328\n\t" \
    "v_mfma_f32_16x16x32_bf16 %7, v[108:111], v[140:143], %7\n\t" \
    "ds_read_b128 v[140:143], %9 offset:3840\n\t" \
    "s_waitcnt lgkmcnt(11)\n\t" \
    "v_mfma_f32_16x16x32_bf16 %6, v[112:115], v[144:147], %6\n\t" \
    "ds_read_b128 v[144:147], %11 offset:3328\n\t" \
    "v_mfma_f32_16x16x32_bf16 %7, v[112:115], v[148:151], %7\n\t" \
    "ds_read_b128 v[148:151], %11 offset:3840\n\t" \
    "s_waitcnt lgkmcnt(5)\n\t" \
    "v_mfma_f32_16x16x32_bf16 %1, v[100:103], v[116:119], %1\n\t" \
    "ds_read_b128 v[116:119], %9 offset:704\n\t" \
    "ds_read_b64 v[108:109], %8 offset:192\n\t" \
    "v_mfma_f32_16x16x32_bf16 %2, v[100:103], v[120:123], %2\n\t" \
    "ds_read_b128 v[120:123], %9 offset:1216\n\t" \
    "ds_read_b64 v[110:111], %8 offset:200\n\t" \
    "v_mfma_f32_16x16x32_bf16 %3, v[100:103], v[124:127], %3\n\t" \
    "ds_read_b128 v[124:127], %9 offset:1728\n\t" \
    "ds_read_b64 v[112:113], %10 offset:320\n\t" \
    "v_mfma_f32_16x16x32_bf16 %4, v[100:103], v[128:131], %4\n\t" \
    "ds_read_b128 v[128:131], %9 offset:2240\n\t" \
    "ds_read_b64 v[114:115], %10 offset:328\n\t" \
    "s_waitcnt lgkmcnt(10)\n\t" \
    "v_mfma_f32_16x16x32_bf16 %5, v[100:103], v[132:135], %5\n\t" \
    "ds_read_b128 v[132:135], %9 offset:2752\n\t" \
    "v_mfma_f32_16x16x32_bf16 %6, v[100:103], v[136:139], %6\n\t" \
    "ds_read_b128 v[136:139], %9 offset:3264\n\t" \
    "v_mfma_f32_16x16x32_bf16 %7, v[100:103], v[140:143], %7\n\t" \
    "ds_read_b128 v[140:143], %9 offset:3776\n\t" \
    "s_waitcnt lgkmcnt(11)\n\t" \
    "v_mfma_f32_16x16x32_bf16 %6, v[104:107], v[144:147], %6\n\t" \
    "ds_read_b128 v[144:147], %11 offset:3392\n\t" \
    "v_mfma_f32_16x16x32_bf16 %7, v[104:107], v[148:151], %7\n\t" \
    "ds_read_b128 v[148:151], %11 offset:3904\n\t" \
    "s_waitcnt lgkmcnt(5)\n\t" \
    "v_mfma_f32_16x16x32_bf16 %1, v[108:111], v[116:119], %1\n\t" \
    "ds_read_b128 v[116:119], %9 offset:640\n\t" \
    "ds_read_b64 v[100:101], %8 offset:128\n\t" \
    "v_mfma_f32_16x16x32_bf16 %2, v[108:111], v[120:123], %2\n\t" \
    "ds_read_b128 v[120:123], %9 offset:1152\n\t" \
    "ds_read_b64 v[102:103], %8 offset:136\n\t" \
    "v_mfma_f32_16x16x32_bf16 %3, v[108:111], v[124:127], %3\n\t" \
    "ds_read_b128 v[124:127], %9 offset:1664\n\t" \
    "ds_read_b64 v[104:105], %10 offset:384\n\t" \
    "v_mfma_f32_16x16x32_bf16 %4, v[108:111], v[128:131], %4\n\t" \
    "ds_read_b128 v[128:131], %9 offset:2176\n\t" \
    "ds_read_b64 v[106:107], %10 offset:392\n\t" \
    "s_waitcnt lgkmcnt(10)\n\t" \
    "v_mfma_f32_16x16x32_bf16 %5, v[108:111], v[132:135], %5\n\t" \
    "ds_read_b128 v[132:135], %9 offset:2688\n\t" \
    "v_mfma_f32_16x16x32_bf16 %6, v[108:111], v[136:139], %6\n\t" \
    "ds_read_b128 v[136:139], %9 offset:3200\n\t" \
    "v_mfma_f32_16x16x32_bf16 %7, v[108:111], v[140:143], %7\n\t" \
    "ds_read_b128 v[140:143], %9 offset:3712\n\t" \
    "s_waitcnt lgkmcnt(11)\n\t" \
    "v_mfma_f32_16x16x32_bf16 %6, v[112:115], v[144:147], %6\n\t" \
    "ds_read_b128 v[144:147], %11 offset:3456\n\t" \
    "v_mfma_f32_16x16x32_bf16 %7, v[112:115], v[148:151], %7\n\t" \
    "ds_read_b128 v[148:151], %11 offset:3968\n\t" \
    "s_waitcnt lgkmcnt(5)\n\t" \
    "v_mfma_f32_16x16x32_bf16 %1, v[100:103], v[116:119], %1\n\t" \
    "ds_read_b128 v[116:119], %9 offset:576\n\t" \
    "ds_read_b64 v[108:109], %8 offset:64\n\t" \
    "v_mfma_f32_16x16x32_bf16 %2, v[100:103], v[120:123], %2\n\t" \
    "ds_read_b128 v[120:123], %9 offset:1088\n\t" \
    "ds_read_b64 v[110:111], %8 offset:72\n\t" \
    "v_mfma_f32_16x16x32_bf16 %3, v[100:103], v[124:127], %3\n\t" \
    "ds_read_b128 v[124:127], %9 offset:1600\n\t" \
    "ds_read_b64 v[112:113], %10 offset:448\n\t" \
    "v_mfma_f32_16x16x32_bf16 %4, v[100:103], v[128:131], %4\n\t" \
    "ds_read_b128 v[128:131], %9 offset:2112\n\t" \
    "ds_read_b64 v[114:115], %10 offset:456\n\t" \
    "s_waitcnt lgkmcnt(10)\n\t" \
    "v_mfma_f32_16x16x32_bf16 %5, v[100:103], v[132:135], %5\n\t" \
    "ds_read_b128 v[132:135], %9 offset:2624\n\t" \
    "v_mfma_f32_16x16x32_bf16 %6, v[100:103], v[136:139], %6\n\t" \
    "ds_read_b128 v[136:139], %9 offset:3136\n\t" \
    "v_mfma_f32_16x16x32_bf16 %7, v[100:103], v[140:143], %7\n\t" \
    "ds_read_b128 v[140:143], %9 offset:3648\n\t" \
    "s_waitcnt lgkmcnt(11)\n\t" \
    "v_mfma_f32_16x16x32_bf16 %6, v[104:107], v[144:147], %6\n\t" \
    "ds_read_b128 v[144:147], %11 offset:3520\n\t" \
    "v_mfma_f32_16x16x32_bf16 %7, v[104:107], v[148:151], %7\n\t" \
    "ds_read_b128 v[148:151], %11 offset:4032\n\t" \
    "s_waitcnt lgkmcnt(5)\n\t" \
    "v_mfma_f32_16x16x32_bf16 %1, v[108:111], v[116:119], %1\n\t" \
    "v_mfma_f32_16x16x32_bf16 %2, v[108:111], v[120:123], %2\n\t" \
    "v_mfma_f32_16x16x32_bf16 %3, v[108:111], v[124:127], %3\n\t" \
    "v_mfma_f32_16x16x32_bf16 %4, v[108:111], v[128:131], %4\n\t" \
    "s_waitcnt lgkmcnt(2)\n\t" \
    "v_mfma_f32_16x16x32_bf16 %5, v[108:111], v[132:135], %5\n\t" \
    "v_mfma_f32_16x16x32_bf16 %6, v[108:111], v[136:139], %6\n\t" \
    "v_mfma_f32_16x16x32_bf16 %7, v[108:111], v[140:143], %7\n\t" \
    "s_waitcnt lgkmcnt(0)\n\t" \
    "v_mfma_f32_16x16x32_bf16 %6, v[112:115], v[144:147], %6\n\t" \
    "v_mfma_f32_16x16x32_bf16 %7, v[112:115], v[148:151], %7\n\t" \
    "s_nop 7\n\t"
#define CONV_ASM_2 \
    "ds_read_b128 v[116:119], %9 offset:1536\n\t" \
    "ds_read_b64 v[100:101], %8 offset:512\n\t" \
    "ds_read_b128 v[120:123], %9 offset:2048\n\t" \
    "ds_read_b64 v[102:103], %8 offset:520\n\t" \
    "ds_read_b128 v[124:127], %9 offset:2560\n\t" \
    "ds_read_b64 v[104:105], %10 offset:0\n\t" \
    "ds_read_b128 v[128:131], %9 offset:3072\n\t" \
    "ds_read_b64 v[106:107], %10 offset:8\n\t" \
    "ds_read_b128 v[132:135], %9 offset:3584\n\t" \
    "ds_read_b128 v[136:139], %9 offset:4096\n\t" \
    "ds_read_b128 v[140:143], %11 offset:2560\n\t" \
    "ds_read_b128 v[144:147], %11 offset:3072\n\t" \
    "ds_read_b128 v[148:151], %11 offset:3584\n\t" \
    "s_waitcnt lgkmcnt(5)\n\t" \
    "v_mfma_f32_16x16x32_bf16 %2, v[100:103], v[116:119], %2\n\t" \
    "ds_read_b128 v[116:119], %9 offset:1472\n\t" \
    "ds_read_b64 v[108:109], %8 offset:448\n\t" \
    "v_mfma_f32_16x16x32_bf16 %3, v[100:103], v[120:123], %3\n\t" \
    "ds_read_b128 v[120:123], %9 offset:1984\n\t" \
    "ds_read_b64 v[110:111], %8 offset:456\n\t" \
    "v_mfma_f32_16x16x32_bf16 %4, v[100:103], v[124:127], %4\n\t" \
    "ds_read_b128 v[124:127], %9 offset:2496\n\t" \
    "ds_read_b64 v[112:113], %10 offset:64\n\t" \
    "v_mfma_f32_16x16x32_bf16 %5, v[100:103], v[128:131], %5\n\t" \
    "ds_read_b128 v[128:131], %9 offset:3008\n\t" \
    "ds_read_b64 v[114:115], %10 offset:72\n\t" \
    "s_waitcnt lgkmcnt(10)\n\t" \
    "v_mfma_f32_16x16x32_bf16 %6, v[100:103], v[132:135], %6\n\t" \
    "ds_read_b128 v[132:135], %9 offset:3520\n\t" \
    "v_mfma_f32_16x16x32_bf16 %7, v[100:103], v[136:139], %7\n\t" \
    "ds_read_b128 v[136:139], %9 offset:4032\n\t" \
    "v_mfma_f32_16x16x32_bf16 %5, v[104:107], v[140:143], %5\n\t" \
    "ds_read_b128 v[140:143], %11 offset:2624\n\t" \
    "s_waitcnt lgkmcnt(11)\n\t" \
    "v_mfma_f32_16x16x32_bf16 %6, v[104:107], v[144:147], %6\n\t" \
    "ds_read_b128 v[144:147], %11 offset:3136\n\t" \
    "v_mfma_f32_16x16x32_bf16 %7, v[104:107], v[148:151], %7\n\t" \
    "ds_read_b128 v[148:151], %11 offset:3648\n\t" \
    "s_waitcnt lgkmcnt(5)\n\t" \
    "v_mfma_f32_16x16x32_bf16 %2, v[108:111], v[116:119], %2\n\t" \
    "ds_read_b128 v[116:119], %9 offset:1408\n\t" \
    "ds_read_b64 v[100:101], %8 offset:384\n\t" \
    "v_mfma_f32_16x16x32_bf16 %3, v[108:111], v[120:123], %3\n\t" \
    "ds_read_b128 v[120:123], %9 offset:1920\n\t" \
    "ds_read_b64 v[102:103], %8 offset:392\n\t" \
    "v_mfma_f32_16x16x32_bf16 %4, v[108:111], v[124:127], %4\n\t" \
    "ds_read_b128 v[124:127], %9 offset:2432\n\t" \
    "ds_read_b64 v[104:105], %10 offset:128\n\t" \
    "v_mfma_f32_16x16x32_bf16 %5, v[108:111], v[128:131], %5\n\t" \
    "ds_read_b128 v[128:131], %9 offset:2944\n\t" \
    "ds_read_b64 v[106:107], %10 offset:136\n\t" \
    "s_waitcnt lgkmcnt(10)\n\t" \
    "v_mfma_f32_16x16x32_bf16 %6, v[108:111], v[132:135], %6\n\t" \
    "ds_read_b128 v[132:135], %9 offset:3456\n\t" \
    "v_mfma_f32_16x16x32_bf16 %7, v[108:111], v[136:139], %7\n\t" \
    "ds_read_b128 v[136:139], %9 offset:3968\n\t" \
    "v_mfma_f32_16x16x32_bf16 %5, v[112:115], v[140:143], %5\n\t" \
    "ds_read_b128 v[140:143], %11 offset:2688\n\t" \
    "s_waitcnt lgkmcnt(11)\n\t" \
    "v_mfma_f32_16x16x32_bf16 %6, v[112:115], v[144:147], %6\n\t" \
    "ds_read_b128 v[144:147], %11 offset:3200\n\t" \
    "v_mfma_f32_16x16x32_bf16 %7, v[112:115], v[148:151], %7\n\t" \
    "ds_read_b128 v[148:151], %11 offset:3712\n\t" \
    "s_waitcnt lgkmcnt(5)\n\t" \
    "v_mfma_f32_16x16x32_bf16 %2, v[100:103], v[116:119], %2\n\t" \
    "ds_read_b128 v[116:119], %9 offset:1344\n\t" \
    "ds_read_b64 v[108:109], %8 offset:320\n\t" \
    "v_mfma_f32_16x16x32_bf16 %3, v[100:103], v[120:123], %3\n\t" \
    "ds_read_b128 v[120:123], %9 offset:1856\n\t" \
    "ds_read_b64 v[110:111], %8 offset:328\n\t" \
    "v_mfma_f32_16x16x32_bf16 %4, v[100:103], v[124:127], %4\n\t" \
    "ds_read_b128 v[124:127], %9 offset:2368\n\t" \
    "ds_read_b64 v[112:113], %10 offset:192\n\t" \
    "v_mfma_f32_16x16x32_bf16 %5, v[100:103], v[128:131], %5\n\t" \
    "ds_read_b128 v[128:131], %9 offset:2880\n\t" \
    "ds_read_b64 v[114:115], %10 offset:200\n\t" \
    "s_waitcnt lgkmcnt(10)\n\t" \
    "v_mfma_f32_16x16x32_bf16 %6, v[100:103], v[132:135], %6\n\t" \
    "ds_read_b128 v[132:135], %9 offset:3392\n\t" \
    "v_mfma_f32_16x16x32_bf16 %7, v[100:103], v[136:139], %7\n\t" \
    "ds_read_b128 v[136:139], %9 offset:3904\n\t" \
    "v_mfma_f32_16x16x32_bf16 %5, v[104:107], v[140:143], %5\n\t" \
    "ds_read_b128 v[140:143], %11 offset:2752\n\t" \
    "s_waitcnt lgkmcnt(11)\n\t" \
    "v_mfma_f32_16x16x32_bf16 %6, v[104:107], v[144:147], %6\n\t" \
    "ds_read_b128 v[144:147], %11 offset:3264\n\t" \
    "v_mfma_f32_16x16x32_bf16 %7, v[104:107], v[148:151], %7\n\t" \
    "ds_read_b128 v[148:151], %11 offset:3776\n\t" \
    "s_waitcnt lgkmcnt(5)\n\t" \
    "v_mfma_f32_16x16x32_bf16 %2, v[108:111], v[116:119], %2\n\t" \
    "ds_read_b128 v[116:119], %9 offset:1280\n\t" \
    "ds_read_b64 v[100:101], %8 offset:256\n\t" \
    "v_mfma_f32_16x16x32_bf16 %3, v[108:111], v[120:123], %3\n\t" \
    "ds_read_b128 v[120:123], %9 offset:1792\n\t" \
    "ds_read_b64 v[102:103], %8 offset:264\n\t" \
    "v_mfma_f32_16x16x32_bf16 %4, v[108:111], v[124:127], %4\n\t" \
    "ds_read_b128 v[124:127], %9 offset:2304\n\t" \
    "ds_read_b64 v[104:105], %10 offset:256\n\t" \
    "v_mfma_f32_16x16x32_bf16 %5, v[108:111], v[128:131], %5\n\t" \
    "ds_read_b128 v[128:131], %9 offset:2816\n\t" \
    "ds_read_b64 v[106:107], %10 offset:264\n\t" \
    "s_waitcnt lgkmcnt(10)\n\t" \
    "v_mfma_f32_16x16x32_bf16 %6, v[108:111], v[132:135], %6\n\t" \
    "ds_read_b128 v[132:135], %9 offset:3328\n\t" \
    "v_mfma_f32_16x16x32_bf16 %7, v[108:111], v[136:139], %7\n\t" \
    "ds_read_b128 v[136:139], %9 offset:3840\n\t" \
    "v_mfma_f32_16x16x32_bf16 %5, v[112:115], v[140:143], %5\n\t" \
    "ds_read_b128 v[140:143], %11 offset:2816\n\t" \
    "s_waitcnt lgkmcnt(11)\n\t" \
    "v_mfma_f32_16x16x32_bf16 %6, v[112:115], v[144:147], %6\n\t" \
    "ds_read_b128 v[144:147], %11 offset:3328\n\t" \
    "v_mfma_f32_16x16x32_bf16 %7, v[112:115], v[148:151], %7\n\t" \
    "ds_read_b128 v[148:151], %11 offset:3840\n\t" \
    "s_waitcnt lgkmcnt(5)\n\t" \
    "v_mfma_f32_16x16x32_bf16 %2, v[100:103], v[116:119], %2\n\t" \
    "ds_read_b128 v[116:119], %9 offset:1216\n\t" \
    "ds_read_b64 v[108:109], %8 offset:192\n\t" \
    "v_mfma_f32_16x16x32_bf16 %3, v[100:103], v[120:123], %3\n\t" \
    "ds_read_b128 v[120:123], %9 offset:1728\n\t" \
    "ds_read_b64 v[110:111], %8 offset:200\n\t" \
    "v_mfma_f32_16x16x32_bf16 %4, v[100:103], v[124:127], %4\n\t" \
    "ds_read_b128 v[124:127], %9 offset:2240\n\t" \
    "ds_read_b64 v[112:113], %10 offset:320\n\t" \
    "v_mfma_f32_16x16x32_bf16 %5, v[100:103], v[128:131], %5\n\t" \
    "ds_read_b128 v[128:131], %9 offset:2752\n\t" \
    "ds_read_b64 v[114:115], %10 offset:328\n\t" \
    "s_waitcnt lgkmcnt(10)\n\t" \
    "v_mfma_f32_16x16x32_bf16 %6, v[100:103], v[132:135], %6\n\t" \
    "ds_read_b128 v[132:135], %9 offset:3264\n\t" \
    "v_mfma_f32_16x16x32_bf16 %7, v[100:103], v[136:139], %7\n\t" \
    "ds_read_b128 v[136:139], %9 offset:3776\n\t" \
    "v_mfma_f32_16x16x32_bf16 %5, v[104:107], v[140:143], %5\n\t" \
    "ds_read_b128 v[140:143], %11 offset:2880\n\t" \
    "s_waitcnt lgkmcnt(11)\n\t" \
    "v_mfma_f32_16x16x32_bf16 %6, v[104:107], v[144:147], %6\n\t" \
    "ds_read_b128 v[144:147], %11 offset:3392\n\t" \
    "v_mfma_f32_16x16x32_bf16 %7, v[104:107], v[148:151], %7\n\t" \
    "ds_read_b128 v[148:151], %11 offset:3904\n\t" \
    "s_waitcnt lgkmcnt(5)\n\t" \
    "v_mfma_f32_16x16x32_bf16 %2, v[108:111], v[116:119], %2\n\t" \
    "ds_read_b128 v[116:119], %9 offset:1152\n\t" \
    "ds_read_b64 v[100:101], %8 offset:128\n\t" \
    "v_mfma_f32_16x16x32_bf16 %3, v[108:111], v[120:123], %3\n\t" \
    "ds_read_b128 v[120:123], %9 offset:1664\n\t" \
    "ds_read_b64 v[102:103], %8 offset:136\n\t" \
    "v_mfma_f32_16x16x32_bf16 %4, v[108:111], v[124:127], %4\n\t" \
    "ds_read_b128 v[124:127], %9 offset:2176\n\t" \
    "ds_read_b64 v[104:105], %10 offset:384\n\t" \
    "v_mfma_f32_16x16x32_bf16 %5, v[108:111], v[128:131], %5\n\t" \
    "ds_read_b128 v[128:131], %9 offset:2688\n\t" \
    "ds_read_b64 v[106:107], %10 offset:392\n\t" \
    "s_waitcnt lgkmcnt(10)\n\t" \
    "v_mfma_f32_16x16x32_bf16 %6, v[108:111], v[132:135], %6\n\t" \
    "ds_read_b128 v[132:135], %9 offset:3200\n\t" \
    "v_mfma_f32_16x16x32_bf16 %7, v[108:111], v[136:139], %7\n\t" \
    "ds_read_b128 v[136:139], %9 offset:3712\n\t" \
    "v_mfma_f32_16x16x32_bf16 %5, v[112:115], v[140:143], %5\n\t" \
    "ds_read_b128 v[140:143], %11 offset:2944\n\t" \
    "s_waitcnt lgkmcnt(11)\n\t" \
    "v_mfma_f32_16x16x32_bf16 %6, v[112:115], v[144:147], %6\n\t" \
    "ds_read_b128 v[144:147], %11 offset:3456\n\t" \
    "v_mfma_f32_16x16x32_bf16 %7, v[112:115], v[148:151], %7\n\t" \
    "ds_read_b128 v[148:151], %11 offset:3968\n\t" \
    "s_waitcnt lgkmcnt(5)\n\t" \
    "v_mfma_f32_16x16x32_bf16 %2, v[100:103], v[116:119], %2\n\t" \
    "ds_read_b128 v[116:119], %9 offset:1088\n\t" \
    "ds_read_b64 v[108:109], %8 offset:64\n\t" \
    "v_mfma_f32_16x16x32_bf16 %3, v[100:103], v[120:123], %3\n\t" \
    "ds_read_b128 v[120:123], %9 offset:1600\n\t" \
    "ds_read_b64 v[110:111], %8 offset:72\n\t" \
    "v_mfma_f32_16x16x32_bf16 %4, v[100:103], v[124:127], %4\n\t" \
    "ds_read_b128 v[124:127], %9 offset:2112\n\t" \
    "ds_read_b64 v[112:113], %10 offset:448\n\t" \
    "v_mfma_f32_16x16x32_bf16 %5, v[100:103], v[128:131], %5\n\t" \
    "ds_read_b128 v[128:131], %9 offset:2624\n\t" \
    "ds_read_b64 v[114:115], %10 offset:456\n\t" \
    "s_waitcnt lgkmcnt(10)\n\t" \
    "v_mfma_f32_16x16x32_bf16 %6, v[100:103], v[132:135], %6\n\t" \
    "ds_read_b128 v[132:135], %9 offset:3136\n\t" \
    "v_mfma_f32_16x16x32_bf16 %7, v[100:103], v[136:139], %7\n\t" \
    "ds_read_b128 v[136:139], %9 offset:3648\n\t" \
    "v_mfma_f32_16x16x32_bf16 %5, v[104:107], v[140:143], %5\n\t" \
    "ds_read_b128 v[140:143], %11 offset:3008\n\t" \
    "s_waitcnt lgkmcnt(11)\n\t" \
    "v_mfma_f32_16x16x32_bf16 %6, v[104:107], v[144:147], %6\n\t" \
    "ds_read_b128 v[144:147], %11 offset:3520\n\t" \
    "v_mfma_f32_16x16x32_bf16 %7, v[104:107], v[148:151], %7\n\t" \
    "ds_read_b128 v[148:151], %11 offset:4032\n\t" \
    "s_waitcnt lgkmcnt(5)\n\t" \
    "v_mfma_f32_16x16x32_bf16 %2, v[108:111], v[116:119], %2\n\t" \
    "v_mfma_f32_16x16x32_bf16 %3, v[108:111], v[120:123], %3\n\t" \
    "v_mfma_f32_16x16x32_bf16 %4, v[108:111], v[124:127], %4\n\t" \
    "v_mfma_f32_16x16x32_bf16 %5, v[108:111], v[128:131], %5\n\t" \
    "s_waitcnt lgkmcnt(2)\n\t" \
    "v_mfma_f32_16x16x32_bf16 %6, v[108:111], v[132:135], %6\n\t" \
    "v_mfma_f32_16x16x32_bf16 %7, v[108:111], v[136:139], %7\n\t" \
    "v_mfma_f32_16x16x32_bf16 %5, v[112:115], v[140:143], %5\n\t" \
    "s_waitcnt lgkmcnt(0)\n\t" \
    "v_mfma_f32_16x16x32_bf16 %6, v[112:115], v[144:147], %6\n\t" \
    "v_mfma_f32_16x16x32_bf16 %7, v[112:115], v[148:151], %7\n\t" \
    "s_nop 7\n\t"
#define CONV_ASM_3 \
    "ds_read_b128 v[116:119], %9 offset:2048\n\t" \
    "ds_read_b64 v[100:101], %8 offset:512\n\t" \
    "ds_read_b128 v[120:123], %9 offset:2560\n\t" \
    "ds_read_b64 v[102:103], %8 offset:520\n\t" \
    "ds_read_b128 v[124:127], %9 offset:3072\n\t" \
    "ds_read_b64 v[104:105], %10 offset:0\n\t" \
    "ds_read_b128 v[128:131], %9 offset:3584\n\t" \
    "ds_read_b64 v[106:107], %10 offset:8\n\t" \
    "ds_read_b128 v[132:135], %9 offset:4096\n\t" \
    "ds_read_b128 v[136:139], %11 offset:2048\n\t" \
    "ds_read_b128 v[140:143], %11 offset:2560\n\t" \
    "ds_read_b128 v[144:147], %11 offset:3072\n\t" \
    "ds_read_b128 v[148:151], %11 offset:3584\n\t" \
    "s_waitcnt lgkmcnt(5)\n\t" \
    "v_mfma_f32_16x16x32_bf16 %3, v[100:103], v[116:119], %3\n\t" \
    "ds_read_b128 v[116:119], %9 offset:1984\n\t" \
    "ds_read_b64 v[108:109], %8 offset:448\n\t" \
    "v_mfma_f32_16x16x32_bf16 %4, v[100:103], v[120:123], %4\n\t" \
    "ds_read_b128 v[120:123], %9 offset:2496\n\t" \
    "ds_read_b64 v[110:111], %8 offset:456\n\t" \
    "v_mfma_f32_16x16x32_bf16 %5, v[100:103], v[124:127], %5\n\t" \
    "ds_read_b128 v[124:127], %9 offset:3008\n\t" \
    "ds_read_b64 v[112:113], %10 offset:64\n\t" \
    "v_mfma_f32_16x16x32_bf16 %6, v[100:103], v[128:131], %6\n\t" \
    "ds_read_b128 v[128:131], %9 offset:3520\n\t" \
    "ds_read_b64 v[114:115], %10 offset:72\n\t" \
    "s_waitcnt lgkmcnt(10)\n\t" \
    "v_mfma_f32_16x16x32_bf16 %7, v[100:103], v[132:135], %7\n\t" \
    "ds_read_b128 v[132:135], %9 offset:4032\n\t" \
    "v_mfma_f32_16x16x32_bf16 %4, v[104:107], v[136:139], %4\n\t" \
    "ds_read_b128 v[136:139], %11 offset:2112\n\t" \
    "v_mfma_f32_16x16x32_bf16 %5, v[104:107], v[140:143], %5\n\t" \
    "ds_read_b128 v[140:143], %11 offset:2624\n\t" \
    "s_waitcnt lgkmcnt(11)\n\t" \
    "v_mfma_f32_16x16x32_bf16 %6, v[104:107], v[144:147], %6\n\t" \
    "ds_read_b128 v[144:147], %11 offset:3136\n\t" \
    "v_mfma_f32_16x16x32_bf16 %7, v[104:107], v[148:151], %7\n\t" \
    "ds_read_b128 v[148:151], %11 offset:3648\n\t" \
    "s_waitcnt lgkmcnt(5)\n\t" \
    "v_mfma_f32_16x16x32_bf16 %3, v[108:111], v[116:119], %3\n\t" \
    "ds_read_b128 v[116:119], %9 offset:1920\n\t" \
    "ds_read_b64 v[100:101], %8 offset:384\n\t" \
    "v_mfma_f32_16x16x32_bf16 %4, v[108:111], v[120:123], %4\n\t" \
    "ds_read_b128 v[120:123], %9 offset:2432\n\t" \
    "ds_read_b64 v[102:103], %8 offset:392\n\t" \
    "v_mfma_f32_16x16x32_bf16 %5, v[108:111], v[124:127], %5\n\t" \
    "ds_read_b128 v[124:127], %9 offset:2944\n\t" \
    "ds_read_b64 v[104:105], %10 offset:128\n\t" \
    "v_mfma_f32_16x16x32_bf16 %6, v[108:111], v[128:131], %6\n\t" \
    "ds_read_b128 v[128:131], %9 offset:3456\n\t" \
    "ds_read_b64 v[106:107], %10 offset:136\n\t" \
    "s_waitcnt lgkmcnt(10)\n\t" \
    "v_mfma_f32_16x16x32_bf16 %7, v[108:111], v[132:135], %7\n\t" \
    "ds_read_b128 v[132:135], %9 offset:3968\n\t" \
    "v_mfma_f32_16x16x32_bf16 %4, v[112:115], v[136:139], %4\n\t" \
    "ds_read_b128 v[136:139], %11 offset:2176\n\t" \
    "v_mfma_f32_16x16x32_bf16 %5, v[112:115], v[140:143], %5\n\t" \
    "ds_read_b128 v[140:143], %11 offset:2688\n\t" \
    "s_waitcnt lgkmcnt(11)\n\t" \
    "v_mfma_f32_16x16x32_bf16 %6, v[112:115], v[144:147], %6\n\t" \
    "ds_read_b128 v[144:147], %11 offset:3200\n\t" \
    "v_mfma_f32_16x16x32_bf16 %7, v[112:115], v[148:151], %7\n\t" \
    "ds_read_b128 v[148:151], %11 offset:3712\n\t" \
    "s_waitcnt lgkmcnt(5)\n\t" \
    "v_mfma_f32_16x16x32_bf16 %3, v[100:103], v[116:119], %3\n\t" \
    "ds_read_b128 v[116:119], %9 offset:1856\n\t" \
    "ds_read_b64 v[108:109], %8 offset:320\n\t" \
    "v_mfma_f32_16x16x32_bf16 %4, v[100:103], v[120:123], %4\n\t" \
    "ds_read_b128 v[120:123], %9 offset:2368\n\t" \
    "ds_read_b64 v[110:111], %8 offset:328\n\t" \
    "v_mfma_f32_16x16x32_bf16 %5, v[100:103], v[124:127], %5\n\t" \
    "ds_read_b128 v[124:127], %9 offset:2880\n\t" \
    "ds_read_b64 v[112:113], %10 offset:192\n\t" \
    "v_mfma_f32_16x16x32_bf16 %6, v[100:103], v[128:131], %6\n\t" \
    "ds_read_b128 v[128:131], %9 offset:3392\n\t" \
    "ds_read_b64 v[114:115], %10 offset:200\n\t" \
    "s_waitcnt lgkmcnt(10)\n\t" \
    "v_mfma_f32_16x16x32_bf16 %7, v[100:103], v[132:135], %7\n\t" \
    "ds_read_b128 v[132:135], %9 offset:3904\n\t" \
    "v_mfma_f32_16x16x32_bf16 %4, v[104:107], v[136:139], %4\n\t" \
    "ds_read_b128 v[136:139], %11 offset:2240\n\t" \
    "v_mfma_f32_16x16x32_bf16 %5, v[104:107], v[140:143], %5\n\t" \
    "ds_read_b128 v[140:143], %11 offset:2752\n\t" \
    "s_waitcnt lgkmcnt(11)\n\t" \
    "v_mfma_f32_16x16x32_bf16 %6, v[104:107], v[144:147], %6\n\t" \
    "ds_read_b128 v[144:147], %11 offset:3264\n\t" \
    "v_mfma_f32_16x16x32_bf16 %7, v[104:107], v[148:151], %7\n\t" \
    "ds_read_b128 v[148:151], %11 offset:3776\n\t" \
    "s_waitcnt lgkmcnt(5)\n\t" \
    "v_mfma_f32_16x16x32_bf16 %3, v[108:111], v[116:119], %3\n\t" \
    "ds_read_b128 v[116:119], %9 offset:1792\n\t" \
    "ds_read_b64 v[100:101], %8 offset:256\n\t" \
    "v_mfma_f32_16x16x32_bf16 %4, v[108:111], v[120:123], %4\n\t" \
    "ds_read_b128 v[120:123], %9 offset:2304\n\t" \
    "ds_read_b64 v[102:103], %8 offset:264\n\t" \
    "v_mfma_f32_16x16x32_bf16 %5, v[108:111], v[124:127], %5\n\t" \
    "ds_read_b128 v[124:127], %9 offset:2816\n\t" \
    "ds_read_b64 v[104:105], %10 offset:256\n\t" \
    "v_mfma_f32_16x16x32_bf16 %6, v[108:111], v[128:131], %6\n\t" \
    "ds_read_b128 v[128:131], %9 offset:3328\n\t" \
    "ds_read_b64 v[106:107], %10 offset:264\n\t" \
    "s_waitcnt lgkmcnt(10)\n\t" \
    "v_mfma_f32_16x16x32_bf16 %7, v[108:111], v[132:135], %7\n\t" \
    "ds_read_b128 v[132:135], %9 offset:3840\n\t" \
    "v_mfma_f32_16x16x32_bf16 %4, v[112:115], v[136:139], %4\n\t" \
    "ds_read_b128 v[136:139], %11 offset:2304\n\t" \
    "v_mfma_f32_16x16x32_bf16 %5, v[112:115], v[140:143], %5\n\t" \
    "ds_read_b128 v[140:143], %11 offset:2816\n\t" \
    "s_waitcnt lgkmcnt(11)\n\t" \
    "v_mfma_f32_16x16x32_bf16 %6, v[112:115], v[144:147], %6\n\t" \
    "ds_read_b128 v[144:147], %11 offset:3328\n\t" \
    "v_mfma_f32_16x16x32_bf16 %7, v[112:115], v[148:151], %7\n\t" \
    "ds_read_b128 v[148:151], %11 offset:3840\n\t" \
    "s_waitcnt lgkmcnt(5)\n\t" \
    "v_mfma_f32_16x16x32_bf16 %3, v[100:103], v[116:119], %3\n\t" \
    "ds_read_b128 v[116:119], %9 offset:1728\n\t" \
    "ds_read_b64 v[108:109], %8 offset:192\n\t" \
    "v_mfma_f32_16x16x32_bf16 %4, v[100:103], v[120:123], %4\n\t" \
    "ds_read_b128 v[120:123], %9 offset:2240\n\t" \
    "ds_read_b64 v[110:111], %8 offset:200\n\t" \
    "v_mfma_f32_16x16x32_bf16 %5, v[100:103], v[124:127], %5\n\t" \
    "ds_read_b128 v[124:127], %9 offset:2752\n\t" \
    "ds_read_b64 v[112:113], %10 offset:320\n\t" \
    "v_mfma_f32_16x16x32_bf16 %6, v[100:103], v[128:131], %6\n\t" \
    "ds_read_b128 v[128:131], %9 offset:3264\n\t" \
    "ds_read_b64 v[114:115], %10 offset:328\n\t" \
    "s_waitcnt lgkmcnt(10)\n\t" \
    "v_mfma_f32_16x16x32_bf16 %7, v[100:103], v[132:135], %7\n\t" \
    "ds_read_b128 v[132:135], %9 offset:3776\n\t" \
    "v_mfma_f32_16x16x32_bf16 %4, v[104:107], v[136:139], %4\n\t" \
    "ds_read_b128 v[136:139], %11 offset:2368\n\t" \
    "v_mfma_f32_16x16x32_bf16 %5, v[104:107], v[140:143], %5\n\t" \
    "ds_read_b128 v[140:143], %11 offset:2880\n\t" \
    "s_waitcnt lgkmcnt(11)\n\t" \
    "v_mfma_f32_16x16x32_bf16 %6, v[104:107], v[144:147], %6\n\t" \
    "ds_read_b128 v[144:147], %11 offset:3392\n\t" \
    "v_mfma_f32_16x16x32_bf16 %7, v[104:107], v[148:151], %7\n\t" \
    "ds_read_b128 v[148:151], %11 offset:3904\n\t" \
    "s_waitcnt lgkmcnt(5)\n\t" \
    "v_mfma_f32_16x16x32_bf16 %3, v[108:111], v[116:119], %3\n\t" \
    "ds_read_b128 v[116:119], %9 offset:1664\n\t" \
    "ds_read_b64 v[100:101], %8 offset:128\n\t" \
    "v_mfma_f32_16x16x32_bf16 %4, v[108:111], v[120:123], %4\n\t" \
    "ds_read_b128 v[120:123], %9 offset:2176\n\t" \
    "ds_read_b64 v[102:103], %8 offset:136\n\t" \
    "v_mfma_f32_16x16x32_bf16 %5, v[108:111], v[124:127], %5\n\t" \
    "ds_read_b128 v[124:127], %9 offset:2688\n\t" \
    "ds_read_b64 v[104:105], %10 offset:384\n\t" \
    "v_mfma_f32_16x16x32_bf16 %6, v[108:111], v[128:131], %6\n\t" \
    "ds_read_b128 v[128:131], %9 offset:3200\n\t" \
    "ds_read_b64 v[106:107], %10 offset:392\n\t" \
    "s_waitcnt lgkmcnt(10)\n\t" \
    "v_mfma_f32_16x16x32_bf16 %7, v[108:111], v[132:135], %7\n\t" \
    "ds_read_b128 v[132:135], %9 offset:3712\n\t" \
    "v_mfma_f32_16x16x32_bf16 %4, v[112:115], v[136:139], %4\n\t" \
    "ds_read_b128 v[136:139], %11 offset:2432\n\t" \
    "v_mfma_f32_16x16x32_bf16 %5, v[112:115], v[140:143], %5\n\t" \
    "ds_read_b128 v[140:143], %11 offset:2944\n\t" \
    "s_waitcnt lgkmcnt(11)\n\t" \
    "v_mfma_f32_16x16x32_bf16 %6, v[112:115], v[144:147], %6\n\t" \
    "ds_read_b128 v[144:147], %11 offset:3456\n\t" \
    "v_mfma_f32_16x16x32_bf16 %7, v[112:115], v[148:151], %7\n\t" \
    "ds_read_b128 v[148:151], %11 offset:3968\n\t" \
    "s_waitcnt lgkmcnt(5)\n\t" \
    "v_mfma_f32_16x16x32_bf16 %3, v[100:103], v[116:119], %3\n\t" \
    "ds_read_b128 v[116:119], %9 offset:1600\n\t" \
    "ds_read_b64 v[108:109], %8 offset:64\n\t" \
    "v_mfma_f32_16x16x32_bf16 %4, v[100:103], v[120:123], %4\n\t" \
    "ds_read_b128 v[120:123], %9 offset:2112\n\t" \
    "ds_read_b64 v[110:111], %8 offset:72\n\t" \
    "v_mfma_f32_16x16x32_bf16 %5, v[100:103], v[124:127], %5\n\t" \
    "ds_read_b128 v[124:127], %9 offset:2624\n\t" \
    "ds_read_b64 v[112:113], %10 offset:448\n\t" \
    "v_mfma_f32_16x16x32_bf16 %6, v[100:103], v[128:131], %6\n\t" \
    "ds_read_b128 v[128:131], %9 offset:3136\n\t" \
    "ds_read_b64 v[114:115], %10 offset:456\n\t" \
    "s_waitcnt lgkmcnt(10)\n\t" \
    "v_mfma_f32_16x16x32_bf16 %7, v[100:103], v[132:135], %7\n\t" \
    "ds_read_b128 v[132:135], %9 offset:3648\n\t" \
    "v_mfma_f32_16x16x32_bf16 %4, v[104:107], v[136:139], %4\n\t" \
    "ds_read_b128 v[136:139], %11 offset:2496\n\t" \
    "v_mfma_f32_16x16x32_bf16 %5, v[104:107], v[140:143], %5\n\t" \
    "ds_read_b128 v[140:143], %11 offset:3008\n\t" \
    "s_waitcnt lgkmcnt(11)\n\t" \
    "v_mfma_f32_16x16x32_bf16 %6, v[104:107], v[144:147], %6\n\t" \
    "ds_read_b128 v[144:147], %11 offset:3520\n\t" \
    "v_mfma_f32_16x16x32_bf16 %7, v[104:107], v[148:151], %7\n\t" \
    "ds_read_b128 v[148:151], %11 offset:4032\n\t" \
    "s_waitcnt lgkmcnt(5)\n\t" \
    "v_mfma_f32_16x16x32_bf16 %3, v[108:111], v[116:119], %3\n\t" \
    "v_mfma_f32_16x16x32_bf16 %4, v[108:111], v[120:123], %4\n\t" \
    "v_mfma_f32_16x16x32_bf16 %5, v[108:111], v[124:127], %5\n\t" \
    "v_mfma_f32_16x16x32_bf16 %6, v[108:111], v[128:131], %6\n\t" \
    "s_waitcnt lgkmcnt(2)\n\t" \
    "v_mfma_f32_16x16x32_bf16 %7, v[108:111], v[132:135], %7\n\t" \
    "v_mfma_f32_16x16x32_bf16 %4, v[112:115], v[136:139], %4\n\t" \
    "v_mfma_f32_16x16x32_bf16 %5, v[112:115], v[140:143], %5\n\t" \
    "s_waitcnt lgkmcnt(0)\n\t" \
    "v_mfma_f32_16x16x32_bf16 %6, v[112:115], v[144:147], %6\n\t" \
    "v_mfma_f32_16x16x32_bf16 %7, v[112:115], v[148:151], %7\n\t" \
    "s_nop 7\n\t"
#define CONV_CLOBBERS "v100", "v101", "v102", "v103", "v104", "v105", "v106", "v107", "v108", "v109", "v110", "v111", "v112", "v113", "v114", "v115", "v116", "v117", "v118", "v119", "v120", "v121", "v122", "v123", "v124", "v125", "v126", "v127", "v128", "v129", "v130", "v131", "v132", "v133", "v134", "v135", "v136", "v137", "v138", "v139", "v140", "v141", "v142", "v143", "v144", "v145", "v146", "v147", "v148", "v149", "v150", "v151"
template <int G> DI void conv_group(lds_p L, lds_p R, f32x4 (&acc)[8], int pa0, int pb0) {
    if ((unsigned)__builtin_amdgcn_readfirstlane(peek(L, C_Q)) >= 8u) __builtin_amdgcn_s_setprio(1);
    const unsigned r0 = (unsigned)(uintptr_t)R;
    const unsigned a1 = r0 + pa0 - 64 * (8 * G + 8), b1 = r0 + pb0 - 64 * (8 * G + 8), a2 = r0 + pa0 - 64 * (63 - 8 * G), b2 = r0 + pb0 - 64 * (63 - 8 * G);
#define CONV_RUN(TEXT) asm volatile("s_waitcnt lgkmcnt(0)\n\t" TEXT \
        : "+v"(acc[0]), "+v"(acc[1]), "+v"(acc[2]), "+v"(acc[3]), "+v"(acc[4]), "+v"(acc[5]), "+v"(acc[6]), "+v"(acc[7]) \
        : "v"(a1), "v"(b1), "v"(a2), "v"(b2) : "memory", CONV_CLOBBERS)
    if (G == 0) CONV_RUN(CONV_ASM_0); else if (G == 1) CONV_RUN(CONV_ASM_1); else if (G == 2) CONV_RUN(CONV_ASM_2); else CONV_RUN(CONV_ASM_3);
#undef CONV_RUN
}

DI void ssm_wave(const Params& p, lds_p L, unsigned lds0, __amdgpu_buffer_rsrc_t rs_ws, int s, int lane, int xcd, int ch0) {
    const int d = ch0 + s;
    __builtin_amdgcn_s_setprio(1);
    lds_p R = L + L_S + s * SCH;
    const int pb = d * 64 + lane;
    const unsigned raw0 = ((const unsigned*)p.are)[pb], raw1 = ((const unsigned*)p.aim)[pb], raw2 = ((const unsigned*)p.bre)[pb], raw3 = ((const unsigned*)p.bim)[pb],
                   raw4 = ((const unsigned*)p.cre)[pb], raw5 = ((const unsigned*)p.cim)[pb];
    const unsigned ldt2 = ((const unsigned*)p.ldt)[d >> 1], dsk2 = ((const unsigned*)p.dsk)[d >> 1];
    const unsigned epoch = gload_sc1((const unsigned*)(p.ws + WS_EPOCH));
    const int t0 = 512 * s + 8 * lane;
    SBAR();
    BARRIER();
    SBAR();
    const f32x2 are = {bflo(raw0), bfhi(raw0)}, aim = {bflo(raw1), bfhi(raw1)}, bre = {bflo(raw2), bfhi(raw2)}, bim = {bflo(raw3), bfhi(raw3)};
    const f32x2 cre = {bflo(raw4), bfhi(raw4)}, cim = {bflo(raw5), bfhi(raw5)};
    const float ldt = (d & 1) ? bfhi(ldt2) : bflo(ldt2), dsk = (d & 1) ? bfhi(dsk2) : bflo(dsk2);
    const float dt = bflo(cvtpk(expf(ldt), 0.f));
    const f32x2 ar2 = (are * dt) * LOG2E, airev = (aim * dt) * INV2PI;
    cplx2 dB; dB.re = bre * dt; dB.im = bim * dt;
    cplx2 C; C.re = cre; C.im = cim;
    const cplx2 CB = cmul(C, dB);
    const cplx2 w1 = cexp(ar2, airev, 1.f), w8 = cexp(ar2, airev, 8.f), w128 = cexp(ar2, airev, 128.f);

    lds_p st0 = R + lane * 16, st1 = R + 4224 + lane * 16;
    const int kq = lane >> 4, q4 = (lane >> 2) & 3, p4 = lane & 3;
    lds_p trb = R + (p4 >> 1) * 4224 + (16 * (kq >> 1) + 4 * (kq & 1) + q4) * 16 + (p4 & 1) * 8;
#define PUT16(V, NEG) do { \
        u32x4 a0, a1, b0, b1, c0, c1, d0, d1; \
        _Pragma("unroll") for (int j = 0; j < 4; ++j) { \
            a0[j] = cvtpk(V[2 * j].re[0], V[2 * j + 1].re[0]); a1[j] = cvtpk(V[8 + 2 * j].re[0], V[9 + 2 * j].re[0]); \
            b0[j] = cvtpk(V[2 * j].re[1], V[2 * j + 1].re[1]); b1[j] = cvtpk(V[8 + 2 * j].re[1], V[9 + 2 * j].re[1]); \
            c0[j] = cvtpk(NEG V[2 * j].im[0], NEG V[2 * j + 1].im[0]); c1[j] = cvtpk(NEG V[8 + 2 * j].im[0], NEG V[9 + 2 * j].im[0]); \
            d0[j] = cvtpk(NEG V[2 * j].im[1], NEG V[2 * j + 1].im[1]); d1[j] = cvtpk(NEG V[8 + 2 * j].im[1], NEG V[9 + 2 * j].im[1]); } \
        ldsw(st0, a0); ldsw(st1, a1); ldsw(st0 + 1024, b0); ldsw(st1 + 1024, b1); \
        ldsw(st0 + 2048, c0); ldsw(st1 + 2048, c1); ldsw(st0 + 3072, d0); ldsw(st1 + 3072, d1); } while (0)
#define GETF(ks) cat8(ldtr(trb + (ks) * 512), ldtr(trb + (ks) * 512 + 128))
    bf16x8 yf[8];
    {
        cplx2 V[16];
        V[0].re = (f32x2){1.f, 1.f}; V[0].im = (f32x2){0.f, 0.f}; V[1].re = w8.re; V[1].im = -w8.im;
        V[2] = cmul(V[1], V[1]); V[3] = cmul(V[2], V[1]); V[4] = cmul(V[2], V[2]);
        V[5] = cmul(V[4], V[1]); V[6] = cmul(V[4], V[2]); V[7] = cmul(V[4], V[3]); V[8] = cmul(V[4], V[4]);
#pragma unroll
        for (int j = 9; j < 16; ++j) V[j] = cmul(V[8], V[j - 8]);
        PUT16(V, +);
#pragma unroll
        for (int ks = 0; ks < 8; ++ks) yf[ks] = GETF(ks);
    }
    const cplx2 w2 = cmul(w1, w1), w3 = cmul(w2, w1), w4 = cmul(w2, w2), w5 = cmul(w4, w1), w6 = cmul(w4, w2), w7 = cmul(w4, w3);
    f32x4 D0 = {}, D1 = {}, D2 = {};
    cplx2 G = cmul(CB, w128);
    {
        cplx2 V[16];
        V[0] = CB; V[1] = cmul(CB, w1); V[2] = cmul(CB, w2); V[3] = cmul(CB, w3); V[4] = cmul(CB, w4); V[5] = cmul(CB, w5); V[6] = cmul(CB, w6); V[7] = cmul(CB, w7);
        V[8] = G; V[9] = cmul(G, w1); V[10] = cmul(G, w2); V[11] = cmul(G, w3); V[12] = cmul(G, w4); V[13] = cmul(G, w5); V[14] = cmul(G, w6); V[15] = cmul(G, w7);
        PUT16(V, +);
        bf16x8 xf[8];
#pragma unroll
        for (int ks = 0; ks < 8; ++ks) xf[ks] = GETF(ks);
        SBAR();
#pragma unroll
        for (int ks = 0; ks < 8; ++ks) D0 = MF16(xf[ks], yf[ks], D0);
    }
    PIN(D0);
    {
        cplx2 V[16];
        G = cmul(G, w128); V[0] = G; V[1] = cmul(G, w2); V[2] = cmul(G, w4); V[3] = cmul(G, w6);
        G = cmul(G, w128); V[4] = G; V[5] = cmul(G, w2); V[6] = cmul(G, w4); V[7] = cmul(G, w6);
#pragma unroll
        for (int i = 0; i < 4; ++i) { G = cmul(G, w128); V[8 + 2 * i] = G; V[9 + 2 * i] = cmul(G, w4); }
        PUT16(V, +);
        bf16x8 xf[8];
#pragma unroll
        for (int ks = 0; ks < 8; ++ks) xf[ks] = GETF(ks);
        SBAR();
#pragma unroll
        for (int ks = 0; ks < 8; ++ks) D1 = MF16(xf[ks], yf[ks], D1);
    }
    {
        cplx2 V[16];
#pragma unroll
        for (int i = 0; i < 9; ++i) { G = cmul(G, w128); V[i] = G; }
#pragma unroll
        for (int i = 9; i < 16; ++i) V[i] = G;
        PUT16(V, +);
        bf16x8 xf[8];
#pragma unroll
        for (int ks = 0; ks < 8; ++ks) xf[ks] = GETF(ks);
        SBAR();
#pragma unroll
        for (int ks = 0; ks < 8; ++ks) D2 = MF16(yf[ks], xf[ks], D2);
    }
#undef PUT16
#undef GETF
    PIN(D2);
    lds_p tab = R + 8448;
    {
        const int j = lane & 15, rq = lane >> 4;
        if (lane == 0) D0[0] += dsk;
        ldsw(tab + 2 * (8 * j + 4 * (rq & 1) + 128 * (rq >> 1)), (u32x2){cvtpk(D0[0], D0[1]), cvtpk(D0[2], D0[3])});
        const int e0 = rq < 2 ? 256 + 64 * rq + 4 * j : 384 + 64 * (rq - 2) + 2 * j, e1 = rq < 2 ? e0 + 2 : e0 + 32;
        ldsw(tab + 2 * e0, cvtpk(D1[0], D1[1])); ldsw(tab + 2 * e1, cvtpk(D1[2], D1[3]));
        if (j < 8 || lane == 8) ldsw(tab + 2 * (512 + 16 * j + 4 * rq), (u32x2){cvtpk(D2[0], D2[1]), cvtpk(D2[2], D2[3])});
    }
#define PK(hi_src, hi_half, lo_src, lo_half) __builtin_amdgcn_perm(hi_src, lo_src, ((hi_half) ? 0x07060000u : 0x05040000u) | ((lo_half) ? 0x0302u : 0x0100u))
    {
#pragma unroll
        for (int i = 0; i < 2; ++i) {
            const int g = lane + 64 * i, m = 255 - g;
            const unsigned lo = ldsr<unsigned short>(tab + 2 * (384 + m)), hi = ldsr<unsigned short>(tab + 2 * (385 + m));
            const unsigned mid = (m & 1) ? hi : lo, ll = PK(lo, 0, lo, 0);
            ldsw(R + 16 * g, (u32x4){PK(hi, 0, hi, 0), PK(mid, 0, hi, 0), ll, ll});
        }
        {
            const int g = 128 + lane, m = 255 - g;
            const unsigned ab = ldsr<unsigned>(tab + 2 * (256 + 2 * m)), c = ldsr<unsigned short>(tab + 2 * (258 + 2 * m));
            ldsw(R + 16 * g, (u32x4){PK(c, 0, c, 0), PK(ab, 1, ab, 1), PK(ab, 0, ab, 1), PK(ab, 0, ab, 0)});
        }
        {
            const int g = 192 + lane, m = 255 - g;
            u32x4 v;
            if (lane < 32) {
                const u32x2 q = ldsr<u32x2>(tab + 2 * (128 + 4 * m)); const unsigned e = ldsr<unsigned short>(tab + 2 * (132 + 4 * m));
                v = (u32x4){PK(q[1], 1, e, 0), PK(q[1], 0, q[1], 0), PK(q[0], 1, q[1], 0), PK(q[0], 0, q[0], 0)};
            } else {
                const u32x4 q = ldsr<u32x4>(tab + 16 * m);
                v = (u32x4){PK(q[3], 0, q[3], 1), PK(q[2], 0, q[2], 1), PK(q[1], 0, q[1], 1), PK(q[0], 0, q[0], 1)};
            }
            ldsw(R + 16 * g, v);
        }
        if (lane < 4) ldsw(R + 16 * (256 + lane), (u32x4){0u, 0u, 0u, 0u});
    }
#undef PK
    if (lane < 32) ldsw(R + SCH_U + 16 * lane, (u32x4){0u, 0u, 0u, 0u});
    if (s == 0) {
        const unsigned tag = (unsigned)__builtin_amdgcn_readfirstlane(epoch) + 1u;
        const unsigned* f = (const unsigned*)(p.ws + WS_FLAG3 + xcd * 1024) + (lane & 7) * 32;
        for (unsigned spins = 0; spins < SPIN_GLB; ++spins) {
            if (__all(gload_sc1(f) == tag)) break;
            __builtin_amdgcn_s_sleep(1);
        }
        __builtin_amdgcn_fence(__ATOMIC_ACQUIRE, "wavefront");
        arrive(L, C_U, lane);
    }
    waitc(L, C_U, 1);
#pragma unroll
    for (int i = 0; i < 4; ++i) dma16_sc1(rs_ws, WS_UT + d * UT_PITCH + 1024 * i + 16 * lane, lds0 + L_S + s * SCH + SCH_U + 512 + 1024 * i);
#pragma unroll
    for (int i = 0; i < 5; ++i) {
        const int g = lane + 64 * i;
        if (i < 4 || lane < 4) {
            const u32x4 a = ldsr<u32x4>(R + 16 * g); const u32x2 b = ldsr<u32x2>(R + 16 * g + 16);
            const unsigned a0 = __builtin_amdgcn_alignbit(a[1], a[0], 16), a1 = __builtin_amdgcn_alignbit(a[2], a[1], 16), a2 = __builtin_amdgcn_alignbit(a[3], a[2], 16),
                           a3 = __builtin_amdgcn_alignbit(b[0], a[3], 16), a4 = __builtin_amdgcn_alignbit(b[1], b[0], 16);
            ldsw(R + 4160 + 16 * g, (u32x4){a0, a1, a2, a3});
            ldsw(R + 8320 + 16 * g, (u32x4){a[1], a[2], a[3], b[0]});
            ldsw(R + 12480 + 16 * g, (u32x4){a1, a2, a3, a4});
        }
    }
    DRAIN();
    f32x4 acc[8] = {};
    {
        const int b = lane & 15, kq = lane >> 4, c = (3 - b) & 3;
        const int pa0 = c * 4160 + 2 * (2031 - b + 8 * kq - c), pb0 = SCH_U + 480 + 32 * b + 16 * kq;
        __builtin_amdgcn_s_setprio(0);
        conv_group<0>(L, R, acc, pa0, pb0); conv_group<1>(L, R, acc, pa0, pb0); conv_group<2>(L, R, acc, pa0, pb0); conv_group<3>(L, R, acc, pa0, pb0);
        __builtin_amdgcn_s_setprio(3);
    }
    PIN(acc[0]); PIN(acc[7]);
#pragma unroll
    for (int nu = 0; nu < 8; ++nu)
        ldsw(R + 512 * nu + 32 * (lane & 15) + 8 * (lane >> 4), (u32x2){cvtpk(acc[nu][0], acc[nu][1]), cvtpk(acc[nu][2], acc[nu][3])});
    arrive(L, C_S2, lane); waitc(L, C_S2, 4);
    {
        u32x4 res[4];
#pragma unroll
        for (int c = 0; c < 4; ++c) res[c] = ldsr<u32x4>(L + L_S + c * SCH + 2 * t0);
        const __amdgpu_buffer_rsrc_t rs_out = __builtin_amdgcn_make_buffer_rsrc((void*)p.out, 0, SEQ * 2 * DM * 2, RSRC_W3);
#pragma unroll
        for (int e = 0; e < 4; ++e) {
            const int oo = ((t0 + 2 * e) * (2 * DM) + DM + ch0) * 2;
            __builtin_amdgcn_raw_buffer_store_b64((u32x2){__builtin_amdgcn_perm(res[1][e], res[0][e], 0x05040100u), __builtin_amdgcn_perm(res[3][e], res[2][e], 0x05040100u)}, rs_out, oo, 0, 0);
            __builtin_amdgcn_raw_buffer_store_b64((u32x2){__builtin_amdgcn_perm(res[1][e], res[0][e], 0x07060302u), __builtin_amdgcn_perm(res[3][e], res[2][e], 0x07060302u)}, rs_out, oo + 4 * DM, 0, 0);
        }
    }

}

__global__ __launch_bounds__(768) void fused_kernel(Params p) {
    extern __shared__ __attribute__((aligned(16))) char smem[];
    lds_p L = (lds_p)smem;
    const unsigned lds0 = (unsigned)(uintptr_t)smem;
    const int tid = threadIdx.x, lane = tid & 63, w = __builtin_amdgcn_readfirstlane(tid >> 6);
    const int bid = blockIdx.x, xcd = bid & 7, idx = ((bid >> 3) + 10) & 31, tb = 2 * xcd + (idx & 1), pi = idx >> 1;
    if (tid < 64) ((LAS unsigned*)(L + L_C))[tid] = 0u;

    const __amdgpu_buffer_rsrc_t rs_ws = __builtin_amdgcn_make_buffer_rsrc((void*)p.ws, 0, WS_BYTES, RSRC_W3);

    if (w >= 8) {
        if (idx >= 14) ssm_wave(p, L, lds0, rs_ws, w - 8, lane, xcd, 72 * xcd + 4 * (idx - 14));
        else if (idx < 8) ut_wave(p, L, rs_ws, w - 8, lane, xcd, idx);
        else if (idx >= 10 && idx < 14) kv_wave(p, L, lds0, rs_ws, w - 8, lane, xcd, 4 * (idx - 10) + w - 8);
        else BARRIER();

        return;
    }

    __builtin_amdgcn_s_setprio(2);
    const unsigned epoch = gload_sc1((const unsigned*)(p.ws + WS_EPOCH));
    SBAR();
    BARRIER();
    const int c16 = lane & 15, kq = lane >> 4, r32 = lane & 31, h = lane >> 5;
    const int q4 = (lane >> 2) & 3, p4 = lane & 3;
    const int kh = w >> 2, wq4 = w & 3;

    unsigned tag;
    bf16x8 qmA, qmB; unsigned qeA, qeB;
    {
        const int T0 = 128 * tb + 16 * w;
        const bool has_ssm = idx >= 14;
        if (idx >= 10 && idx < 14) waitc(L, C_D, 4);
        {
            const __amdgpu_buffer_rsrc_t rs_wq = __builtin_amdgcn_make_buffer_rsrc((void*)p.wq, 0, DM * DM * 2, RSRC_W3);
            int kl = (lane * 43) >> 8, g = lane - 6 * kl;
            const unsigned dst = lds0 + L_W + w * 6912;
#pragma unroll
            for (int i = 0; i < 7; ++i) {
                const int vo = swap23(72 * w + kl) * 1152 + ((72 * pi) & ~15) + 16 * g;
                if (g < 5 && (i < 6 || lane < 48)) dma16(rs_wq, vo, dst + 1024 * i);
                g += 4; kl += 10; if (g >= 6) { g -= 6; kl += 1; }
            }
        }
        SBAR();
        arrive(L, C_X, lane); waitc<3>(L, C_X, 8);
        constexpr int XPF = 4;
        u32x4 xa[9], xb[9];
        const unsigned short* xp = p.x + (size_t)(T0 + (lane >> 3)) * DM + 8 * (lane & 7);
#pragma unroll
        for (int s = 0; s < XPF; ++s) { xa[s] = *(const u32x4*)(xp + 64 * s); xb[s] = *(const u32x4*)(xp + 64 * s + 8 * DM); }
        SBAR();
        asm volatile("s_waitcnt vmcnt(8)" ::: "memory");
        SBAR();
        arrive(L, C_W, lane);
        f32x4 D0 = {}, D1 = {}, D2 = {};
        unsigned fl0 = 0;
        const unsigned* flp = (const unsigned*)(p.ws + WS_FLAG1 + xcd * 4096) + (lane & 31) * 32;
        {
            lds_p wb = L + L_W + (16 * (kq >> 1) + 4 * (kq & 1) + q4) * 96 + 8 * p4 + 8 * (pi & 1);
            lds_p xs = L + L_Q + w * 2048;
            lds_p xw = xs + (lane >> 3) * 128 + 16 * ((lane & 7) ^ (lane >> 4));
            lds_p xw2 = xs + 1024 + (lane >> 3) * 128 + 16 * ((lane & 7) ^ (4 + (lane >> 4)));
            lds_p xr0 = xs + c16 * 128 + 16 * (kq ^ ((c16 >> 1) & 7)), xr1 = xs + c16 * 128 + 16 * ((4 + kq) ^ ((c16 >> 1) & 7));
            lds_p wb2 = wb - 8 * p4 + 64;
            bf16x8 f0, f1, g0, g1;
            ldsw(xw, xa[0]); ldsw(xw2, xb[0]); f0 = ldsr<bf16x8>(xr0); f1 = ldsr<bf16x8>(xr1);
            waitc<3>(L, C_W, 8);
            if (w >= 4) __builtin_amdgcn_s_setprio(1); else __builtin_amdgcn_s_setprio(0);
            bf16x8 wf[2][3];
#define QRD(n, ks) do { wf[n][0] = cat8(ldtr(wb + (ks) * 3072), ldtr(wb + (ks) * 3072 + 768)); wf[n][1] = cat8(ldtr(wb + (ks) * 3072 + 32), ldtr(wb + (ks) * 3072 + 768 + 32)); \
                        wf[n][2] = cat8(ldtr(wb2 + (ks) * 3072), ldtr(wb2 + (ks) * 3072 + 768)); } while (0)
#define QMM(n, XF) do { D0 = MF16(wf[n][0], XF, D0); D1 = MF16(wf[n][1], XF, D1); D2 = MF16(wf[n][2], XF, D2); } while (0)
            QRD(0, 0);
#pragma unroll
            for (int s = 0; s < 9; ++s) {
                if (s + XPF < 9) { xa[s + XPF] = *(const u32x4*)(xp + 64 * (s + XPF)); xb[s + XPF] = *(const u32x4*)(xp + 64 * (s + XPF) + 8 * DM); }
                if (s == 8 && w == 7) fl0 = gload_sc1(flp);
                SBAR();
                QRD(1, 2 * s + 1);
                SBAR();
                if (s & 1) { QMM(0, g0); } else { QMM(0, f0); }
                SBAR();
                if (s < 8) {
                    ldsw(xw, xa[s + 1]); ldsw(xw2, xb[s + 1]);
                    if (s & 1) { f0 = ldsr<bf16x8>(xr0); } else { g0 = ldsr<bf16x8>(xr0); }
                    QRD(0, 2 * s + 2);
                }
                SBAR();
                if (s & 1) { QMM(1, g1); } else { QMM(1, f1); }
                SBAR();
                if (s < 8) { if (s & 1) { f1 = ldsr<bf16x8>(xr1); } else { g1 = ldsr<bf16x8>(xr1); } }
                SBAR();
            }
#undef QRD
#undef QMM
        }
        __builtin_amdgcn_s_setprio(2);
        tag = (unsigned)__builtin_amdgcn_readfirstlane(epoch) + 1u;
        {
            lds_p qs = L + L_Q + w * 2048;
            ldsw(qs + c16 * 48 + 8 * kq, (u32x2){cvtpk(D0[0] * QSCALE, D0[1] * QSCALE), cvtpk(D0[2] * QSCALE, D0[3] * QSCALE)});
            const int a0 = kq == 0 ? c16 * 48 + 32 : (16 + c16) * 48 + 2 * (4 * kq - 2);
            const int a1 = (16 + c16) * 48 + 8 * kq;
            ldsw(qs + a0, cvtpk(D1[0] * QSCALE, D1[1] * QSCALE));
            ldsw(qs + a1, cvtpk(D1[2] * QSCALE, D1[3] * QSCALE));
            if (kq == 0) {
                ldsw(qs + (16 + c16) * 48 + 28, cvtpk(D2[0] * QSCALE, D2[1] * QSCALE));
                ldsw(qs + (16 + c16) * 48 + 32, cvtpk(D2[2] * QSCALE, D2[3] * QSCALE));
            }
        }
        arrive(L, C_Q, lane);
        if (w == 7) {
            if (!__all(fl0 == tag)) {
                for (unsigned spins = 0; spins < SPIN_GLB; ++spins) {
                    if (__all(gload_sc1(flp) == tag)) break;
                    __builtin_amdgcn_s_sleep(1);
                }
            }
            __builtin_amdgcn_fence(__ATOMIC_ACQUIRE, "wavefront");
            arrive(L, C_F1, lane);
        }
        waitc<3>(L, C_Q, 8);
        {
            lds_p qa = L + L_Q + w * 2048 + r32 * 48, qb = L + L_Q + (w ^ 4) * 2048 + r32 * 48;
            qmA = ldsr<bf16x8>(qa + 16 * h); qmB = ldsr<bf16x8>(qb + 16 * h);
            qeA = ldsr<unsigned>(qa + 32); qeB = ldsr<unsigned>(qb + 32);
        }
        waitc<3>(L, C_F1, 1);
    }

    {
        const bool has_ssm2 = idx >= 14;
        lds_p ring = L + L_W + kh * (8 * KVT);
        const int dmo = WS_KV + (32 * kh + wq4) * KVT + lane * 16;
        const unsigned dmd = lds0 + L_W + kh * (8 * KVT) + wq4 * KVT;
#define KV_DMA_A(cc) do { dma16_sc1(rs_ws, dmo + (cc) * (4 * KVT), dmd + ((cc) & 1) * (4 * KVT)); dma16_sc1(rs_ws, dmo + (cc) * (4 * KVT) + 1024, dmd + ((cc) & 1) * (4 * KVT) + 1024); } while (0)
#define KV_DMA_B(cc) do { if (lane < 32) dma16_sc1(rs_ws, dmo + (cc) * (4 * KVT) + 2048, dmd + ((cc) & 1) * (4 * KVT) + 2048); } while (0)
        const __amdgpu_buffer_rsrc_t rs_wo = __builtin_amdgcn_make_buffer_rsrc((void*)p.wo, 0, DM * DM * 2, RSRC_W3);
        int woi = 11, wov, wov8;
        { const int s0 = 64 * w + lane, k0 = (s0 * 7282) >> 16, s8 = 512 + lane, k8 = (s8 * 7282) >> 16;
          wov = k0 * 1152 + 144 * (pi >> 1) + 16 * (s0 - 9 * k0); wov8 = k8 * 1152 + 144 * (pi >> 1) + 16 * (s8 - 9 * k8); }
        lds_p kb = ring + lane * 16, eb = ring + 2304 + r32 * 8, vb = ring + 1024 + (h * 20 + (r32 < 19 ? r32 : 19)) * 16;
        const f32x16 Z = {};
        unsigned mA = 0, mB = 0;
        f32x16 OA, OB;
        for (int pass = 0; pass < 2; ++pass) {
            KV_DMA_A(0); KV_DMA_B(0); KV_DMA_A(1); KV_DMA_B(1);
            asm volatile("s_waitcnt vmcnt(3)" ::: "memory");
            arrive(L, C_RW, lane); waitc<3>(L, C_RW, 8 * (pass + 1));
            if (pass == 0) {
                const bf16x8 km = ldsr<bf16x8>(L + L_W + lane * 16); const bf16x4 ke = ldsr<bf16x4>(L + L_W + 2304 + r32 * 8);
                const u32x2 ea = {h ? 0u : qeA, 0u}, ebq = {h ? 0u : qeB, 0u};
                f32x16 sa = MF32(km, qmA, Z), sb = MF32(km, qmB, Z);
                sa = MF32E(ke, __builtin_bit_cast(bf16x4, ea), sa); sb = MF32E(ke, __builtin_bit_cast(bf16x4, ebq), sb);
                float ma = sa[0], mb = sb[0];
#pragma unroll
                for (int i = 1; i < 16; ++i) { ma = fmaxf(ma, sa[i]); mb = fmaxf(mb, sb[i]); }
                auto ra = __builtin_amdgcn_permlane32_swap(__float_as_uint(ma), __float_as_uint(ma), false, false);
                auto rb = __builtin_amdgcn_permlane32_swap(__float_as_uint(mb), __float_as_uint(mb), false, false);
                ma = fmaxf(__uint_as_float(ra[0]), __uint_as_float(ra[1])); mb = fmaxf(__uint_as_float(rb[0]), __uint_as_float(rb[1]));
                const float ah = bflo(cvtpk(ma, 0.f)), bh = bflo(cvtpk(mb, 0.f));
                mA = cvtpk(-ah, ah - ma); mB = cvtpk(-bh, bh - mb);
            }
            const u32x2 ea = {h ? 0u : qeA, h ? 0u : mA}, ebq = {h ? 0u : qeB, h ? 0u : mB};
            const bf16x4 qxA = __builtin_bit_cast(bf16x4, ea), qxB = __builtin_bit_cast(bf16x4, ebq);
            f32x16 SA, SB = Z; u32x4 pa0, pa1, pb0 = {}, pb1 = {}; bf16x8 km, vx0, vx1, vy0, vy1; bf16x4 ke;
            OA = Z; OB = Z;
            km = ldsr<bf16x8>(kb); ke = ldsr<bf16x4>(eb);
            SA = MF32(km, qmA, Z); SA = MF32E(ke, qxA, SA);
            vx0 = ldsr<bf16x8>(vb); vx1 = ldsr<bf16x8>(vb + 640);
#define PH(SC, SN, QM, QX, ON, pp0, pp1, pn0, pn1, V0, V1, LD1, LD2, LD4) do { \
            ON = MF32(V0, BC8(pp0), ON); SBAR(); \
            LD1; \
            SC[0] = EXP2(SC[0]); SC[1] = EXP2(SC[1]); SC[2] = EXP2(SC[2]); SC[3] = EXP2(SC[3]); SBAR(); \
            SN = MF32(km, QM, Z); SBAR(); \
            LD2; \
            SC[4] = EXP2(SC[4]); SC[5] = EXP2(SC[5]); pn0[0] = cvtpk(SC[0], SC[1]); SC[6] = EXP2(SC[6]); SC[7] = EXP2(SC[7]); pn0[1] = cvtpk(SC[2], SC[3]); SBAR(); \
            ON = MF32(V1, BC8(pp1), ON); SBAR(); \
            SC[8] = EXP2(SC[8]); SC[9] = EXP2(SC[9]); pn0[2] = cvtpk(SC[4], SC[5]); SC[10] = EXP2(SC[10]); SC[11] = EXP2(SC[11]); pn0[3] = cvtpk(SC[6], SC[7]); PIN(pn0); SBAR(); \
            SN = MF32E(ke, QX, SN); SBAR(); \
            LD4; \
            SC[12] = EXP2(SC[12]); SC[13] = EXP2(SC[13]); pn1[0] = cvtpk(SC[8], SC[9]); SC[14] = EXP2(SC[14]); SC[15] = EXP2(SC[15]); pn1[1] = cvtpk(SC[10], SC[11]); \
            pn1[2] = cvtpk(SC[12], SC[13]); pn1[3] = cvtpk(SC[14], SC[15]); PIN(pn1); SBAR(); \
        } while (0)
#define WO1() if (woi < 11) { \
                if (woi < 9) dma16s(rs_wo, wov, 73728 * woi, lds0 + L_S + 9216 * woi + 1024 * w); \
                else if (woi == 9) dma16s(rs_wo, wov8, 73728 * w, lds0 + L_S + 9216 * w + 8192); \
                else if (w == 0) dma16s(rs_wo, wov8, 73728 * 8, lds0 + L_S + 9216 * 8 + 8192); \
                woi += 1; }
#define VRD(t, N0, N1) (N0 = ldsr<bf16x8>(vb + (t) * KVT), N1 = ldsr<bf16x8>(vb + (t) * KVT + 640))
#define KMRD(t) (km = ldsr<bf16x8>(kb + (((t) + 1) & 7) * KVT))
#define KERD(t) (ke = ldsr<bf16x4>(eb + (((t) + 1) & 7) * KVT))
#define PHA(P0, P1, LD1, LD2, LD4) PH(SA, SB, qmB, qxB, OB, pb0, pb1, pa0, pa1, P0, P1, LD1, LD2, LD4);
#define PHB(N0, N1, LD1, LD2, LD4) PH(SB, SA, qmA, qxA, OA, pa0, pa1, pb0, pb1, N0, N1, LD1, LD2, LD4);
#define TA(t) PHA(vx0, vx1, VRD(t, vy0, vy1), KMRD(t), KERD(t))
#define TB() PHB(vy0, vy1, WO1(), (void)0, (void)0)
#define UA(t) PHA(vy0, vy1, VRD(t, vx0, vx1), KMRD(t), KERD(t))
#define UB() PHB(vx0, vx1, WO1(), (void)0, (void)0)
#define PRIO(x) __builtin_amdgcn_s_setprio(x);
#define LAST(t, b) \
            PHA(vy0, vy1, VRD(t, vx0, vx1), (void)0, (void)0) \
            asm volatile("s_waitcnt vmcnt(0)" ::: "memory"); \
            if ((b) < 7) { if (has_ssm2 && (b) < 2 && pass == 0) { arrive(L, C_B, lane); waitc(L, C_B, 8 * ((b) + 1)); } else __builtin_amdgcn_s_barrier(); } \
            CBAR(); PRIO(3) KMRD(t); KERD(t); SBAR(); \
            PHB(vx0, vx1, (void)0, if ((b) + 2 < 8) KV_DMA_A((b) + 2), if ((b) + 2 < 8) KV_DMA_B((b) + 2))
            PRIO(3)
            for (int it = 0; it < 4; ++it) {
                TA(0) PRIO(2) TB() UA(1) PRIO(1) UB() TA(2) PRIO(0) TB()
                LAST(3, 2 * it)
                if (2 * it == 2 + (2 & 1) && pass == 0 && !(2 & 1)) woi = 0;
                TA(4) PRIO(2) TB() UA(5) PRIO(1) UB() TA(6) PRIO(0) TB()
                LAST(7, 2 * it + 1)
                if (2 * it + 1 == 2 && pass == 0) woi = 0;
            }
            OB = MF32(vx0, BC8(pb0), OB); OB = MF32(vx1, BC8(pb1), OB);
#undef TA
#undef TB
#undef UA
#undef UB
#undef LAST
#undef PHA
#undef PHB
#undef VRD
#undef WO1
#undef KMRD
#undef KERD
#undef PH
            PRIO(0)
            lds_p xo = (w < 4 ? L + L_X1 : L + L_Q) + wq4 * 3072, xi = (w < 4 ? L + L_Q : L + L_X1) + wq4 * 3072;
#pragma unroll
            for (int g = 0; g < 3; ++g) ldsw(xo + g * 1024 + lane * 16, (f32x4){OB[4 * g], OB[4 * g + 1], OB[4 * g + 2], OB[4 * g + 3]});
            BARRIER();
            float o[12];
#pragma unroll
            for (int g = 0; g < 3; ++g) {
                const f32x4 t = ldsr<f32x4>(xi + g * 1024 + lane * 16);
#pragma unroll
                for (int i = 0; i < 4; ++i) o[4 * g + i] = OA[4 * g + i] + t[i];
            }
            const float lsum = __uint_as_float(__builtin_amdgcn_permlane32_swap(__float_as_uint(o[10]), __float_as_uint(o[10]), false, false)[0]);
            const float rl = __builtin_amdgcn_rcpf(lsum);
            float chk = lsum;
#pragma unroll
            for (int i = 0; i < 10; ++i) chk = __builtin_fmaf(o[i], 0.f, chk);
            const bool bad = !(chk < 1.2676506e30f);
            {
                lds_p os = xi;
                lds_p ob = os + (r32 & 15) * 72 + (r32 >> 4) * 36 + 8 * h;
                ldsw(ob, cvtpk(o[0] * rl, o[1] * rl)); ldsw(ob + 4, cvtpk(o[2] * rl, o[3] * rl));
                ldsw(ob + 16, cvtpk(o[4] * rl, o[5] * rl)); ldsw(ob + 20, cvtpk(o[6] * rl, o[7] * rl));
                if (h == 0) ldsw(ob + 32, cvtpk(o[8] * rl, o[9] * rl));
#pragma unroll
                for (int i = 0; i < 3; ++i) {
                    const int e = lane + 64 * i;
                    if (e < 144) {
                        const int tk = e & 15, pc = e >> 4, k0 = 36 * pi + 4 * pc;
                        const u32x2 v = ldsr<u32x2>(os + tk * 72 + pc * 8);
                        __builtin_amdgcn_raw_buffer_store_b64(v, rs_ws, WS_O + (8 * tb + w) * 18432 + (k0 >> 5) * 1024 + (((k0 >> 3) & 2) + ((k0 >> 2) & 1)) * 256 + tk * 16 + (k0 & 8), 0, AUX_SC1);
                    }
                }
            }
            if (pass == 0 && __any(bad) && lane == 0) ((volatile LAS unsigned*)(L + L_C))[C_BAD] = 1u;
            DRAIN();
            BARRIER();
            const bool redo = pass == 0 && __builtin_amdgcn_readfirstlane(((volatile LAS unsigned*)(L + L_C))[C_BAD]) != 0;
            if (!redo) break;
            {
                const u32x2 ea0 = {h ? 0u : qeA, 0u}, eb0 = {h ? 0u : qeB, 0u};
                float ma = -INFINITY, mb = -INFINITY;
                for (int t = 0; t < 64; ++t) {
                    const u32x4 kmv = __builtin_amdgcn_raw_buffer_load_b128(rs_ws, WS_KV + t * KVT + lane * 16, 0, AUX_SC1);
                    const u32x2 kev = __builtin_amdgcn_raw_buffer_load_b64(rs_ws, WS_KV + t * KVT + 2304 + r32 * 8, 0, AUX_SC1);
                    f32x16 sa = MF32(BC8(kmv), qmA, Z), sb = MF32(BC8(kmv), qmB, Z);
                    sa = MF32E(__builtin_bit_cast(bf16x4, kev), __builtin_bit_cast(bf16x4, ea0), sa);
                    sb = MF32E(__builtin_bit_cast(bf16x4, kev), __builtin_bit_cast(bf16x4, eb0), sb);
#pragma unroll
                    for (int i = 0; i < 16; ++i) { ma = fmaxf(ma, sa[i]); mb = fmaxf(mb, sb[i]); }
                }
                ma = fmaxf(ma, __shfl_xor(ma, 32)); mb = fmaxf(mb, __shfl_xor(mb, 32));
                const float ah = bflo(cvtpk(ma, 0.f)), bh = bflo(cvtpk(mb, 0.f));
                mA = cvtpk(-ah, ah - ma); mB = cvtpk(-bh, bh - mb);
            }
        }
#undef KV_DMA_A
#undef KV_DMA_B
#undef PRIO
        if (w == 0 && lane == 0) gstore_sc1((unsigned*)(p.ws + WS_FLAG2 + (tb * 16 + pi) * 128), tag);
    }

    {
        constexpr int PF3 = 5;
        lds_p wb = L + L_S + (16 * (kq >> 1) + 4 * (kq & 1) + q4) * 144 + 8 * p4 + kh * (9 * 32 * 144);
        bf16x8 wf[PF3][5];
#define WORD3(i, T) cat8(ldtr(wb + (i) * 4608 + 32 * (T)), ldtr(wb + (i) * 4608 + 1152 + 32 * (T)))
#pragma unroll
        for (int i = 0; i < PF3; ++i)
#pragma unroll
            for (int T = 0; T < 5; ++T) wf[i][T] = WORD3(i, T);
        SBAR();
        if (w == 7) {
            const unsigned* f = (const unsigned*)(p.ws + WS_FLAG2 + tb * 2048) + (lane & 15) * 32;
            for (unsigned spins = 0; spins < SPIN_GLB; ++spins) {
                if (__all(gload_sc1(f) == tag)) break;
                __builtin_amdgcn_s_sleep(1);
            }
            __builtin_amdgcn_fence(__ATOMIC_ACQUIRE, "wavefront");
        }
        BARRIER();
        const int Tt = 128 * tb + 64 * (pi & 1) + 16 * wq4, F0 = 72 * (pi >> 1);
        u32x4 of[9];
#pragma unroll
        for (int i = 0; i < 9; ++i) of[i] = __builtin_amdgcn_raw_buffer_load_b128(rs_ws, WS_O + (Tt >> 4) * 18432 + (9 * kh + i) * 1024 + lane * 16, 0, AUX_SC1);
        SBAR();
        f32x4 D[5] = {};
#pragma unroll
        for (int i = 0; i < 9; ++i) {
#pragma unroll
            for (int T = 0; T < 5; ++T) D[T] = MF16(wf[i % PF3][T], BC8(of[i]), D[T]);
            SBAR();
            if (i + PF3 < 9) {
#pragma unroll
                for (int T = 0; T < 5; ++T) wf[i % PF3][T] = WORD3(i + PF3, T);
            }
            SBAR();
        }
#undef WORD3
        lds_p xo = L + L_W + w * 3072, xi = L + L_W + (w ^ 4) * 3072;
        if (kh == 0) { ldsw(xo + lane * 16, D[3]); ldsw(xo + 1024 + lane * 16, D[4]); }
        else { ldsw(xo + lane * 16, D[0]); ldsw(xo + 1024 + lane * 16, D[1]); ldsw(xo + 2048 + lane * 16, D[2]); }
        BARRIER();
        const __amdgpu_buffer_rsrc_t rs_out = __builtin_amdgcn_make_buffer_rsrc((void*)p.out, 0, SEQ * 2 * DM * 2, RSRC_W3);
        const int oo = ((Tt + c16) * (2 * DM) + F0 + 4 * kq) * 2;
        if (kh == 0) {
#pragma unroll
            for (int T = 0; T < 3; ++T) {
                const f32x4 v = D[T] + ldsr<f32x4>(xi + T * 1024 + lane * 16);
                __builtin_amdgcn_raw_buffer_store_b64((u32x2){cvtpk(v[0], v[1]), cvtpk(v[2], v[3])}, rs_out, oo + 32 * T, 0, 0);
            }
        } else {
#pragma unroll
            for (int T = 3; T < 5; ++T) {
                const f32x4 v = D[T] + ldsr<f32x4>(xi + (T - 3) * 1024 + lane * 16);
                if (T == 3 || kq < 2) __builtin_amdgcn_raw_buffer_store_b64((u32x2){cvtpk(v[0], v[1]), cvtpk(v[2], v[3])}, rs_out, oo + 32 * T, 0, 0);
            }
        }

        if (bid == 0 && w == 7) {
            const unsigned* f = (const unsigned*)(p.ws + WS_FLAG2);
            for (unsigned spins = 0; spins < SPIN_GLB; ++spins) {
                bool ok = true;
#pragma unroll
                for (int i = 0; i < 4; ++i) { const int e = lane + 64 * i; ok &= gload_sc1(f + e * 32) == tag; }
                if (__all(ok)) break;
                __builtin_amdgcn_s_sleep(2);
            }
            if (lane == 0) gstore_sc1((unsigned*)(p.ws + WS_EPOCH), tag);
        }
    }
}

}

extern "C" void kernel_launch(void* const* d_in, const int* in_sizes, int n_in, void* d_out, int out_size, void* d_ws, size_t ws_size,
                              hipStream_t stream) {
    Params p;
    p.x = (const unsigned short*)d_in[0]; p.wq = (const unsigned short*)d_in[1]; p.wk = (const unsigned short*)d_in[2];
    p.wv = (const unsigned short*)d_in[3]; p.wo = (const unsigned short*)d_in[4]; p.u = (const unsigned short*)d_in[5];
    p.are = (const unsigned short*)d_in[6]; p.aim = (const unsigned short*)d_in[7]; p.bre = (const unsigned short*)d_in[8];
    p.bim = (const unsigned short*)d_in[9]; p.cre = (const unsigned short*)d_in[10]; p.cim = (const unsigned short*)d_in[11];
    p.ldt = (const unsigned short*)d_in[12]; p.dsk = (const unsigned short*)d_in[13];
    p.out = (unsigned short*)d_out; p.ws = (char*)d_ws;
    (void)hipFuncSetAttribute((const void*)fused_kernel, hipFuncAttributeMaxDynamicSharedMemorySize, L_END);
    fused_kernel<<<256, 768, L_END, stream>>>(p);
}
```
